```python
import torch

def _softmax(x, axis=-1):
    return torch.softmax(x, dim=axis)

def _gelu(x):
    return torch.nn.functional.gelu(x, approximate='tanh')

def _silu(x):
    return torch.nn.functional.silu(x)

def _sigmoid(x):
    return torch.sigmoid(x)

def _layernorm(x, axis=-1, eps=1e-05):
    m = x.mean(dim=axis, keepdim=True)
    v = x.var(dim=axis, keepdim=True, unbiased=False)
    return (x - m) / torch.sqrt(v + eps)

def _scatter_add(tgt, idx, src, axis=0):
    return tgt.clone().index_add_(axis, idx, src)

def _segment_sum(src, seg, n):
    out = torch.zeros((n,) + src.shape[1:], dtype=src.dtype)
    return out.index_add_(0, seg, src)

def _pad(x, pw):
    flat = tuple((v for pair in reversed(pw) for v in pair))
    return torch.nn.functional.pad(x, flat)
kernel_name = 'adaLN_cond__gqa_block_cached__moe_arch_mod__prompt'

def setup_inputs():

    _w = lambda a: (lambda t: t.to(torch.bfloat16) if t.is_floating_point() else t)(torch.as_tensor(a))

    def _A_setup():
        B, Sq, D = (1, 2048, 448)
        Hq, Hkv, Sk, Dh = (16, 8, 2048, 256)
        assert Hq % Hkv == 0, (Hq, Hkv)
        return {'x': _w((lambda *s: torch.randn(*s, dtype=torch.bfloat16) * 0.05)(B, Sq, D)), 'k_cache': _w((lambda *s: torch.randn(*s, dtype=torch.bfloat16) * 3.2)(B, Hkv, Sk, Dh)), 'v_cache': _w((lambda *s: torch.randn(*s, dtype=torch.bfloat16) * 0.4)(B, Hkv, Sk, Dh)), 'wq': _w((lambda *s: torch.randn(*s, dtype=torch.bfloat16) * 3.2)(D, Hq * Dh) * D ** (-0.5)), 'wo': _w((lambda *s: torch.randn(*s, dtype=torch.bfloat16) * 0.4)(Hq * Dh, D) * (Hq * Dh) ** (-0.5))}
    _A_in = _A_setup()

    def _B_setup():
        B, S, D, F, C = (1, 2048, 448, 1792, 1)
        return {'x': _w((lambda *s: torch.randn(*s, dtype=torch.bfloat16) * 0.05)(B, S, D)), 'w_r': _w((lambda *s: torch.randn(*s, dtype=torch.bfloat16) * 3.2)(D)), 'w_gate': _w((lambda *s: torch.randn(*s, dtype=torch.bfloat16) * 0.1)(D, F)), 'w_up': _w((lambda *s: torch.randn(*s, dtype=torch.bfloat16) * 0.1)(D, F)), 'w_down': _w((lambda *s: torch.randn(*s, dtype=torch.bfloat16) * 0.1)(F, D))}
    _B_in = _B_setup()
    return {'A_x': _A_in['x'], 'A_k_cache': _A_in['k_cache'], 'A_v_cache': _A_in['v_cache'], 'A_wq': _A_in['wq'], 'A_wo': _A_in['wo'], 'B_x': _B_in['x'], 'B_w_r': _B_in['w_r'], 'B_w_gate': _B_in['w_gate'], 'B_w_up': _B_in['w_up'], 'B_w_down': _B_in['w_down'], 'c': _w((lambda *s: torch.randn(*s, dtype=torch.bfloat16) * 0.1)(1, 448) * 8.0), 'w_mod': _w((lambda *s: torch.randn(*s, dtype=torch.bfloat16) * 0.1)(448, 6 * 448) * 448 ** (-0.5) * 8.0)}

def reference(A_x, A_k_cache, A_v_cache, A_wq, A_wo, B_x, B_w_r, B_w_gate, B_w_up, B_w_down, c, w_mod):

    def _A_ref(x, k_cache, v_cache, wq, wo):
        B, Sq, D = x.shape
        Hq, Dh = (16, 256)
        Hkv = k_cache.shape[1]
        rep = Hq // Hkv
        q = (lambda x, *d: x.permute(*d))((x @ wq).reshape(B, Sq, Hq, Dh), 0, 2, 1, 3)
        k = (lambda x, n, axis: torch.repeat_interleave(x, n, dim=axis))(k_cache, rep, axis=1)
        v = (lambda x, n, axis: torch.repeat_interleave(x, n, dim=axis))(v_cache, rep, axis=1)
        scores = torch.einsum('bhqd,bhkd->bhqk', q, k) * Dh ** (-0.5)
        p = _softmax(scores, axis=-1)
        o = torch.einsum('bhqk,bhkd->bhqd', p, v)
        return (lambda x, *d: x.permute(*d))(o, 0, 2, 1, 3).reshape(B, Sq, Hq * Dh) @ wo

    def _B_ref(x, w_r, w_gate, w_up, w_down):
        B, S, D = x.shape
        C = 1
        r = x @ w_r
        idx = torch.argsort(-r, axis=-1)[:, :C]
        idx_e = (lambda x, n, axis: torch.repeat_interleave(x, n, dim=axis))(idx.reshape(B, C, 1), D, axis=2)
        x_sel = (lambda x, i, axis: torch.take_along_dim(x, i, dim=axis))(x, idx_e, axis=1)
        g = _silu(x_sel @ w_gate)
        u = x_sel @ w_up
        y = g * u @ w_down
        r_sel = _sigmoid((lambda x, i, axis: torch.take_along_dim(x, i, dim=axis))(r, idx, axis=1))
        y = y * r_sel.reshape(B, C, 1)
        b_off = (torch.arange(B) * S).reshape(B, 1)
        flat_idx = (idx + b_off).reshape(B * C)
        zero = (x * 0.0).reshape(B * S, D)
        upd = _scatter_add(zero, flat_idx, y.reshape(B * C, D), axis=0)
        return x + upd.reshape(B, S, D)
    x = A_x
    mod = (_silu(c) @ w_mod).reshape(c.shape[0], 6, 1, -1)
    sa, ca, ga, sb, cb, gb = (mod[:, 0].clone(), mod[:, 1].clone(), mod[:, 2].clone(), mod[:, 3].clone(), mod[:, 4].clone(), mod[:, 5].clone())
    h = _layernorm(x, axis=-1) * (1 + ca) + sa
    x = x + ga * _A_ref(h, A_k_cache, A_v_cache, A_wq, A_wo)
    h = _layernorm(x, axis=-1) * (1 + cb) + sb
    x = x + gb * _B_ref(h, B_w_r, B_w_gate, B_w_up, B_w_down)
    return x
```

```cpp
#include <hip/hip_runtime.h>
#include <hip/hip_bf16.h>
#include <stdint.h>
#define MIMIC 0
typedef unsigned short u16;
__device__ __forceinline__ float b2f(u16 v){ return __uint_as_float(((unsigned)v)<<16); }
__device__ __forceinline__ u16 f2b(float f){ __hip_bfloat16 h = __float2bfloat16(f); return *(u16*)&h; }
__device__ __forceinline__ float rb(float f){ return b2f(f2b(f)); }
constexpr int S=2048, D=448, HQ=16, HKV=8, DH=256, F=1792;

__global__ void k_mod(const u16* c, const u16* w, float* mod){
  int n = blockIdx.x*blockDim.x+threadIdx.x; if(n>=6*D) return;
  float acc=0;
  for(int k=0;k<D;k++){ float x=b2f(c[k]); float s=rb(x/(1.f+__expf(-x))); acc+=s*b2f(w[k*6*D+n]); }
  mod[n]=rb(acc);
}
__device__ float wsum(float v){ for(int o=32;o;o>>=1) v+=__shfl_xor(v,o); return v; }
__global__ void k_ln(const u16* x, const float* sh, const float* sc, u16* h){
  int row=blockIdx.x, l=threadIdx.x; float v[7]; float s=0;
  for(int i=0;i<7;i++){ v[i]=b2f(x[row*D+l+64*i]); s+=v[i]; }
  float mean=wsum(s)/D; float q=0; for(int i=0;i<7;i++){ float d=v[i]-mean; q+=d*d; }
  float var=rb(wsum(q)/D); float m=rb(mean); float sd=rb(sqrtf(rb(var+1e-5f)));
  for(int i=0;i<7;i++){ int c=l+64*i; float t=rb(rb(v[i]-m)/sd); t=rb(t*rb(1.f+sc[c])); h[row*D+c]=f2b(t+sh[c]); }
}
__global__ void k_gemm(const u16* A, const u16* B, u16* C, int M, int N, int K){
  __shared__ float As[16][17], Bs[16][17];
  int tx=threadIdx.x, ty=threadIdx.y; int r=blockIdx.y*16+ty, c=blockIdx.x*16+tx; float acc=0;
  for(int k0=0;k0<K;k0+=16){ As[ty][tx]=b2f(A[r*K+k0+tx]); Bs[ty][tx]=b2f(B[(k0+ty)*N+c]); __syncthreads();
    for(int k=0;k<16;k++) acc+=As[ty][k]*Bs[k][tx]; __syncthreads(); }
  C[r*N+c]=f2b(acc);
}
__global__ void k_attn(const u16* q, const u16* K, const u16* V, u16* o){
  constexpr int R=4;
  __shared__ float qs[R][DH]; __shared__ float p[R][S]; __shared__ float red[R][4];
  int h=blockIdx.y, r0=blockIdx.x*R, t=threadIdx.x, kv=h/2;
  for(int i=0;i<R;i++) qs[i][t]=b2f(q[(r0+i)*HQ*DH+h*DH+t]);
  __syncthreads();
  float mx[R]; for(int i=0;i<R;i++) mx[i]=-1e30f;
  for(int k=t;k<S;k+=256){ const uint4* kp=(const uint4*)(K+((size_t)kv*S+k)*DH); float a[R]={0,0,0,0};
    for(int d=0;d<DH/8;d++){ uint4 u=kp[d]; unsigned w[4]={u.x,u.y,u.z,u.w};
      for(int j=0;j<4;j++){ float lo=__uint_as_float(w[j]<<16), hi=__uint_as_float(w[j]&0xffff0000u);
        for(int i=0;i<R;i++){ a[i]+=lo*qs[i][d*8+2*j]+hi*qs[i][d*8+2*j+1]; } } }
    for(int i=0;i<R;i++){ float s=a[i];
      s*=0.0625f; p[i][k]=s; mx[i]=fmaxf(mx[i],s);} }
  for(int i=0;i<R;i++){ float m=mx[i]; for(int o2=32;o2;o2>>=1) m=fmaxf(m,__shfl_xor(m,o2)); if((t&63)==0) red[i][t>>6]=m; }
  __syncthreads();
  float sm[R];
  for(int i=0;i<R;i++){ mx[i]=fmaxf(fmaxf(red[i][0],red[i][1]),fmaxf(red[i][2],red[i][3])); sm[i]=0; }
  __syncthreads();
  for(int k=t;k<S;k+=256) for(int i=0;i<R;i++){ float e=__expf(p[i][k]-mx[i]); sm[i]+=e; p[i][k]=rb(e); }
  for(int i=0;i<R;i++){ float s=wsum(sm[i]); if((t&63)==0) red[i][t>>6]=s; }
  __syncthreads();
  float acc[R]={0,0,0,0};
  for(int k=0;k<S;k++){ float v=b2f(V[((size_t)kv*S+k)*DH+t]); for(int i=0;i<R;i++) acc[i]+=p[i][k]*v; }
  for(int i=0;i<R;i++){ float l=red[i][0]+red[i][1]+red[i][2]+red[i][3]; o[(r0+i)*HQ*DH+h*DH+t]=f2b(acc[i]/l); }
}
__global__ void k_post(const u16* x, const u16* att, const float* mod, const u16* wr, u16* x1o, u16* h2o, float* r, u16* out){
  int row=blockIdx.x, l=threadIdx.x; float v[7]; float s=0;
  const float* ga=mod+2*D; const float* sb=mod+3*D; const float* cb=mod+4*D; const float* gb=mod+5*D;
  for(int i=0;i<7;i++){ int c=l+64*i; v[i]=rb(b2f(x[row*D+c])+rb(ga[c]*b2f(att[row*D+c]))); s+=v[i]; }
  float mean=wsum(s)/D; float q=0; for(int i=0;i<7;i++){ float d=v[i]-mean; q+=d*d; }
  float var=rb(wsum(q)/D); float m=rb(mean); float sd=rb(sqrtf(rb(var+1e-5f))); float ra=0;
  for(int i=0;i<7;i++){ int c=l+64*i; float t=rb(rb(v[i]-m)/sd); t=rb(t*rb(1.f+cb[c])); t=rb(t+sb[c]);
    h2o[row*D+c]=f2b(t); x1o[row*D+c]=f2b(v[i]); ra+=t*b2f(wr[c]); out[row*D+c]=f2b(v[i]+rb(gb[c]*t)); }
  ra=wsum(ra); if(l==0) r[row]=rb(ra);
}
__global__ void k_moe(const float* r, const u16* x1, const u16* h2, const u16* wg, const u16* wu, const u16* wd, const float* mod, u16* out){
  __shared__ float bv[256]; __shared__ int bi[256]; __shared__ float xs[D]; __shared__ float gu[F];
  int t=threadIdx.x; float b=-INFINITY; int ix=0;
  for(int i=t;i<S;i+=256){ if(r[i]>b){b=r[i];ix=i;} }
  bv[t]=b; bi[t]=ix; __syncthreads();
  if(t==0){ for(int i=1;i<256;i++){ if(bv[i]>b || (bv[i]==b && bi[i]<ix)){b=bv[i];ix=bi[i];} } bv[0]=b; bi[0]=ix; }
  __syncthreads(); b=bv[0]; ix=bi[0];
  for(int i=t;i<D;i+=256) xs[i]=b2f(h2[ix*D+i]); __syncthreads();
  for(int f=t;f<F;f+=256){ float g=0,u=0; for(int k=0;k<D;k++){ g+=xs[k]*b2f(wg[k*F+f]); u+=xs[k]*b2f(wu[k*F+f]); }
    g=rb(g); u=rb(u); g=rb(g/(1.f+__expf(-g))); gu[f]=rb(g*u); }
  __syncthreads();
  float rs=rb(1.f/(1.f+__expf(-b))); const float* gb=mod+5*D;
  for(int c=t;c<D;c+=256){ float y=0; for(int f=0;f<F;f++) y+=gu[f]*b2f(wd[f*D+c]); y=rb(rb(y)*rs);
    float hh=rb(xs[c]+y); out[ix*D+c]=f2b(b2f(x1[ix*D+c])+rb(gb[c]*hh)); }
}
extern "C" void kernel_launch(void* const* d_in, const int* in_sizes, int n_in, void* d_out, int out_size, void* d_ws, size_t ws_size, hipStream_t stream){
  const u16* x=(const u16*)d_in[0]; const u16* K=(const u16*)d_in[1]; const u16* V=(const u16*)d_in[2];
  const u16* wq=(const u16*)d_in[3]; const u16* wo=(const u16*)d_in[4]; const u16* wr=(const u16*)d_in[6];
  const u16* wg=(const u16*)d_in[7]; const u16* wu=(const u16*)d_in[8]; const u16* wd=(const u16*)d_in[9];
  const u16* c=(const u16*)d_in[10]; const u16* wm=(const u16*)d_in[11];
  char* ws=(char*)d_ws; float* mod=(float*)ws; float* r=(float*)(ws+65536);
  u16* h=(u16*)(ws+(1<<20)); u16* att=(u16*)(ws+(4<<20)); u16* x1=(u16*)(ws+(8<<20)); u16* h2=(u16*)(ws+(12<<20));
  u16* q=(u16*)(ws+(32<<20)); u16* o=(u16*)(ws+(64<<20));
  k_mod<<<(6*D+63)/64,64,0,stream>>>(c,wm,mod);
  k_ln<<<S,64,0,stream>>>(x,mod,mod+D,h);
  k_gemm<<<dim3(HQ*DH/16,S/16),dim3(16,16),0,stream>>>(h,wq,q,S,HQ*DH,D);
  k_attn<<<dim3(S/4,HQ),256,0,stream>>>(q,K,V,o);
  k_gemm<<<dim3(D/16,S/16),dim3(16,16),0,stream>>>(o,wo,att,S,D,HQ*DH);
  k_post<<<S,64,0,stream>>>(x,att,mod,wr,x1,h2,r,(u16*)d_out);
  k_moe<<<1,256,0,stream>>>(r,x1,h2,wg,wu,wd,mod,(u16*)d_out);
}
```

```cpp
#include <hip/hip_runtime.h>

__global__ __launch_bounds__(256, 1) void k_main(const void* x, const void* cnd, const void* wm, const void* wq, const void* K,
                                                const void* V, const void* wo, void* ws, const void* wr, const void* wg,
                                                const void* wu, const void* wd, void* out) {
  __shared__ __attribute__((aligned(16))) char lds[163840];
  unsigned tid = threadIdx.x;
  asm volatile("" ::"v"(lds));
  asm volatile(
      "s_load_dword s53, s[50:51], 0x20000\n"
      "v_and_b32 v254, 63, v255\n"
      "v_lshrrev_b32 v243, 6, v255\n"
      "v_lshlrev_b32 v235, 2, v255\n"
      "v_readfirstlane_b32 s80, v243\n"
      "s_mov_b32 s76, s38\n"
      "s_and_b32 s77, s39, 0xffff\n"
      "s_mov_b32 s78, 896\n"
      "s_mov_b32 s79, 0x20000\n"
      "buffer_load_dword v234, v235, s[76:79], 0 offen\n"
      "s_and_b32 s93, s52, 7\n"
      "s_lshr_b32 s92, s52, 3\n"
      "s_lshl_b32 s81, s80, 13\n"
      "s_mov_b32 s76, s50\n"
      "s_and_b32 s77, s51, 0xffff\n"
      "s_mov_b32 s78, 131072\n"
      "s_mov_b32 s79, 0x20000\n"
      "s_lshr_b32 s84, s92, 2\n"
      "s_lshl_b32 s84, s84, 3\n"
      "s_add_u32 s84, s84, s93\n"
      "s_lshl_b32 s84, s84, 2\n"
      "s_and_b32 s85, s92, 3\n"
      "s_add_u32 s90, s84, s85\n"
      "s_lshl_b32 s84, s90, 5\n"
      "s_sub_u32 s85, 2408448, s84\n"
      "s_cmp_lt_u32 s90, 168\n"
      "s_cselect_b32 s85, s85, 0\n"
      "s_cselect_b32 s84, s84, 0\n"
      "s_add_u32 s68, s40, s84\n"
      "s_addc_u32 s69, s41, 0\n"
      "s_and_b32 s69, s69, 0xffff\n"
      "s_mov_b32 s70, s85\n"
      "s_mov_b32 s71, 0x20000\n"
      "v_lshrrev_b32 v243, 1, v254\n"
      "v_mul_u32_u24 v243, 5376, v243\n"
      "v_and_b32 v252, 1, v254\n"
      "v_lshl_add_u32 v243, v252, 4, v243\n"
      "s_mul_i32 s84, s80, 688128\n"
      "s_add_u32 m0, s81, 131072\n"
      "s_add_u32 s85, s84, 0\n"
      "buffer_load_dwordx4 v243, s[68:71], s85 offen lds\n"
      "s_add_u32 m0, s81, 132096\n"
      "s_add_u32 s85, s84, 172032\n"
      "buffer_load_dwordx4 v243, s[68:71], s85 offen lds\n"
      "s_add_u32 m0, s81, 133120\n"
      "s_add_u32 s85, s84, 344064\n"
      "buffer_load_dwordx4 v243, s[68:71], s85 offen lds\n"
      "s_add_u32 m0, s81, 134144\n"
      "s_add_u32 s85, s84, 516096\n"
      "buffer_load_dwordx4 v243, s[68:71], s85 offen lds\n"
      "s_mov_b32 s72, s36\n"
      "s_and_b32 s73, s37, 0xffff\n"
      "s_mov_b32 s74, 1835008\n"
      "s_mov_b32 s75, 0x20000\n"
      "s_mul_i32 s98, s92, 7\n"
      "s_lshr_b32 s98, s98, 5\n"
      "s_lshl_b32 s84, s98, 3\n"
      "v_add_u32 v243, s84, v254\n"
      "v_subrev_u32 v252, 56, v243\n"
      "v_min_u32 v243, v243, v252\n"
      "v_cmp_gt_u32 vcc, 56, v254\n"
      "v_mov_b32 v252, 0x1000000\n"
      "v_lshlrev_b32 v242, 4, v254\n"
      "v_cndmask_b32 v240, v252, v243, vcc\n"
      "v_lshlrev_b32 v255, 4, v240\n"
      "s_lshl_b32 s96, s92, 2\n"
      "s_add_u32 s96, s96, s80\n"
      "s_mul_i32 s96, s96, 14336\n"
      "buffer_load_dwordx4 v[128:131], v255, s[72:75], s96 offen offset:0\n"
      "buffer_load_dwordx4 v[132:135], v255, s[72:75], s96 offen offset:896\n"
      "buffer_load_dwordx4 v[136:139], v255, s[72:75], s96 offen offset:1792\n"
      "buffer_load_dwordx4 v[140:143], v255, s[72:75], s96 offen offset:2688\n"
      "s_add_u32 s96, s96, 3584\n"
      "buffer_load_dwordx4 v[144:147], v255, s[72:75], s96 offen offset:0\n"
      "buffer_load_dwordx4 v[148:151], v255, s[72:75], s96 offen offset:896\n"
      "buffer_load_dwordx4 v[152:155], v255, s[72:75], s96 offen offset:1792\n"
      "buffer_load_dwordx4 v[156:159], v255, s[72:75], s96 offen offset:2688\n"
      "s_add_u32 s96, s96, 3584\n"
      "s_lshl_b32 s84, s93, 10\n"
      "s_sub_u32 s85, 3670016, s84\n"
      "s_add_u32 s64, s42, s84\n"
      "s_addc_u32 s65, s43, 0\n"
      "s_and_b32 s65, s65, 0xffff\n"
      "s_mov_b32 s66, s85\n"
      "s_mov_b32 s67, 0x20000\n"
      "s_lshl_b32 s84, s93, 20\n"
      "s_add_u32 s56, s44, s84\n"
      "s_addc_u32 s57, s45, 0\n"
      "s_and_b32 s57, s57, 0xffff\n"
      "s_mov_b32 s58, 1048576\n"
      "s_mov_b32 s59, 0x20000\n"
      "s_add_u32 s60, s46, s84\n"
      "s_addc_u32 s61, s47, 0\n"
      "s_and_b32 s61, s61, 0xffff\n"
      "s_mov_b32 s62, 1048576\n"
      "s_mov_b32 s63, 0x20000\n"
      "s_mov_b32 s82, 0\n"
      "s_mov_b32 s83, 32768\n"
      "v_lshlrev_b32 v216, 7, v254\n"
      "buffer_load_dword a254, off, s[76:79], 0 sc1\n"
      "v_lshrrev_b32 v252, 4, v254\n"
      "v_and_b32 v244, 15, v254\n"
      "s_lshl_b32 s84, s80, 8\n"
      "v_lshl_add_u32 v245, v252, 1, 0\n"
      "v_xor_b32 v245, v245, v244\n"
      "v_lshlrev_b32 v245, 4, v245\n"
      "v_lshl_add_u32 v245, v252, 13, v245\n"
      "v_add_u32 v245, s84, v245\n"
      "v_add_u32 v208, 0, v245\n"
      "v_add_u32 v209, 31744, v245\n"
      "v_add_u32 v212, 131072, v245\n"
      "v_add_u32 v213, 162816, v245\n"
      "v_lshl_add_u32 v245, v252, 1, 8\n"
      "v_xor_b32 v245, v245, v244\n"
      "v_lshlrev_b32 v245, 4, v245\n"
      "v_lshl_add_u32 v245, v252, 13, v245\n"
      "v_add_u32 v245, s84, v245\n"
      "v_add_u32 v210, 63488, v245\n"
      "v_add_u32 v211, 95232, v245\n"
      "v_add_u32 v214, 194560, v245\n"
      "v_add_u32 v215, 226304, v245\n"
      "s_lshl_b32 s91, s98, 19\n"
      "v_bfe_u32 v244, v254, 2, 2\n"
      "v_lshl_add_u32 v244, v252, 3, v244\n"
      "v_and_b32 v245, 3, v254\n"
      "v_lshlrev_b32 v245, 3, v245\n"
      "v_lshl_add_u32 v236, v244, 5, v245\n"
      "s_add_u32 s85, s81, 131072\n"
      "v_add_u32 v236, s85, v236\n"
      "s_add_u32 s84, s84, 90112\n"
      "v_lshl_add_u32 v237, v252, 4, s84\n"
      "v_bfe_u32 v253, v254, 2, 3\n"
      "v_lshl_add_u32 v253, v253, 5, v245\n"
      "v_lshl_add_u32 v244, v244, 8, v253\n"
      "v_add_u32 v192, s81, v244\n"
      "v_xor_b32 v193, 32, v192\n"
      "v_xor_b32 v194, 64, v192\n"
      "v_xor_b32 v195, 96, v192\n"
      "v_xor_b32 v196, 128, v192\n"
      "v_xor_b32 v197, 160, v192\n"
      "v_xor_b32 v198, 192, v192\n"
      "v_xor_b32 v199, 224, v192\n"
      "v_add_u32 v200, 98304, v192\n"
      "v_add_u32 v201, 98304, v193\n"
      "v_add_u32 v202, 98304, v194\n"
      "v_add_u32 v203, 98304, v195\n"
      "v_add_u32 v204, 98304, v196\n"
      "v_add_u32 v205, 98304, v197\n"
      "v_add_u32 v206, 98304, v198\n"
      "v_add_u32 v207, 98304, v199\n"
      "v_add_u32 v239, 91136, v242\n"
      "v_lshl_add_u32 v238, s80, 10, v239\n"
      "v_add_u32 v235, 90112, v235\n"
      "v_and_b32 v244, 14, v254\n"
      "v_lshlrev_b32 v244, 13, v244\n"
      "v_lshl_add_u32 v244, v252, 4, v244\n"
      "v_and_b32 v245, 1, v254\n"
      "v_lshl_add_u32 v241, v245, 3, v244\n"
      "v_mov_b32 v253, 0xffff0000\n"
      "v_lshlrev_b32 v240, 5, v240\n"
      "v_lshl_add_u32 v240, s93, 14, v240\n"
      "s_waitcnt vmcnt(13)\n"
      "v_lshlrev_b32 v244, 16, v234\n"
      "v_and_b32 v245, 0xffff0000, v234\n"
      "v_mul_f32 v246, 0xbfb8aa3b, v244\n"
      "v_mul_f32 v247, 0xbfb8aa3b, v245\n"
      "v_exp_f32 v246, v246\n"
      "v_exp_f32 v247, v247\n"
      "s_nop 0\n"
      "v_add_f32 v246, 1.0, v246\n"
      "v_add_f32 v247, 1.0, v247\n"
      "v_rcp_f32 v246, v246\n"
      "v_rcp_f32 v247, v247\n"
      "s_nop 0\n"
      "v_mul_f32 v244, v244, v246\n"
      "v_mul_f32 v245, v245, v247\n"
      "v_cvt_pk_bf16_f32 v244, v244, v245\n"
      "ds_write_b32 v235, v244\n"
      "s_waitcnt vmcnt(9) lgkmcnt(0)\n"
      "s_barrier\n"
      "ds_read_b64_tr_b16 a[0:1], v236 offset:0\n"
      "ds_read_b64_tr_b16 a[2:3], v236 offset:128\n"
      "ds_read_b128 a[4:7], v237 offset:0\n"
      "ds_read_b64_tr_b16 a[8:9], v236 offset:1024\n"
      "ds_read_b64_tr_b16 a[10:11], v236 offset:1152\n"
      "ds_read_b128 a[12:15], v237 offset:64\n"
      "ds_read_b64_tr_b16 a[16:17], v236 offset:2048\n"
      "ds_read_b64_tr_b16 a[18:19], v236 offset:2176\n"
      "ds_read_b128 a[20:23], v237 offset:128\n"
      "ds_read_b64_tr_b16 a[24:25], v236 offset:3072\n"
      "ds_read_b64_tr_b16 a[26:27], v236 offset:3200\n"
      "ds_read_b128 a[28:31], v237 offset:192\n"
      "s_mul_i32 s53, s53, 0x9e3779b1\n"
      "s_add_u32 s53, s53, 0x7f4a7c15\n"
      "buffer_load_dwordx4 v[160:163], v255, s[72:75], s96 offen offset:0\n"
      "buffer_load_dwordx4 v[164:167], v255, s[72:75], s96 offen offset:896\n"
      "buffer_load_dwordx4 v[168:171], v255, s[72:75], s96 offen offset:1792\n"
      "buffer_load_dwordx4 v[172:175], v255, s[72:75], s96 offen offset:2688\n"
      "s_add_u32 s96, s96, 3584\n"
      "s_waitcnt lgkmcnt(0)\n"
      "v_mfma_f32_16x16x32_bf16 v[248:251], a[0:3], a[4:7], 0\n"
      "v_mfma_f32_16x16x32_bf16 v[248:251], a[8:11], a[12:15], v[248:251]\n"
      "v_mfma_f32_16x16x32_bf16 v[248:251], a[16:19], a[20:23], v[248:251]\n"
      "v_mfma_f32_16x16x32_bf16 v[248:251], a[24:27], a[28:31], v[248:251]\n"
      "s_sub_u32 s84, s90, 28\n"
      "s_sub_u32 s85, s90, 112\n"
      "s_min_u32 s84, s84, s85\n"
      "s_cmp_lt_u32 s84, 28\n"
      "s_cselect_b32 s86, 1.0, 0\n"
      "s_lshl_b32 s84, s90, 6\n"
      "s_lshl_b32 s85, s80, 20\n"
      "s_add_u32 s84, s84, s85\n"
      "s_cmp_lt_u32 s90, 168\n"
      "s_cselect_b32 s84, s84, 1073741824\n"
      "ds_write_b128 v238, v[248:251]\n"
      "s_waitcnt lgkmcnt(0)\n"
      "s_barrier\n"
      "ds_read_b128 v[218:221], v239 offset:0\n"
      "ds_read_b128 v[222:225], v239 offset:1024\n"
      "ds_read_b128 v[226:229], v239 offset:2048\n"
      "ds_read_b128 v[230:233], v239 offset:3072\n"
      "v_and_b32 v245, 1, v254\n"
      "v_cmp_eq_u32 vcc, 1, v245\n"
      "v_mov_b32 v245, s53\n"
      "s_waitcnt lgkmcnt(0)\n"
      "v_pk_add_f32 v[218:219], v[218:219], v[222:223]\n"
      "v_pk_add_f32 v[226:227], v[226:227], v[230:231]\n"
      "v_pk_add_f32 v[220:221], v[220:221], v[224:225]\n"
      "v_pk_add_f32 v[228:229], v[228:229], v[232:233]\n"
      "v_pk_add_f32 v[218:219], v[218:219], v[226:227]\n"
      "v_pk_add_f32 v[220:221], v[220:221], v[228:229]\n"
      "v_cvt_pk_bf16_f32 v218, 0, v218\n"
      "v_cvt_pk_bf16_f32 v219, 0, v219\n"
      "v_cvt_pk_bf16_f32 v220, 0, v220\n"
      "v_cvt_pk_bf16_f32 v221, 0, v221\n"
      "v_add_f32 v218, s86, v218\n"
      "v_add_f32 v219, s86, v219\n"
      "v_add_f32 v220, s86, v220\n"
      "v_add_f32 v221, s86, v221\n"
      "v_cvt_pk_bf16_f32 v218, v218, v219\n"
      "v_cvt_pk_bf16_f32 v220, v220, v221\n"
      "v_cndmask_b32 v244, v218, v220, vcc\n"
      "buffer_store_dwordx2 v[244:245], v241, s[76:79], s84 offen sc1\n"
      "s_mul_i32 s84, s93, 458752\n"
      "s_add_u32 s68, s48, s84\n"
      "s_addc_u32 s69, s49, 0\n"
      "s_and_b32 s69, s69, 0xffff\n"
      "s_mov_b32 s70, 458752\n"
      "s_mov_b32 s71, 0x20000\n"
      "buffer_load_dwordx4 v[176:179], v255, s[72:75], s96 offen offset:0\n"
      "buffer_load_dwordx4 v[180:183], v255, s[72:75], s96 offen offset:896\n"
      "buffer_load_dwordx4 v[184:187], v255, s[72:75], s96 offen offset:1792\n"
      "buffer_load_dwordx4 v[188:191], v255, s[72:75], s96 offen offset:2688\n"
      "s_mov_b32 s75, 0x20000\n"
      "s_mul_i32 s84, s92, 114688\n"
      "s_lshl_b32 s85, s80, 16\n"
      "s_add_u32 s84, s84, s85\n"
      "s_lshl_b32 s85, s92, 10\n"
      "s_cmp_eq_u32 s80, 2\n"
      "s_cselect_b64 s[72:73], s[56:57], s[60:61]\n"
      "s_cmp_lt_u32 s80, 2\n"
      "s_cselect_b64 s[72:73], s[64:65], s[72:73]\n"
      "s_cselect_b32 s74, s66, 163840\n"
      "s_cselect_b32 s84, s84, s85\n"
      "s_cselect_b32 s85, 7168, s83\n"
      "s_cselect_b32 s86, 0, 1024\n"
      "s_sub_u32 s85, s85, s86\n"
      "v_lshrrev_b32 v243, 3, v254\n"
      "v_mul_u32_u24 v243, s85, v243\n"
      "v_add_u32 v243, v243, v216\n"
      "buffer_load_dword v217, v243, s[72:75], s84 offen\n"
      "s_getpc_b64 s[72:73]\n"
      "KPC_%=:\n"
      "s_and_b32 s73, s73, 0xffff\n"
      "s_mov_b32 s74, KEND_%=-KPC_%=\n"
      "s_sub_u32 s84, 31, s92\n"
      "s_lshl_b32 s84, s84, 13\n"
      "s_cmp_eq_u32 s80, 2\n"
      "s_cselect_b32 s84, s84, 1073741824\n"
      "buffer_load_dword v217, v216, s[72:75], s84 offen\n"
      "s_waitcnt vmcnt(16)\n"
      "v_lshlrev_b32 v0, 16, v128\n"
      "v_and_b32 v1, v253, v128\n"
      "v_lshlrev_b32 v2, 16, v129\n"
      "v_and_b32 v3, v253, v129\n"
      "v_lshlrev_b32 v4, 16, v130\n"
      "v_and_b32 v5, v253, v130\n"
      "v_lshlrev_b32 v6, 16, v131\n"
      "v_and_b32 v7, v253, v131\n"
      "v_pk_add_f32 v[248:249], v[0:1], v[2:3]\n"
      "v_pk_add_f32 v[250:251], v[4:5], v[6:7]\n"
      "v_pk_add_f32 v[248:249], v[248:249], v[250:251]\n"
      "v_add_f32 v218, v248, v249\n"
      "v_lshlrev_b32 v8, 16, v132\n"
      "v_and_b32 v9, v253, v132\n"
      "v_lshlrev_b32 v10, 16, v133\n"
      "v_and_b32 v11, v253, v133\n"
      "v_lshlrev_b32 v12, 16, v134\n"
      "v_and_b32 v13, v253, v134\n"
      "v_lshlrev_b32 v14, 16, v135\n"
      "v_and_b32 v15, v253, v135\n"
      "v_pk_add_f32 v[248:249], v[8:9], v[10:11]\n"
      "v_pk_add_f32 v[250:251], v[12:13], v[14:15]\n"
      "v_pk_add_f32 v[248:249], v[248:249], v[250:251]\n"
      "v_add_f32 v219, v248, v249\n"
      "v_lshlrev_b32 v16, 16, v136\n"
      "v_and_b32 v17, v253, v136\n"
      "v_lshlrev_b32 v18, 16, v137\n"
      "v_and_b32 v19, v253, v137\n"
      "v_lshlrev_b32 v20, 16, v138\n"
      "v_and_b32 v21, v253, v138\n"
      "v_lshlrev_b32 v22, 16, v139\n"
      "v_and_b32 v23, v253, v139\n"
      "v_pk_add_f32 v[248:249], v[16:17], v[18:19]\n"
      "v_pk_add_f32 v[250:251], v[20:21], v[22:23]\n"
      "v_pk_add_f32 v[248:249], v[248:249], v[250:251]\n"
      "v_add_f32 v220, v248, v249\n"
      "v_lshlrev_b32 v24, 16, v140\n"
      "v_and_b32 v25, v253, v140\n"
      "v_lshlrev_b32 v26, 16, v141\n"
      "v_and_b32 v27, v253, v141\n"
      "v_lshlrev_b32 v28, 16, v142\n"
      "v_and_b32 v29, v253, v142\n"
      "v_lshlrev_b32 v30, 16, v143\n"
      "v_and_b32 v31, v253, v143\n"
      "v_pk_add_f32 v[248:249], v[24:25], v[26:27]\n"
      "v_pk_add_f32 v[250:251], v[28:29], v[30:31]\n"
      "v_pk_add_f32 v[248:249], v[248:249], v[250:251]\n"
      "v_add_f32 v221, v248, v249\n"
      "s_waitcnt vmcnt(12)\n"
      "v_lshlrev_b32 v32, 16, v144\n"
      "v_and_b32 v33, v253, v144\n"
      "v_lshlrev_b32 v34, 16, v145\n"
      "v_and_b32 v35, v253, v145\n"
      "v_lshlrev_b32 v36, 16, v146\n"
      "v_and_b32 v37, v253, v146\n"
      "v_lshlrev_b32 v38, 16, v147\n"
      "v_and_b32 v39, v253, v147\n"
      "v_pk_add_f32 v[248:249], v[32:33], v[34:35]\n"
      "v_pk_add_f32 v[250:251], v[36:37], v[38:39]\n"
      "v_pk_add_f32 v[248:249], v[248:249], v[250:251]\n"
      "v_add_f32 v222, v248, v249\n"
      "v_lshlrev_b32 v40, 16, v148\n"
      "v_and_b32 v41, v253, v148\n"
      "v_lshlrev_b32 v42, 16, v149\n"
      "v_and_b32 v43, v253, v149\n"
      "v_lshlrev_b32 v44, 16, v150\n"
      "v_and_b32 v45, v253, v150\n"
      "v_lshlrev_b32 v46, 16, v151\n"
      "v_and_b32 v47, v253, v151\n"
      "v_pk_add_f32 v[248:249], v[40:41], v[42:43]\n"
      "v_pk_add_f32 v[250:251], v[44:45], v[46:47]\n"
      "v_pk_add_f32 v[248:249], v[248:249], v[250:251]\n"
      "v_add_f32 v223, v248, v249\n"
      "v_lshlrev_b32 v48, 16, v152\n"
      "v_and_b32 v49, v253, v152\n"
      "v_lshlrev_b32 v50, 16, v153\n"
      "v_and_b32 v51, v253, v153\n"
      "v_lshlrev_b32 v52, 16, v154\n"
      "v_and_b32 v53, v253, v154\n"
      "v_lshlrev_b32 v54, 16, v155\n"
      "v_and_b32 v55, v253, v155\n"
      "v_pk_add_f32 v[248:249], v[48:49], v[50:51]\n"
      "v_pk_add_f32 v[250:251], v[52:53], v[54:55]\n"
      "v_pk_add_f32 v[248:249], v[248:249], v[250:251]\n"
      "v_add_f32 v224, v248, v249\n"
      "v_lshlrev_b32 v56, 16, v156\n"
      "v_and_b32 v57, v253, v156\n"
      "v_lshlrev_b32 v58, 16, v157\n"
      "v_and_b32 v59, v253, v157\n"
      "v_lshlrev_b32 v60, 16, v158\n"
      "v_and_b32 v61, v253, v158\n"
      "v_lshlrev_b32 v62, 16, v159\n"
      "v_and_b32 v63, v253, v159\n"
      "v_pk_add_f32 v[248:249], v[56:57], v[58:59]\n"
      "v_pk_add_f32 v[250:251], v[60:61], v[62:63]\n"
      "v_pk_add_f32 v[248:249], v[248:249], v[250:251]\n"
      "v_add_f32 v225, v248, v249\n"
      "s_waitcnt vmcnt(7)\n"
      "v_lshlrev_b32 v64, 16, v160\n"
      "v_and_b32 v65, v253, v160\n"
      "v_lshlrev_b32 v66, 16, v161\n"
      "v_and_b32 v67, v253, v161\n"
      "v_lshlrev_b32 v68, 16, v162\n"
      "v_and_b32 v69, v253, v162\n"
      "v_lshlrev_b32 v70, 16, v163\n"
      "v_and_b32 v71, v253, v163\n"
      "v_pk_add_f32 v[248:249], v[64:65], v[66:67]\n"
      "v_pk_add_f32 v[250:251], v[68:69], v[70:71]\n"
      "v_pk_add_f32 v[248:249], v[248:249], v[250:251]\n"
      "v_add_f32 v226, v248, v249\n"
      "v_lshlrev_b32 v72, 16, v164\n"
      "v_and_b32 v73, v253, v164\n"
      "v_lshlrev_b32 v74, 16, v165\n"
      "v_and_b32 v75, v253, v165\n"
      "v_lshlrev_b32 v76, 16, v166\n"
      "v_and_b32 v77, v253, v166\n"
      "v_lshlrev_b32 v78, 16, v167\n"
      "v_and_b32 v79, v253, v167\n"
      "v_pk_add_f32 v[248:249], v[72:73], v[74:75]\n"
      "v_pk_add_f32 v[250:251], v[76:77], v[78:79]\n"
      "v_pk_add_f32 v[248:249], v[248:249], v[250:251]\n"
      "v_add_f32 v227, v248, v249\n"
      "v_lshlrev_b32 v80, 16, v168\n"
      "v_and_b32 v81, v253, v168\n"
      "v_lshlrev_b32 v82, 16, v169\n"
      "v_and_b32 v83, v253, v169\n"
      "v_lshlrev_b32 v84, 16, v170\n"
      "v_and_b32 v85, v253, v170\n"
      "v_lshlrev_b32 v86, 16, v171\n"
      "v_and_b32 v87, v253, v171\n"
      "v_pk_add_f32 v[248:249], v[80:81], v[82:83]\n"
      "v_pk_add_f32 v[250:251], v[84:85], v[86:87]\n"
      "v_pk_add_f32 v[248:249], v[248:249], v[250:251]\n"
      "v_add_f32 v228, v248, v249\n"
      "v_lshlrev_b32 v88, 16, v172\n"
      "v_and_b32 v89, v253, v172\n"
      "v_lshlrev_b32 v90, 16, v173\n"
      "v_and_b32 v91, v253, v173\n"
      "v_lshlrev_b32 v92, 16, v174\n"
      "v_and_b32 v93, v253, v174\n"
      "v_lshlrev_b32 v94, 16, v175\n"
      "v_and_b32 v95, v253, v175\n"
      "v_pk_add_f32 v[248:249], v[88:89], v[90:91]\n"
      "v_pk_add_f32 v[250:251], v[92:93], v[94:95]\n"
      "v_pk_add_f32 v[248:249], v[248:249], v[250:251]\n"
      "v_add_f32 v229, v248, v249\n"
      "s_waitcnt vmcnt(2)\n"
      "v_lshlrev_b32 v96, 16, v176\n"
      "v_and_b32 v97, v253, v176\n"
      "v_lshlrev_b32 v98, 16, v177\n"
      "v_and_b32 v99, v253, v177\n"
      "v_lshlrev_b32 v100, 16, v178\n"
      "v_and_b32 v101, v253, v178\n"
      "v_lshlrev_b32 v102, 16, v179\n"
      "v_and_b32 v103, v253, v179\n"
      "v_pk_add_f32 v[248:249], v[96:97], v[98:99]\n"
      "v_pk_add_f32 v[250:251], v[100:101], v[102:103]\n"
      "v_pk_add_f32 v[248:249], v[248:249], v[250:251]\n"
      "v_add_f32 v230, v248, v249\n"
      "v_lshlrev_b32 v104, 16, v180\n"
      "v_and_b32 v105, v253, v180\n"
      "v_lshlrev_b32 v106, 16, v181\n"
      "v_and_b32 v107, v253, v181\n"
      "v_lshlrev_b32 v108, 16, v182\n"
      "v_and_b32 v109, v253, v182\n"
      "v_lshlrev_b32 v110, 16, v183\n"
      "v_and_b32 v111, v253, v183\n"
      "v_pk_add_f32 v[248:249], v[104:105], v[106:107]\n"
      "v_pk_add_f32 v[250:251], v[108:109], v[110:111]\n"
      "v_pk_add_f32 v[248:249], v[248:249], v[250:251]\n"
      "v_add_f32 v231, v248, v249\n"
      "v_lshlrev_b32 v112, 16, v184\n"
      "v_and_b32 v113, v253, v184\n"
      "v_lshlrev_b32 v114, 16, v185\n"
      "v_and_b32 v115, v253, v185\n"
      "v_lshlrev_b32 v116, 16, v186\n"
      "v_and_b32 v117, v253, v186\n"
      "v_lshlrev_b32 v118, 16, v187\n"
      "v_and_b32 v119, v253, v187\n"
      "v_pk_add_f32 v[248:249], v[112:113], v[114:115]\n"
      "v_pk_add_f32 v[250:251], v[116:117], v[118:119]\n"
      "v_pk_add_f32 v[248:249], v[248:249], v[250:251]\n"
      "v_add_f32 v232, v248, v249\n"
      "v_lshlrev_b32 v120, 16, v188\n"
      "v_and_b32 v121, v253, v188\n"
      "v_lshlrev_b32 v122, 16, v189\n"
      "v_and_b32 v123, v253, v189\n"
      "v_lshlrev_b32 v124, 16, v190\n"
      "v_and_b32 v125, v253, v190\n"
      "v_lshlrev_b32 v126, 16, v191\n"
      "v_and_b32 v127, v253, v191\n"
      "v_pk_add_f32 v[248:249], v[120:121], v[122:123]\n"
      "v_pk_add_f32 v[250:251], v[124:125], v[126:127]\n"
      "v_pk_add_f32 v[248:249], v[248:249], v[250:251]\n"
      "v_add_f32 v233, v248, v249\n"
      "s_nop 1\n"
      "v_permlane32_swap_b32 v218, v226\n"
      "v_permlane32_swap_b32 v219, v227\n"
      "v_permlane32_swap_b32 v220, v228\n"
      "v_permlane32_swap_b32 v221, v229\n"
      "v_permlane32_swap_b32 v222, v230\n"
      "v_permlane32_swap_b32 v223, v231\n"
      "v_permlane32_swap_b32 v224, v232\n"
      "v_permlane32_swap_b32 v225, v233\n"
      "v_add_f32 v218, v218, v226\n"
      "v_add_f32 v219, v219, v227\n"
      "v_add_f32 v220, v220, v228\n"
      "v_add_f32 v221, v221, v229\n"
      "v_add_f32 v222, v222, v230\n"
      "v_add_f32 v223, v223, v231\n"
      "v_add_f32 v224, v224, v232\n"
      "v_add_f32 v225, v225, v233\n"
      "v_permlane16_swap_b32 v218, v222\n"
      "v_permlane16_swap_b32 v219, v223\n"
      "v_permlane16_swap_b32 v220, v224\n"
      "v_permlane16_swap_b32 v221, v225\n"
      "v_add_f32 v218, v218, v222\n"
      "v_add_f32 v219, v219, v223\n"
      "v_add_f32 v220, v220, v224\n"
      "v_add_f32 v221, v221, v225\n"
      "v_add_f32_dpp v218, v218, v218 row_ror:8 row_mask:0xf bank_mask:0xf\n"
      "v_add_f32_dpp v219, v219, v219 row_ror:8 row_mask:0xf bank_mask:0xf\n"
      "v_add_f32_dpp v220, v220, v220 row_ror:8 row_mask:0xf bank_mask:0xf\n"
      "v_add_f32_dpp v221, v221, v221 row_ror:8 row_mask:0xf bank_mask:0xf\n"
      "v_add_f32_dpp v218, v218, v218 row_ror:4 row_mask:0xf bank_mask:0xf\n"
      "v_add_f32_dpp v219, v219, v219 row_ror:4 row_mask:0xf bank_mask:0xf\n"
      "v_add_f32_dpp v220, v220, v220 row_ror:4 row_mask:0xf bank_mask:0xf\n"
      "v_add_f32_dpp v221, v221, v221 row_ror:4 row_mask:0xf bank_mask:0xf\n"
      "v_add_f32_dpp v218, v218, v218 row_ror:2 row_mask:0xf bank_mask:0xf\n"
      "v_add_f32_dpp v219, v219, v219 row_ror:2 row_mask:0xf bank_mask:0xf\n"
      "v_add_f32_dpp v220, v220, v220 row_ror:2 row_mask:0xf bank_mask:0xf\n"
      "v_add_f32_dpp v221, v221, v221 row_ror:2 row_mask:0xf bank_mask:0xf\n"
      "v_add_f32_dpp v218, v218, v218 row_ror:1 row_mask:0xf bank_mask:0xf\n"
      "v_add_f32_dpp v219, v219, v219 row_ror:1 row_mask:0xf bank_mask:0xf\n"
      "v_add_f32_dpp v220, v220, v220 row_ror:1 row_mask:0xf bank_mask:0xf\n"
      "v_add_f32_dpp v221, v221, v221 row_ror:1 row_mask:0xf bank_mask:0xf\n"
      "v_mul_f32 v218, 0xbb124925, v218\n"
      "v_mul_f32 v219, 0xbb124925, v219\n"
      "v_mul_f32 v220, 0xbb124925, v220\n"
      "v_mul_f32 v221, 0xbb124925, v221\n"
      "s_lshr_b64 exec, -1, 8\n"
      "v_readlane_b32 s84, v218, 0\n"
      "v_readlane_b32 s86, v219, 0\n"
      "s_nop 0\n"
      "v_pk_add_f32 v[128:129], v[0:1], s[84:85] op_sel:[0,0] op_sel_hi:[1,0]\n"
      "v_pk_add_f32 v[130:131], v[2:3], s[84:85] op_sel:[0,0] op_sel_hi:[1,0]\n"
      "v_pk_add_f32 v[132:133], v[4:5], s[84:85] op_sel:[0,0] op_sel_hi:[1,0]\n"
      "v_pk_add_f32 v[134:135], v[6:7], s[84:85] op_sel:[0,0] op_sel_hi:[1,0]\n"
      "v_pk_mul_f32 v[128:129], v[128:129], v[128:129]\n"
      "v_pk_mul_f32 v[130:131], v[130:131], v[130:131]\n"
      "v_pk_fma_f32 v[128:129], v[132:133], v[132:133], v[128:129]\n"
      "v_pk_fma_f32 v[130:131], v[134:135], v[134:135], v[130:131]\n"
      "v_pk_add_f32 v[128:129], v[128:129], v[130:131]\n"
      "v_add_f32 v176, v128, v129\n"
      "v_readlane_b32 s84, v220, 0\n"
      "v_pk_add_f32 v[128:129], v[8:9], s[86:87] op_sel:[0,0] op_sel_hi:[1,0]\n"
      "v_pk_add_f32 v[130:131], v[10:11], s[86:87] op_sel:[0,0] op_sel_hi:[1,0]\n"
      "v_pk_add_f32 v[132:133], v[12:13], s[86:87] op_sel:[0,0] op_sel_hi:[1,0]\n"
      "v_pk_add_f32 v[134:135], v[14:15], s[86:87] op_sel:[0,0] op_sel_hi:[1,0]\n"
      "v_pk_mul_f32 v[128:129], v[128:129], v[128:129]\n"
      "v_pk_mul_f32 v[130:131], v[130:131], v[130:131]\n"
      "v_pk_fma_f32 v[128:129], v[132:133], v[132:133], v[128:129]\n"
      "v_pk_fma_f32 v[130:131], v[134:135], v[134:135], v[130:131]\n"
      "v_pk_add_f32 v[128:129], v[128:129], v[130:131]\n"
      "v_add_f32 v177, v128, v129\n"
      "v_readlane_b32 s86, v221, 0\n"
      "v_pk_add_f32 v[128:129], v[16:17], s[84:85] op_sel:[0,0] op_sel_hi:[1,0]\n"
      "v_pk_add_f32 v[130:131], v[18:19], s[84:85] op_sel:[0,0] op_sel_hi:[1,0]\n"
      "v_pk_add_f32 v[132:133], v[20:21], s[84:85] op_sel:[0,0] op_sel_hi:[1,0]\n"
      "v_pk_add_f32 v[134:135], v[22:23], s[84:85] op_sel:[0,0] op_sel_hi:[1,0]\n"
      "v_pk_mul_f32 v[128:129], v[128:129], v[128:129]\n"
      "v_pk_mul_f32 v[130:131], v[130:131], v[130:131]\n"
      "v_pk_fma_f32 v[128:129], v[132:133], v[132:133], v[128:129]\n"
      "v_pk_fma_f32 v[130:131], v[134:135], v[134:135], v[130:131]\n"
      "v_pk_add_f32 v[128:129], v[128:129], v[130:131]\n"
      "v_add_f32 v178, v128, v129\n"
      "v_readlane_b32 s84, v218, 16\n"
      "v_pk_add_f32 v[128:129], v[24:25], s[86:87] op_sel:[0,0] op_sel_hi:[1,0]\n"
      "v_pk_add_f32 v[130:131], v[26:27], s[86:87] op_sel:[0,0] op_sel_hi:[1,0]\n"
      "v_pk_add_f32 v[132:133], v[28:29], s[86:87] op_sel:[0,0] op_sel_hi:[1,0]\n"
      "v_pk_add_f32 v[134:135], v[30:31], s[86:87] op_sel:[0,0] op_sel_hi:[1,0]\n"
      "v_pk_mul_f32 v[128:129], v[128:129], v[128:129]\n"
      "v_pk_mul_f32 v[130:131], v[130:131], v[130:131]\n"
      "v_pk_fma_f32 v[128:129], v[132:133], v[132:133], v[128:129]\n"
      "v_pk_fma_f32 v[130:131], v[134:135], v[134:135], v[130:131]\n"
      "v_pk_add_f32 v[128:129], v[128:129], v[130:131]\n"
      "v_add_f32 v179, v128, v129\n"
      "v_readlane_b32 s86, v219, 16\n"
      "v_pk_add_f32 v[128:129], v[32:33], s[84:85] op_sel:[0,0] op_sel_hi:[1,0]\n"
      "v_pk_add_f32 v[130:131], v[34:35], s[84:85] op_sel:[0,0] op_sel_hi:[1,0]\n"
      "v_pk_add_f32 v[132:133], v[36:37], s[84:85] op_sel:[0,0] op_sel_hi:[1,0]\n"
      "v_pk_add_f32 v[134:135], v[38:39], s[84:85] op_sel:[0,0] op_sel_hi:[1,0]\n"
      "v_pk_mul_f32 v[128:129], v[128:129], v[128:129]\n"
      "v_pk_mul_f32 v[130:131], v[130:131], v[130:131]\n"
      "v_pk_fma_f32 v[128:129], v[132:133], v[132:133], v[128:129]\n"
      "v_pk_fma_f32 v[130:131], v[134:135], v[134:135], v[130:131]\n"
      "v_pk_add_f32 v[128:129], v[128:129], v[130:131]\n"
      "v_add_f32 v180, v128, v129\n"
      "v_readlane_b32 s84, v220, 16\n"
      "v_pk_add_f32 v[128:129], v[40:41], s[86:87] op_sel:[0,0] op_sel_hi:[1,0]\n"
      "v_pk_add_f32 v[130:131], v[42:43], s[86:87] op_sel:[0,0] op_sel_hi:[1,0]\n"
      "v_pk_add_f32 v[132:133], v[44:45], s[86:87] op_sel:[0,0] op_sel_hi:[1,0]\n"
      "v_pk_add_f32 v[134:135], v[46:47], s[86:87] op_sel:[0,0] op_sel_hi:[1,0]\n"
      "v_pk_mul_f32 v[128:129], v[128:129], v[128:129]\n"
      "v_pk_mul_f32 v[130:131], v[130:131], v[130:131]\n"
      "v_pk_fma_f32 v[128:129], v[132:133], v[132:133], v[128:129]\n"
      "v_pk_fma_f32 v[130:131], v[134:135], v[134:135], v[130:131]\n"
      "v_pk_add_f32 v[128:129], v[128:129], v[130:131]\n"
      "v_add_f32 v181, v128, v129\n"
      "v_readlane_b32 s86, v221, 16\n"
      "v_pk_add_f32 v[128:129], v[48:49], s[84:85] op_sel:[0,0] op_sel_hi:[1,0]\n"
      "v_pk_add_f32 v[130:131], v[50:51], s[84:85] op_sel:[0,0] op_sel_hi:[1,0]\n"
      "v_pk_add_f32 v[132:133], v[52:53], s[84:85] op_sel:[0,0] op_sel_hi:[1,0]\n"
      "v_pk_add_f32 v[134:135], v[54:55], s[84:85] op_sel:[0,0] op_sel_hi:[1,0]\n"
      "v_pk_mul_f32 v[128:129], v[128:129], v[128:129]\n"
      "v_pk_mul_f32 v[130:131], v[130:131], v[130:131]\n"
      "v_pk_fma_f32 v[128:129], v[132:133], v[132:133], v[128:129]\n"
      "v_pk_fma_f32 v[130:131], v[134:135], v[134:135], v[130:131]\n"
      "v_pk_add_f32 v[128:129], v[128:129], v[130:131]\n"
      "v_add_f32 v182, v128, v129\n"
      "v_readlane_b32 s84, v218, 32\n"
      "v_pk_add_f32 v[128:129], v[56:57], s[86:87] op_sel:[0,0] op_sel_hi:[1,0]\n"
      "v_pk_add_f32 v[130:131], v[58:59], s[86:87] op_sel:[0,0] op_sel_hi:[1,0]\n"
      "v_pk_add_f32 v[132:133], v[60:61], s[86:87] op_sel:[0,0] op_sel_hi:[1,0]\n"
      "v_pk_add_f32 v[134:135], v[62:63], s[86:87] op_sel:[0,0] op_sel_hi:[1,0]\n"
      "v_pk_mul_f32 v[128:129], v[128:129], v[128:129]\n"
      "v_pk_mul_f32 v[130:131], v[130:131], v[130:131]\n"
      "v_pk_fma_f32 v[128:129], v[132:133], v[132:133], v[128:129]\n"
      "v_pk_fma_f32 v[130:131], v[134:135], v[134:135], v[130:131]\n"
      "v_pk_add_f32 v[128:129], v[128:129], v[130:131]\n"
      "v_add_f32 v183, v128, v129\n"
      "v_readlane_b32 s86, v219, 32\n"
      "v_pk_add_f32 v[128:129], v[64:65], s[84:85] op_sel:[0,0] op_sel_hi:[1,0]\n"
      "v_pk_add_f32 v[130:131], v[66:67], s[84:85] op_sel:[0,0] op_sel_hi:[1,0]\n"
      "v_pk_add_f32 v[132:133], v[68:69], s[84:85] op_sel:[0,0] op_sel_hi:[1,0]\n"
      "v_pk_add_f32 v[134:135], v[70:71], s[84:85] op_sel:[0,0] op_sel_hi:[1,0]\n"
      "v_pk_mul_f32 v[128:129], v[128:129], v[128:129]\n"
      "v_pk_mul_f32 v[130:131], v[130:131], v[130:131]\n"
      "v_pk_fma_f32 v[128:129], v[132:133], v[132:133], v[128:129]\n"
      "v_pk_fma_f32 v[130:131], v[134:135], v[134:135], v[130:131]\n"
      "v_pk_add_f32 v[128:129], v[128:129], v[130:131]\n"
      "v_add_f32 v184, v128, v129\n"
      "v_readlane_b32 s84, v220, 32\n"
      "v_pk_add_f32 v[128:129], v[72:73], s[86:87] op_sel:[0,0] op_sel_hi:[1,0]\n"
      "v_pk_add_f32 v[130:131], v[74:75], s[86:87] op_sel:[0,0] op_sel_hi:[1,0]\n"
      "v_pk_add_f32 v[132:133], v[76:77], s[86:87] op_sel:[0,0] op_sel_hi:[1,0]\n"
      "v_pk_add_f32 v[134:135], v[78:79], s[86:87] op_sel:[0,0] op_sel_hi:[1,0]\n"
      "v_pk_mul_f32 v[128:129], v[128:129], v[128:129]\n"
      "v_pk_mul_f32 v[130:131], v[130:131], v[130:131]\n"
      "v_pk_fma_f32 v[128:129], v[132:133], v[132:133], v[128:129]\n"
      "v_pk_fma_f32 v[130:131], v[134:135], v[134:135], v[130:131]\n"
      "v_pk_add_f32 v[128:129], v[128:129], v[130:131]\n"
      "v_add_f32 v185, v128, v129\n"
      "v_readlane_b32 s86, v221, 32\n"
      "v_pk_add_f32 v[128:129], v[80:81], s[84:85] op_sel:[0,0] op_sel_hi:[1,0]\n"
      "v_pk_add_f32 v[130:131], v[82:83], s[84:85] op_sel:[0,0] op_sel_hi:[1,0]\n"
      "v_pk_add_f32 v[132:133], v[84:85], s[84:85] op_sel:[0,0] op_sel_hi:[1,0]\n"
      "v_pk_add_f32 v[134:135], v[86:87], s[84:85] op_sel:[0,0] op_sel_hi:[1,0]\n"
      "v_pk_mul_f32 v[128:129], v[128:129], v[128:129]\n"
      "v_pk_mul_f32 v[130:131], v[130:131], v[130:131]\n"
      "v_pk_fma_f32 v[128:129], v[132:133], v[132:133], v[128:129]\n"
      "v_pk_fma_f32 v[130:131], v[134:135], v[134:135], v[130:131]\n"
      "v_pk_add_f32 v[128:129], v[128:129], v[130:131]\n"
      "v_add_f32 v186, v128, v129\n"
      "v_readlane_b32 s84, v218, 48\n"
      "v_pk_add_f32 v[128:129], v[88:89], s[86:87] op_sel:[0,0] op_sel_hi:[1,0]\n"
      "v_pk_add_f32 v[130:131], v[90:91], s[86:87] op_sel:[0,0] op_sel_hi:[1,0]\n"
      "v_pk_add_f32 v[132:133], v[92:93], s[86:87] op_sel:[0,0] op_sel_hi:[1,0]\n"
      "v_pk_add_f32 v[134:135], v[94:95], s[86:87] op_sel:[0,0] op_sel_hi:[1,0]\n"
      "v_pk_mul_f32 v[128:129], v[128:129], v[128:129]\n"
      "v_pk_mul_f32 v[130:131], v[130:131], v[130:131]\n"
      "v_pk_fma_f32 v[128:129], v[132:133], v[132:133], v[128:129]\n"
      "v_pk_fma_f32 v[130:131], v[134:135], v[134:135], v[130:131]\n"
      "v_pk_add_f32 v[128:129], v[128:129], v[130:131]\n"
      "v_add_f32 v187, v128, v129\n"
      "v_readlane_b32 s86, v219, 48\n"
      "v_pk_add_f32 v[128:129], v[96:97], s[84:85] op_sel:[0,0] op_sel_hi:[1,0]\n"
      "v_pk_add_f32 v[130:131], v[98:99], s[84:85] op_sel:[0,0] op_sel_hi:[1,0]\n"
      "v_pk_add_f32 v[132:133], v[100:101], s[84:85] op_sel:[0,0] op_sel_hi:[1,0]\n"
      "v_pk_add_f32 v[134:135], v[102:103], s[84:85] op_sel:[0,0] op_sel_hi:[1,0]\n"
      "v_pk_mul_f32 v[128:129], v[128:129], v[128:129]\n"
      "v_pk_mul_f32 v[130:131], v[130:131], v[130:131]\n"
      "v_pk_fma_f32 v[128:129], v[132:133], v[132:133], v[128:129]\n"
      "v_pk_fma_f32 v[130:131], v[134:135], v[134:135], v[130:131]\n"
      "v_pk_add_f32 v[128:129], v[128:129], v[130:131]\n"
      "v_add_f32 v188, v128, v129\n"
      "v_readlane_b32 s84, v220, 48\n"
      "v_pk_add_f32 v[128:129], v[104:105], s[86:87] op_sel:[0,0] op_sel_hi:[1,0]\n"
      "v_pk_add_f32 v[130:131], v[106:107], s[86:87] op_sel:[0,0] op_sel_hi:[1,0]\n"
      "v_pk_add_f32 v[132:133], v[108:109], s[86:87] op_sel:[0,0] op_sel_hi:[1,0]\n"
      "v_pk_add_f32 v[134:135], v[110:111], s[86:87] op_sel:[0,0] op_sel_hi:[1,0]\n"
      "v_pk_mul_f32 v[128:129], v[128:129], v[128:129]\n"
      "v_pk_mul_f32 v[130:131], v[130:131], v[130:131]\n"
      "v_pk_fma_f32 v[128:129], v[132:133], v[132:133], v[128:129]\n"
      "v_pk_fma_f32 v[130:131], v[134:135], v[134:135], v[130:131]\n"
      "v_pk_add_f32 v[128:129], v[128:129], v[130:131]\n"
      "v_add_f32 v189, v128, v129\n"
      "v_readlane_b32 s86, v221, 48\n"
      "v_pk_add_f32 v[128:129], v[112:113], s[84:85] op_sel:[0,0] op_sel_hi:[1,0]\n"
      "v_pk_add_f32 v[130:131], v[114:115], s[84:85] op_sel:[0,0] op_sel_hi:[1,0]\n"
      "v_pk_add_f32 v[132:133], v[116:117], s[84:85] op_sel:[0,0] op_sel_hi:[1,0]\n"
      "v_pk_add_f32 v[134:135], v[118:119], s[84:85] op_sel:[0,0] op_sel_hi:[1,0]\n"
      "v_pk_mul_f32 v[128:129], v[128:129], v[128:129]\n"
      "v_pk_mul_f32 v[130:131], v[130:131], v[130:131]\n"
      "v_pk_fma_f32 v[128:129], v[132:133], v[132:133], v[128:129]\n"
      "v_pk_fma_f32 v[130:131], v[134:135], v[134:135], v[130:131]\n"
      "v_pk_add_f32 v[128:129], v[128:129], v[130:131]\n"
      "v_add_f32 v190, v128, v129\n"
      "v_pk_add_f32 v[128:129], v[120:121], s[86:87] op_sel:[0,0] op_sel_hi:[1,0]\n"
      "v_pk_add_f32 v[130:131], v[122:123], s[86:87] op_sel:[0,0] op_sel_hi:[1,0]\n"
      "v_pk_add_f32 v[132:133], v[124:125], s[86:87] op_sel:[0,0] op_sel_hi:[1,0]\n"
      "v_pk_add_f32 v[134:135], v[126:127], s[86:87] op_sel:[0,0] op_sel_hi:[1,0]\n"
      "v_pk_mul_f32 v[128:129], v[128:129], v[128:129]\n"
      "v_pk_mul_f32 v[130:131], v[130:131], v[130:131]\n"
      "v_pk_fma_f32 v[128:129], v[132:133], v[132:133], v[128:129]\n"
      "v_pk_fma_f32 v[130:131], v[134:135], v[134:135], v[130:131]\n"
      "v_pk_add_f32 v[128:129], v[128:129], v[130:131]\n"
      "v_add_f32 v191, v128, v129\n"
      "s_mov_b64 exec, -1\n"
      "s_nop 1\n"
      "v_permlane32_swap_b32 v176, v184\n"
      "v_permlane32_swap_b32 v177, v185\n"
      "v_permlane32_swap_b32 v178, v186\n"
      "v_permlane32_swap_b32 v179, v187\n"
      "v_permlane32_swap_b32 v180, v188\n"
      "v_permlane32_swap_b32 v181, v189\n"
      "v_permlane32_swap_b32 v182, v190\n"
      "v_permlane32_swap_b32 v183, v191\n"
      "v_add_f32 v176, v176, v184\n"
      "v_add_f32 v177, v177, v185\n"
      "v_add_f32 v178, v178, v186\n"
      "v_add_f32 v179, v179, v187\n"
      "v_add_f32 v180, v180, v188\n"
      "v_add_f32 v181, v181, v189\n"
      "v_add_f32 v182, v182, v190\n"
      "v_add_f32 v183, v183, v191\n"
      "v_permlane16_swap_b32 v176, v180\n"
      "v_permlane16_swap_b32 v177, v181\n"
      "v_permlane16_swap_b32 v178, v182\n"
      "v_permlane16_swap_b32 v179, v183\n"
      "v_add_f32 v176, v176, v180\n"
      "v_add_f32 v177, v177, v181\n"
      "v_add_f32 v178, v178, v182\n"
      "v_add_f32 v179, v179, v183\n"
      "v_add_f32_dpp v176, v176, v176 row_ror:8 row_mask:0xf bank_mask:0xf\n"
      "v_add_f32_dpp v177, v177, v177 row_ror:8 row_mask:0xf bank_mask:0xf\n"
      "v_add_f32_dpp v178, v178, v178 row_ror:8 row_mask:0xf bank_mask:0xf\n"
      "v_add_f32_dpp v179, v179, v179 row_ror:8 row_mask:0xf bank_mask:0xf\n"
      "v_add_f32_dpp v176, v176, v176 row_ror:4 row_mask:0xf bank_mask:0xf\n"
      "v_add_f32_dpp v177, v177, v177 row_ror:4 row_mask:0xf bank_mask:0xf\n"
      "v_add_f32_dpp v178, v178, v178 row_ror:4 row_mask:0xf bank_mask:0xf\n"
      "v_add_f32_dpp v179, v179, v179 row_ror:4 row_mask:0xf bank_mask:0xf\n"
      "v_add_f32_dpp v176, v176, v176 row_ror:2 row_mask:0xf bank_mask:0xf\n"
      "v_add_f32_dpp v177, v177, v177 row_ror:2 row_mask:0xf bank_mask:0xf\n"
      "v_add_f32_dpp v178, v178, v178 row_ror:2 row_mask:0xf bank_mask:0xf\n"
      "v_add_f32_dpp v179, v179, v179 row_ror:2 row_mask:0xf bank_mask:0xf\n"
      "v_add_f32_dpp v176, v176, v176 row_ror:1 row_mask:0xf bank_mask:0xf\n"
      "v_add_f32_dpp v177, v177, v177 row_ror:1 row_mask:0xf bank_mask:0xf\n"
      "v_add_f32_dpp v178, v178, v178 row_ror:1 row_mask:0xf bank_mask:0xf\n"
      "v_add_f32_dpp v179, v179, v179 row_ror:1 row_mask:0xf bank_mask:0xf\n"
      "v_mul_f32 v176, 0x3b124925, v176\n"
      "v_mul_f32 v177, 0x3b124925, v177\n"
      "v_mul_f32 v178, 0x3b124925, v178\n"
      "v_mul_f32 v179, 0x3b124925, v179\n"
      "v_cvt_pk_bf16_f32 v176, 0, v176\n"
      "v_cvt_pk_bf16_f32 v177, 0, v177\n"
      "v_cvt_pk_bf16_f32 v178, 0, v178\n"
      "v_cvt_pk_bf16_f32 v179, 0, v179\n"
      "v_add_f32 v176, 0x3727c5ac, v176\n"
      "v_add_f32 v177, 0x3727c5ac, v177\n"
      "v_add_f32 v178, 0x3727c5ac, v178\n"
      "v_add_f32 v179, 0x3727c5ac, v179\n"
      "v_cvt_pk_bf16_f32 v176, 0, v176\n"
      "v_cvt_pk_bf16_f32 v177, 0, v177\n"
      "v_cvt_pk_bf16_f32 v178, 0, v178\n"
      "v_cvt_pk_bf16_f32 v179, 0, v179\n"
      "v_sqrt_f32 v176, v176\n"
      "v_sqrt_f32 v177, v177\n"
      "v_sqrt_f32 v178, v178\n"
      "v_sqrt_f32 v179, v179\n"
      "v_cvt_pk_bf16_f32 v218, 0, v218\n"
      "v_cvt_pk_bf16_f32 v219, 0, v219\n"
      "v_cvt_pk_bf16_f32 v220, 0, v220\n"
      "v_cvt_pk_bf16_f32 v221, 0, v221\n"
      "v_cvt_pk_bf16_f32 v176, 0, v176\n"
      "v_cvt_pk_bf16_f32 v177, 0, v177\n"
      "v_cvt_pk_bf16_f32 v178, 0, v178\n"
      "v_cvt_pk_bf16_f32 v179, 0, v179\n"
      "v_rcp_f32 v176, v176\n"
      "v_rcp_f32 v177, v177\n"
      "v_rcp_f32 v178, v178\n"
      "v_rcp_f32 v179, v179\n"
      "s_lshr_b32 s84, s92, 3\n"
      "s_lshl_b32 s84, s84, 3\n"
      "s_add_u32 s84, s84, 0x48\n"
      "s_load_dwordx2 s[72:73], s[34:35], s84\n"
      "s_nop 0\n"
      "v_readlane_b32 s84, v218, 0\n"
      "v_readlane_b32 s85, v176, 0\n"
      "v_readlane_b32 s86, v219, 0\n"
      "v_readlane_b32 s87, v177, 0\n"
      "v_pk_add_f32 v[0:1], v[0:1], s[84:85] op_sel:[0,0] op_sel_hi:[1,0]\n"
      "v_pk_add_f32 v[2:3], v[2:3], s[84:85] op_sel:[0,0] op_sel_hi:[1,0]\n"
      "v_pk_add_f32 v[4:5], v[4:5], s[84:85] op_sel:[0,0] op_sel_hi:[1,0]\n"
      "v_pk_add_f32 v[6:7], v[6:7], s[84:85] op_sel:[0,0] op_sel_hi:[1,0]\n"
      "v_cvt_pk_bf16_f32 v0, 0, v0\n"
      "v_cvt_pk_bf16_f32 v1, 0, v1\n"
      "v_cvt_pk_bf16_f32 v2, 0, v2\n"
      "v_cvt_pk_bf16_f32 v3, 0, v3\n"
      "v_cvt_pk_bf16_f32 v4, 0, v4\n"
      "v_cvt_pk_bf16_f32 v5, 0, v5\n"
      "v_cvt_pk_bf16_f32 v6, 0, v6\n"
      "v_cvt_pk_bf16_f32 v7, 0, v7\n"
      "s_add_u32 m0, s81, 98304\n"
      "s_nop 0\n"
      "buffer_load_dwordx4 v208, s[64:67], s91 offen offset:0 lds\n"
      "v_pk_mul_f32 v[0:1], v[0:1], s[84:85] op_sel:[0,1] op_sel_hi:[1,1]\n"
      "v_pk_mul_f32 v[2:3], v[2:3], s[84:85] op_sel:[0,1] op_sel_hi:[1,1]\n"
      "v_pk_mul_f32 v[4:5], v[4:5], s[84:85] op_sel:[0,1] op_sel_hi:[1,1]\n"
      "v_pk_mul_f32 v[6:7], v[6:7], s[84:85] op_sel:[0,1] op_sel_hi:[1,1]\n"
      "v_cvt_pk_bf16_f32 v0, 0, v0\n"
      "v_cvt_pk_bf16_f32 v1, 0, v1\n"
      "v_cvt_pk_bf16_f32 v2, 0, v2\n"
      "v_cvt_pk_bf16_f32 v3, 0, v3\n"
      "v_cvt_pk_bf16_f32 v4, 0, v4\n"
      "v_cvt_pk_bf16_f32 v5, 0, v5\n"
      "v_cvt_pk_bf16_f32 v6, 0, v6\n"
      "v_cvt_pk_bf16_f32 v7, 0, v7\n"
      "v_readlane_b32 s84, v220, 0\n"
      "v_readlane_b32 s85, v178, 0\n"
      "v_pk_add_f32 v[8:9], v[8:9], s[86:87] op_sel:[0,0] op_sel_hi:[1,0]\n"
      "v_pk_add_f32 v[10:11], v[10:11], s[86:87] op_sel:[0,0] op_sel_hi:[1,0]\n"
      "v_pk_add_f32 v[12:13], v[12:13], s[86:87] op_sel:[0,0] op_sel_hi:[1,0]\n"
      "v_pk_add_f32 v[14:15], v[14:15], s[86:87] op_sel:[0,0] op_sel_hi:[1,0]\n"
      "v_cvt_pk_bf16_f32 v8, 0, v8\n"
      "v_cvt_pk_bf16_f32 v9, 0, v9\n"
      "v_cvt_pk_bf16_f32 v10, 0, v10\n"
      "v_cvt_pk_bf16_f32 v11, 0, v11\n"
      "v_cvt_pk_bf16_f32 v12, 0, v12\n"
      "v_cvt_pk_bf16_f32 v13, 0, v13\n"
      "v_cvt_pk_bf16_f32 v14, 0, v14\n"
      "v_cvt_pk_bf16_f32 v15, 0, v15\n"
      "buffer_load_dwordx4 v209, s[64:67], s91 offen offset:1024 lds\n"
      "v_pk_mul_f32 v[8:9], v[8:9], s[86:87] op_sel:[0,1] op_sel_hi:[1,1]\n"
      "v_pk_mul_f32 v[10:11], v[10:11], s[86:87] op_sel:[0,1] op_sel_hi:[1,1]\n"
      "v_pk_mul_f32 v[12:13], v[12:13], s[86:87] op_sel:[0,1] op_sel_hi:[1,1]\n"
      "v_pk_mul_f32 v[14:15], v[14:15], s[86:87] op_sel:[0,1] op_sel_hi:[1,1]\n"
      "v_cvt_pk_bf16_f32 v8, 0, v8\n"
      "v_cvt_pk_bf16_f32 v9, 0, v9\n"
      "v_cvt_pk_bf16_f32 v10, 0, v10\n"
      "v_cvt_pk_bf16_f32 v11, 0, v11\n"
      "v_cvt_pk_bf16_f32 v12, 0, v12\n"
      "v_cvt_pk_bf16_f32 v13, 0, v13\n"
      "v_cvt_pk_bf16_f32 v14, 0, v14\n"
      "v_cvt_pk_bf16_f32 v15, 0, v15\n"
      "v_readlane_b32 s86, v221, 0\n"
      "v_readlane_b32 s87, v179, 0\n"
      "v_pk_add_f32 v[16:17], v[16:17], s[84:85] op_sel:[0,0] op_sel_hi:[1,0]\n"
      "v_pk_add_f32 v[18:19], v[18:19], s[84:85] op_sel:[0,0] op_sel_hi:[1,0]\n"
      "v_pk_add_f32 v[20:21], v[20:21], s[84:85] op_sel:[0,0] op_sel_hi:[1,0]\n"
      "v_pk_add_f32 v[22:23], v[22:23], s[84:85] op_sel:[0,0] op_sel_hi:[1,0]\n"
      "v_cvt_pk_bf16_f32 v16, 0, v16\n"
      "v_cvt_pk_bf16_f32 v17, 0, v17\n"
      "v_cvt_pk_bf16_f32 v18, 0, v18\n"
      "v_cvt_pk_bf16_f32 v19, 0, v19\n"
      "v_cvt_pk_bf16_f32 v20, 0, v20\n"
      "v_cvt_pk_bf16_f32 v21, 0, v21\n"
      "v_cvt_pk_bf16_f32 v22, 0, v22\n"
      "v_cvt_pk_bf16_f32 v23, 0, v23\n"
      "buffer_load_dwordx4 v210, s[64:67], s91 offen offset:2048 lds\n"
      "v_pk_mul_f32 v[16:17], v[16:17], s[84:85] op_sel:[0,1] op_sel_hi:[1,1]\n"
      "v_pk_mul_f32 v[18:19], v[18:19], s[84:85] op_sel:[0,1] op_sel_hi:[1,1]\n"
      "v_pk_mul_f32 v[20:21], v[20:21], s[84:85] op_sel:[0,1] op_sel_hi:[1,1]\n"
      "v_pk_mul_f32 v[22:23], v[22:23], s[84:85] op_sel:[0,1] op_sel_hi:[1,1]\n"
      "v_cvt_pk_bf16_f32 v16, 0, v16\n"
      "v_cvt_pk_bf16_f32 v17, 0, v17\n"
      "v_cvt_pk_bf16_f32 v18, 0, v18\n"
      "v_cvt_pk_bf16_f32 v19, 0, v19\n"
      "v_cvt_pk_bf16_f32 v20, 0, v20\n"
      "v_cvt_pk_bf16_f32 v21, 0, v21\n"
      "v_cvt_pk_bf16_f32 v22, 0, v22\n"
      "v_cvt_pk_bf16_f32 v23, 0, v23\n"
      "v_readlane_b32 s84, v218, 16\n"
      "v_readlane_b32 s85, v176, 16\n"
      "v_pk_add_f32 v[24:25], v[24:25], s[86:87] op_sel:[0,0] op_sel_hi:[1,0]\n"
      "v_pk_add_f32 v[26:27], v[26:27], s[86:87] op_sel:[0,0] op_sel_hi:[1,0]\n"
      "v_pk_add_f32 v[28:29], v[28:29], s[86:87] op_sel:[0,0] op_sel_hi:[1,0]\n"
      "v_pk_add_f32 v[30:31], v[30:31], s[86:87] op_sel:[0,0] op_sel_hi:[1,0]\n"
      "v_cvt_pk_bf16_f32 v24, 0, v24\n"
      "v_cvt_pk_bf16_f32 v25, 0, v25\n"
      "v_cvt_pk_bf16_f32 v26, 0, v26\n"
      "v_cvt_pk_bf16_f32 v27, 0, v27\n"
      "v_cvt_pk_bf16_f32 v28, 0, v28\n"
      "v_cvt_pk_bf16_f32 v29, 0, v29\n"
      "v_cvt_pk_bf16_f32 v30, 0, v30\n"
      "v_cvt_pk_bf16_f32 v31, 0, v31\n"
      "buffer_load_dwordx4 v211, s[64:67], s91 offen offset:3072 lds\n"
      "v_pk_mul_f32 v[24:25], v[24:25], s[86:87] op_sel:[0,1] op_sel_hi:[1,1]\n"
      "v_pk_mul_f32 v[26:27], v[26:27], s[86:87] op_sel:[0,1] op_sel_hi:[1,1]\n"
      "v_pk_mul_f32 v[28:29], v[28:29], s[86:87] op_sel:[0,1] op_sel_hi:[1,1]\n"
      "v_pk_mul_f32 v[30:31], v[30:31], s[86:87] op_sel:[0,1] op_sel_hi:[1,1]\n"
      "v_cvt_pk_bf16_f32 v24, 0, v24\n"
      "v_cvt_pk_bf16_f32 v25, 0, v25\n"
      "v_cvt_pk_bf16_f32 v26, 0, v26\n"
      "v_cvt_pk_bf16_f32 v27, 0, v27\n"
      "v_cvt_pk_bf16_f32 v28, 0, v28\n"
      "v_cvt_pk_bf16_f32 v29, 0, v29\n"
      "v_cvt_pk_bf16_f32 v30, 0, v30\n"
      "v_cvt_pk_bf16_f32 v31, 0, v31\n"
      "v_readlane_b32 s86, v219, 16\n"
      "v_readlane_b32 s87, v177, 16\n"
      "v_pk_add_f32 v[32:33], v[32:33], s[84:85] op_sel:[0,0] op_sel_hi:[1,0]\n"
      "v_pk_add_f32 v[34:35], v[34:35], s[84:85] op_sel:[0,0] op_sel_hi:[1,0]\n"
      "v_pk_add_f32 v[36:37], v[36:37], s[84:85] op_sel:[0,0] op_sel_hi:[1,0]\n"
      "v_pk_add_f32 v[38:39], v[38:39], s[84:85] op_sel:[0,0] op_sel_hi:[1,0]\n"
      "v_cvt_pk_bf16_f32 v32, 0, v32\n"
      "v_cvt_pk_bf16_f32 v33, 0, v33\n"
      "v_cvt_pk_bf16_f32 v34, 0, v34\n"
      "v_cvt_pk_bf16_f32 v35, 0, v35\n"
      "v_cvt_pk_bf16_f32 v36, 0, v36\n"
      "v_cvt_pk_bf16_f32 v37, 0, v37\n"
      "v_cvt_pk_bf16_f32 v38, 0, v38\n"
      "v_cvt_pk_bf16_f32 v39, 0, v39\n"
      "s_add_u32 m0, s81, 102400\n"
      "s_nop 0\n"
      "buffer_load_dwordx4 v212, s[64:67], s91 offen offset:0 lds\n"
      "v_pk_mul_f32 v[32:33], v[32:33], s[84:85] op_sel:[0,1] op_sel_hi:[1,1]\n"
      "v_pk_mul_f32 v[34:35], v[34:35], s[84:85] op_sel:[0,1] op_sel_hi:[1,1]\n"
      "v_pk_mul_f32 v[36:37], v[36:37], s[84:85] op_sel:[0,1] op_sel_hi:[1,1]\n"
      "v_pk_mul_f32 v[38:39], v[38:39], s[84:85] op_sel:[0,1] op_sel_hi:[1,1]\n"
      "v_cvt_pk_bf16_f32 v32, 0, v32\n"
      "v_cvt_pk_bf16_f32 v33, 0, v33\n"
      "v_cvt_pk_bf16_f32 v34, 0, v34\n"
      "v_cvt_pk_bf16_f32 v35, 0, v35\n"
      "v_cvt_pk_bf16_f32 v36, 0, v36\n"
      "v_cvt_pk_bf16_f32 v37, 0, v37\n"
      "v_cvt_pk_bf16_f32 v38, 0, v38\n"
      "v_cvt_pk_bf16_f32 v39, 0, v39\n"
      "v_readlane_b32 s84, v220, 16\n"
      "v_readlane_b32 s85, v178, 16\n"
      "v_pk_add_f32 v[40:41], v[40:41], s[86:87] op_sel:[0,0] op_sel_hi:[1,0]\n"
      "v_pk_add_f32 v[42:43], v[42:43], s[86:87] op_sel:[0,0] op_sel_hi:[1,0]\n"
      "v_pk_add_f32 v[44:45], v[44:45], s[86:87] op_sel:[0,0] op_sel_hi:[1,0]\n"
      "v_pk_add_f32 v[46:47], v[46:47], s[86:87] op_sel:[0,0] op_sel_hi:[1,0]\n"
      "v_cvt_pk_bf16_f32 v40, 0, v40\n"
      "v_cvt_pk_bf16_f32 v41, 0, v41\n"
      "v_cvt_pk_bf16_f32 v42, 0, v42\n"
      "v_cvt_pk_bf16_f32 v43, 0, v43\n"
      "v_cvt_pk_bf16_f32 v44, 0, v44\n"
      "v_cvt_pk_bf16_f32 v45, 0, v45\n"
      "v_cvt_pk_bf16_f32 v46, 0, v46\n"
      "v_cvt_pk_bf16_f32 v47, 0, v47\n"
      "buffer_load_dwordx4 v213, s[64:67], s91 offen offset:1024 lds\n"
      "v_pk_mul_f32 v[40:41], v[40:41], s[86:87] op_sel:[0,1] op_sel_hi:[1,1]\n"
      "v_pk_mul_f32 v[42:43], v[42:43], s[86:87] op_sel:[0,1] op_sel_hi:[1,1]\n"
      "v_pk_mul_f32 v[44:45], v[44:45], s[86:87] op_sel:[0,1] op_sel_hi:[1,1]\n"
      "v_pk_mul_f32 v[46:47], v[46:47], s[86:87] op_sel:[0,1] op_sel_hi:[1,1]\n"
      "v_cvt_pk_bf16_f32 v40, 0, v40\n"
      "v_cvt_pk_bf16_f32 v41, 0, v41\n"
      "v_cvt_pk_bf16_f32 v42, 0, v42\n"
      "v_cvt_pk_bf16_f32 v43, 0, v43\n"
      "v_cvt_pk_bf16_f32 v44, 0, v44\n"
      "v_cvt_pk_bf16_f32 v45, 0, v45\n"
      "v_cvt_pk_bf16_f32 v46, 0, v46\n"
      "v_cvt_pk_bf16_f32 v47, 0, v47\n"
      "v_readlane_b32 s86, v221, 16\n"
      "v_readlane_b32 s87, v179, 16\n"
      "v_pk_add_f32 v[48:49], v[48:49], s[84:85] op_sel:[0,0] op_sel_hi:[1,0]\n"
      "v_pk_add_f32 v[50:51], v[50:51], s[84:85] op_sel:[0,0] op_sel_hi:[1,0]\n"
      "v_pk_add_f32 v[52:53], v[52:53], s[84:85] op_sel:[0,0] op_sel_hi:[1,0]\n"
      "v_pk_add_f32 v[54:55], v[54:55], s[84:85] op_sel:[0,0] op_sel_hi:[1,0]\n"
      "v_cvt_pk_bf16_f32 v48, 0, v48\n"
      "v_cvt_pk_bf16_f32 v49, 0, v49\n"
      "v_cvt_pk_bf16_f32 v50, 0, v50\n"
      "v_cvt_pk_bf16_f32 v51, 0, v51\n"
      "v_cvt_pk_bf16_f32 v52, 0, v52\n"
      "v_cvt_pk_bf16_f32 v53, 0, v53\n"
      "v_cvt_pk_bf16_f32 v54, 0, v54\n"
      "v_cvt_pk_bf16_f32 v55, 0, v55\n"
      "buffer_load_dwordx4 v214, s[64:67], s91 offen offset:2048 lds\n"
      "v_pk_mul_f32 v[48:49], v[48:49], s[84:85] op_sel:[0,1] op_sel_hi:[1,1]\n"
      "v_pk_mul_f32 v[50:51], v[50:51], s[84:85] op_sel:[0,1] op_sel_hi:[1,1]\n"
      "v_pk_mul_f32 v[52:53], v[52:53], s[84:85] op_sel:[0,1] op_sel_hi:[1,1]\n"
      "v_pk_mul_f32 v[54:55], v[54:55], s[84:85] op_sel:[0,1] op_sel_hi:[1,1]\n"
      "v_cvt_pk_bf16_f32 v48, 0, v48\n"
      "v_cvt_pk_bf16_f32 v49, 0, v49\n"
      "v_cvt_pk_bf16_f32 v50, 0, v50\n"
      "v_cvt_pk_bf16_f32 v51, 0, v51\n"
      "v_cvt_pk_bf16_f32 v52, 0, v52\n"
      "v_cvt_pk_bf16_f32 v53, 0, v53\n"
      "v_cvt_pk_bf16_f32 v54, 0, v54\n"
      "v_cvt_pk_bf16_f32 v55, 0, v55\n"
      "v_readlane_b32 s84, v218, 32\n"
      "v_readlane_b32 s85, v176, 32\n"
      "v_pk_add_f32 v[56:57], v[56:57], s[86:87] op_sel:[0,0] op_sel_hi:[1,0]\n"
      "v_pk_add_f32 v[58:59], v[58:59], s[86:87] op_sel:[0,0] op_sel_hi:[1,0]\n"
      "v_pk_add_f32 v[60:61], v[60:61], s[86:87] op_sel:[0,0] op_sel_hi:[1,0]\n"
      "v_pk_add_f32 v[62:63], v[62:63], s[86:87] op_sel:[0,0] op_sel_hi:[1,0]\n"
      "v_cvt_pk_bf16_f32 v56, 0, v56\n"
      "v_cvt_pk_bf16_f32 v57, 0, v57\n"
      "v_cvt_pk_bf16_f32 v58, 0, v58\n"
      "v_cvt_pk_bf16_f32 v59, 0, v59\n"
      "v_cvt_pk_bf16_f32 v60, 0, v60\n"
      "v_cvt_pk_bf16_f32 v61, 0, v61\n"
      "v_cvt_pk_bf16_f32 v62, 0, v62\n"
      "v_cvt_pk_bf16_f32 v63, 0, v63\n"
      "buffer_load_dwordx4 v215, s[64:67], s91 offen offset:3072 lds\n"
      "s_add_u32 s91, s91, 262144\n"
      "s_cmp_ge_u32 s91, 3670016\n"
      "s_cselect_b32 s91, 0, s91\n"
      "v_pk_mul_f32 v[56:57], v[56:57], s[86:87] op_sel:[0,1] op_sel_hi:[1,1]\n"
      "v_pk_mul_f32 v[58:59], v[58:59], s[86:87] op_sel:[0,1] op_sel_hi:[1,1]\n"
      "v_pk_mul_f32 v[60:61], v[60:61], s[86:87] op_sel:[0,1] op_sel_hi:[1,1]\n"
      "v_pk_mul_f32 v[62:63], v[62:63], s[86:87] op_sel:[0,1] op_sel_hi:[1,1]\n"
      "v_cvt_pk_bf16_f32 v56, 0, v56\n"
      "v_cvt_pk_bf16_f32 v57, 0, v57\n"
      "v_cvt_pk_bf16_f32 v58, 0, v58\n"
      "v_cvt_pk_bf16_f32 v59, 0, v59\n"
      "v_cvt_pk_bf16_f32 v60, 0, v60\n"
      "v_cvt_pk_bf16_f32 v61, 0, v61\n"
      "v_cvt_pk_bf16_f32 v62, 0, v62\n"
      "v_cvt_pk_bf16_f32 v63, 0, v63\n"
      "v_readlane_b32 s86, v219, 32\n"
      "v_readlane_b32 s87, v177, 32\n"
      "buffer_load_dwordx4 v[144:147], v240, s[76:79], 0 offen offset:0 sc1\n"
      "buffer_load_dwordx4 v[148:151], v240, s[76:79], 0 offen offset:16 sc1\n"
      "buffer_load_dwordx4 v[152:155], v240, s[76:79], 0 offen offset:1792 sc1\n"
      "buffer_load_dwordx4 v[156:159], v240, s[76:79], 0 offen offset:1808 sc1\n"
      "v_pk_add_f32 v[64:65], v[64:65], s[84:85] op_sel:[0,0] op_sel_hi:[1,0]\n"
      "v_pk_add_f32 v[66:67], v[66:67], s[84:85] op_sel:[0,0] op_sel_hi:[1,0]\n"
      "v_pk_add_f32 v[68:69], v[68:69], s[84:85] op_sel:[0,0] op_sel_hi:[1,0]\n"
      "v_pk_add_f32 v[70:71], v[70:71], s[84:85] op_sel:[0,0] op_sel_hi:[1,0]\n"
      "v_cvt_pk_bf16_f32 v64, 0, v64\n"
      "v_cvt_pk_bf16_f32 v65, 0, v65\n"
      "v_cvt_pk_bf16_f32 v66, 0, v66\n"
      "v_cvt_pk_bf16_f32 v67, 0, v67\n"
      "v_cvt_pk_bf16_f32 v68, 0, v68\n"
      "v_cvt_pk_bf16_f32 v69, 0, v69\n"
      "v_cvt_pk_bf16_f32 v70, 0, v70\n"
      "v_cvt_pk_bf16_f32 v71, 0, v71\n"
      "v_pk_mul_f32 v[64:65], v[64:65], s[84:85] op_sel:[0,1] op_sel_hi:[1,1]\n"
      "v_pk_mul_f32 v[66:67], v[66:67], s[84:85] op_sel:[0,1] op_sel_hi:[1,1]\n"
      "v_pk_mul_f32 v[68:69], v[68:69], s[84:85] op_sel:[0,1] op_sel_hi:[1,1]\n"
      "v_pk_mul_f32 v[70:71], v[70:71], s[84:85] op_sel:[0,1] op_sel_hi:[1,1]\n"
      "v_cvt_pk_bf16_f32 v64, 0, v64\n"
      "v_cvt_pk_bf16_f32 v65, 0, v65\n"
      "v_cvt_pk_bf16_f32 v66, 0, v66\n"
      "v_cvt_pk_bf16_f32 v67, 0, v67\n"
      "v_cvt_pk_bf16_f32 v68, 0, v68\n"
      "v_cvt_pk_bf16_f32 v69, 0, v69\n"
      "v_cvt_pk_bf16_f32 v70, 0, v70\n"
      "v_cvt_pk_bf16_f32 v71, 0, v71\n"
      "v_readlane_b32 s84, v220, 32\n"
      "v_readlane_b32 s85, v178, 32\n"
      "v_pk_add_f32 v[72:73], v[72:73], s[86:87] op_sel:[0,0] op_sel_hi:[1,0]\n"
      "v_pk_add_f32 v[74:75], v[74:75], s[86:87] op_sel:[0,0] op_sel_hi:[1,0]\n"
      "v_pk_add_f32 v[76:77], v[76:77], s[86:87] op_sel:[0,0] op_sel_hi:[1,0]\n"
      "v_pk_add_f32 v[78:79], v[78:79], s[86:87] op_sel:[0,0] op_sel_hi:[1,0]\n"
      "v_cvt_pk_bf16_f32 v72, 0, v72\n"
      "v_cvt_pk_bf16_f32 v73, 0, v73\n"
      "v_cvt_pk_bf16_f32 v74, 0, v74\n"
      "v_cvt_pk_bf16_f32 v75, 0, v75\n"
      "v_cvt_pk_bf16_f32 v76, 0, v76\n"
      "v_cvt_pk_bf16_f32 v77, 0, v77\n"
      "v_cvt_pk_bf16_f32 v78, 0, v78\n"
      "v_cvt_pk_bf16_f32 v79, 0, v79\n"
      "v_pk_mul_f32 v[72:73], v[72:73], s[86:87] op_sel:[0,1] op_sel_hi:[1,1]\n"
      "v_pk_mul_f32 v[74:75], v[74:75], s[86:87] op_sel:[0,1] op_sel_hi:[1,1]\n"
      "v_pk_mul_f32 v[76:77], v[76:77], s[86:87] op_sel:[0,1] op_sel_hi:[1,1]\n"
      "v_pk_mul_f32 v[78:79], v[78:79], s[86:87] op_sel:[0,1] op_sel_hi:[1,1]\n"
      "v_cvt_pk_bf16_f32 v72, 0, v72\n"
      "v_cvt_pk_bf16_f32 v73, 0, v73\n"
      "v_cvt_pk_bf16_f32 v74, 0, v74\n"
      "v_cvt_pk_bf16_f32 v75, 0, v75\n"
      "v_cvt_pk_bf16_f32 v76, 0, v76\n"
      "v_cvt_pk_bf16_f32 v77, 0, v77\n"
      "v_cvt_pk_bf16_f32 v78, 0, v78\n"
      "v_cvt_pk_bf16_f32 v79, 0, v79\n"
      "v_readlane_b32 s86, v221, 32\n"
      "v_readlane_b32 s87, v179, 32\n"
      "v_pk_add_f32 v[80:81], v[80:81], s[84:85] op_sel:[0,0] op_sel_hi:[1,0]\n"
      "v_pk_add_f32 v[82:83], v[82:83], s[84:85] op_sel:[0,0] op_sel_hi:[1,0]\n"
      "v_pk_add_f32 v[84:85], v[84:85], s[84:85] op_sel:[0,0] op_sel_hi:[1,0]\n"
      "v_pk_add_f32 v[86:87], v[86:87], s[84:85] op_sel:[0,0] op_sel_hi:[1,0]\n"
      "v_cvt_pk_bf16_f32 v80, 0, v80\n"
      "v_cvt_pk_bf16_f32 v81, 0, v81\n"
      "v_cvt_pk_bf16_f32 v82, 0, v82\n"
      "v_cvt_pk_bf16_f32 v83, 0, v83\n"
      "v_cvt_pk_bf16_f32 v84, 0, v84\n"
      "v_cvt_pk_bf16_f32 v85, 0, v85\n"
      "v_cvt_pk_bf16_f32 v86, 0, v86\n"
      "v_cvt_pk_bf16_f32 v87, 0, v87\n"
      "v_pk_mul_f32 v[80:81], v[80:81], s[84:85] op_sel:[0,1] op_sel_hi:[1,1]\n"
      "v_pk_mul_f32 v[82:83], v[82:83], s[84:85] op_sel:[0,1] op_sel_hi:[1,1]\n"
      "v_pk_mul_f32 v[84:85], v[84:85], s[84:85] op_sel:[0,1] op_sel_hi:[1,1]\n"
      "v_pk_mul_f32 v[86:87], v[86:87], s[84:85] op_sel:[0,1] op_sel_hi:[1,1]\n"
      "v_cvt_pk_bf16_f32 v80, 0, v80\n"
      "v_cvt_pk_bf16_f32 v81, 0, v81\n"
      "v_cvt_pk_bf16_f32 v82, 0, v82\n"
      "v_cvt_pk_bf16_f32 v83, 0, v83\n"
      "v_cvt_pk_bf16_f32 v84, 0, v84\n"
      "v_cvt_pk_bf16_f32 v85, 0, v85\n"
      "v_cvt_pk_bf16_f32 v86, 0, v86\n"
      "v_cvt_pk_bf16_f32 v87, 0, v87\n"
      "v_readlane_b32 s84, v218, 48\n"
      "v_readlane_b32 s85, v176, 48\n"
      "v_pk_add_f32 v[88:89], v[88:89], s[86:87] op_sel:[0,0] op_sel_hi:[1,0]\n"
      "v_pk_add_f32 v[90:91], v[90:91], s[86:87] op_sel:[0,0] op_sel_hi:[1,0]\n"
      "v_pk_add_f32 v[92:93], v[92:93], s[86:87] op_sel:[0,0] op_sel_hi:[1,0]\n"
      "v_pk_add_f32 v[94:95], v[94:95], s[86:87] op_sel:[0,0] op_sel_hi:[1,0]\n"
      "v_cvt_pk_bf16_f32 v88, 0, v88\n"
      "v_cvt_pk_bf16_f32 v89, 0, v89\n"
      "v_cvt_pk_bf16_f32 v90, 0, v90\n"
      "v_cvt_pk_bf16_f32 v91, 0, v91\n"
      "v_cvt_pk_bf16_f32 v92, 0, v92\n"
      "v_cvt_pk_bf16_f32 v93, 0, v93\n"
      "v_cvt_pk_bf16_f32 v94, 0, v94\n"
      "v_cvt_pk_bf16_f32 v95, 0, v95\n"
      "v_pk_mul_f32 v[88:89], v[88:89], s[86:87] op_sel:[0,1] op_sel_hi:[1,1]\n"
      "v_pk_mul_f32 v[90:91], v[90:91], s[86:87] op_sel:[0,1] op_sel_hi:[1,1]\n"
      "v_pk_mul_f32 v[92:93], v[92:93], s[86:87] op_sel:[0,1] op_sel_hi:[1,1]\n"
      "v_pk_mul_f32 v[94:95], v[94:95], s[86:87] op_sel:[0,1] op_sel_hi:[1,1]\n"
      "v_cvt_pk_bf16_f32 v88, 0, v88\n"
      "v_cvt_pk_bf16_f32 v89, 0, v89\n"
      "v_cvt_pk_bf16_f32 v90, 0, v90\n"
      "v_cvt_pk_bf16_f32 v91, 0, v91\n"
      "v_cvt_pk_bf16_f32 v92, 0, v92\n"
      "v_cvt_pk_bf16_f32 v93, 0, v93\n"
      "v_cvt_pk_bf16_f32 v94, 0, v94\n"
      "v_cvt_pk_bf16_f32 v95, 0, v95\n"
      "v_readlane_b32 s86, v219, 48\n"
      "v_readlane_b32 s87, v177, 48\n"
      "v_pk_add_f32 v[96:97], v[96:97], s[84:85] op_sel:[0,0] op_sel_hi:[1,0]\n"
      "v_pk_add_f32 v[98:99], v[98:99], s[84:85] op_sel:[0,0] op_sel_hi:[1,0]\n"
      "v_pk_add_f32 v[100:101], v[100:101], s[84:85] op_sel:[0,0] op_sel_hi:[1,0]\n"
      "v_pk_add_f32 v[102:103], v[102:103], s[84:85] op_sel:[0,0] op_sel_hi:[1,0]\n"
      "v_cvt_pk_bf16_f32 v96, 0, v96\n"
      "v_cvt_pk_bf16_f32 v97, 0, v97\n"
      "v_cvt_pk_bf16_f32 v98, 0, v98\n"
      "v_cvt_pk_bf16_f32 v99, 0, v99\n"
      "v_cvt_pk_bf16_f32 v100, 0, v100\n"
      "v_cvt_pk_bf16_f32 v101, 0, v101\n"
      "v_cvt_pk_bf16_f32 v102, 0, v102\n"
      "v_cvt_pk_bf16_f32 v103, 0, v103\n"
      "v_pk_mul_f32 v[96:97], v[96:97], s[84:85] op_sel:[0,1] op_sel_hi:[1,1]\n"
      "v_pk_mul_f32 v[98:99], v[98:99], s[84:85] op_sel:[0,1] op_sel_hi:[1,1]\n"
      "v_pk_mul_f32 v[100:101], v[100:101], s[84:85] op_sel:[0,1] op_sel_hi:[1,1]\n"
      "v_pk_mul_f32 v[102:103], v[102:103], s[84:85] op_sel:[0,1] op_sel_hi:[1,1]\n"
      "v_cvt_pk_bf16_f32 v96, 0, v96\n"
      "v_cvt_pk_bf16_f32 v97, 0, v97\n"
      "v_cvt_pk_bf16_f32 v98, 0, v98\n"
      "v_cvt_pk_bf16_f32 v99, 0, v99\n"
      "v_cvt_pk_bf16_f32 v100, 0, v100\n"
      "v_cvt_pk_bf16_f32 v101, 0, v101\n"
      "v_cvt_pk_bf16_f32 v102, 0, v102\n"
      "v_cvt_pk_bf16_f32 v103, 0, v103\n"
      "v_readlane_b32 s84, v220, 48\n"
      "v_readlane_b32 s85, v178, 48\n"
      "v_pk_add_f32 v[104:105], v[104:105], s[86:87] op_sel:[0,0] op_sel_hi:[1,0]\n"
      "v_pk_add_f32 v[106:107], v[106:107], s[86:87] op_sel:[0,0] op_sel_hi:[1,0]\n"
      "v_pk_add_f32 v[108:109], v[108:109], s[86:87] op_sel:[0,0] op_sel_hi:[1,0]\n"
      "v_pk_add_f32 v[110:111], v[110:111], s[86:87] op_sel:[0,0] op_sel_hi:[1,0]\n"
      "v_cvt_pk_bf16_f32 v104, 0, v104\n"
      "v_cvt_pk_bf16_f32 v105, 0, v105\n"
      "v_cvt_pk_bf16_f32 v106, 0, v106\n"
      "v_cvt_pk_bf16_f32 v107, 0, v107\n"
      "v_cvt_pk_bf16_f32 v108, 0, v108\n"
      "v_cvt_pk_bf16_f32 v109, 0, v109\n"
      "v_cvt_pk_bf16_f32 v110, 0, v110\n"
      "v_cvt_pk_bf16_f32 v111, 0, v111\n"
      "v_pk_mul_f32 v[104:105], v[104:105], s[86:87] op_sel:[0,1] op_sel_hi:[1,1]\n"
      "v_pk_mul_f32 v[106:107], v[106:107], s[86:87] op_sel:[0,1] op_sel_hi:[1,1]\n"
      "v_pk_mul_f32 v[108:109], v[108:109], s[86:87] op_sel:[0,1] op_sel_hi:[1,1]\n"
      "v_pk_mul_f32 v[110:111], v[110:111], s[86:87] op_sel:[0,1] op_sel_hi:[1,1]\n"
      "v_cvt_pk_bf16_f32 v104, 0, v104\n"
      "v_cvt_pk_bf16_f32 v105, 0, v105\n"
      "v_cvt_pk_bf16_f32 v106, 0, v106\n"
      "v_cvt_pk_bf16_f32 v107, 0, v107\n"
      "v_cvt_pk_bf16_f32 v108, 0, v108\n"
      "v_cvt_pk_bf16_f32 v109, 0, v109\n"
      "v_cvt_pk_bf16_f32 v110, 0, v110\n"
      "v_cvt_pk_bf16_f32 v111, 0, v111\n"
      "v_readlane_b32 s86, v221, 48\n"
      "v_readlane_b32 s87, v179, 48\n"
      "v_pk_add_f32 v[112:113], v[112:113], s[84:85] op_sel:[0,0] op_sel_hi:[1,0]\n"
      "v_pk_add_f32 v[114:115], v[114:115], s[84:85] op_sel:[0,0] op_sel_hi:[1,0]\n"
      "v_pk_add_f32 v[116:117], v[116:117], s[84:85] op_sel:[0,0] op_sel_hi:[1,0]\n"
      "v_pk_add_f32 v[118:119], v[118:119], s[84:85] op_sel:[0,0] op_sel_hi:[1,0]\n"
      "v_cvt_pk_bf16_f32 v112, 0, v112\n"
      "v_cvt_pk_bf16_f32 v113, 0, v113\n"
      "v_cvt_pk_bf16_f32 v114, 0, v114\n"
      "v_cvt_pk_bf16_f32 v115, 0, v115\n"
      "v_cvt_pk_bf16_f32 v116, 0, v116\n"
      "v_cvt_pk_bf16_f32 v117, 0, v117\n"
      "v_cvt_pk_bf16_f32 v118, 0, v118\n"
      "v_cvt_pk_bf16_f32 v119, 0, v119\n"
      "v_pk_mul_f32 v[112:113], v[112:113], s[84:85] op_sel:[0,1] op_sel_hi:[1,1]\n"
      "v_pk_mul_f32 v[114:115], v[114:115], s[84:85] op_sel:[0,1] op_sel_hi:[1,1]\n"
      "v_pk_mul_f32 v[116:117], v[116:117], s[84:85] op_sel:[0,1] op_sel_hi:[1,1]\n"
      "v_pk_mul_f32 v[118:119], v[118:119], s[84:85] op_sel:[0,1] op_sel_hi:[1,1]\n"
      "v_cvt_pk_bf16_f32 v112, 0, v112\n"
      "v_cvt_pk_bf16_f32 v113, 0, v113\n"
      "v_cvt_pk_bf16_f32 v114, 0, v114\n"
      "v_cvt_pk_bf16_f32 v115, 0, v115\n"
      "v_cvt_pk_bf16_f32 v116, 0, v116\n"
      "v_cvt_pk_bf16_f32 v117, 0, v117\n"
      "v_cvt_pk_bf16_f32 v118, 0, v118\n"
      "v_cvt_pk_bf16_f32 v119, 0, v119\n"
      "v_pk_add_f32 v[120:121], v[120:121], s[86:87] op_sel:[0,0] op_sel_hi:[1,0]\n"
      "v_pk_add_f32 v[122:123], v[122:123], s[86:87] op_sel:[0,0] op_sel_hi:[1,0]\n"
      "v_pk_add_f32 v[124:125], v[124:125], s[86:87] op_sel:[0,0] op_sel_hi:[1,0]\n"
      "v_pk_add_f32 v[126:127], v[126:127], s[86:87] op_sel:[0,0] op_sel_hi:[1,0]\n"
      "v_cvt_pk_bf16_f32 v120, 0, v120\n"
      "v_cvt_pk_bf16_f32 v121, 0, v121\n"
      "v_cvt_pk_bf16_f32 v122, 0, v122\n"
      "v_cvt_pk_bf16_f32 v123, 0, v123\n"
      "v_cvt_pk_bf16_f32 v124, 0, v124\n"
      "v_cvt_pk_bf16_f32 v125, 0, v125\n"
      "v_cvt_pk_bf16_f32 v126, 0, v126\n"
      "v_cvt_pk_bf16_f32 v127, 0, v127\n"
      "v_pk_mul_f32 v[120:121], v[120:121], s[86:87] op_sel:[0,1] op_sel_hi:[1,1]\n"
      "v_pk_mul_f32 v[122:123], v[122:123], s[86:87] op_sel:[0,1] op_sel_hi:[1,1]\n"
      "v_pk_mul_f32 v[124:125], v[124:125], s[86:87] op_sel:[0,1] op_sel_hi:[1,1]\n"
      "v_pk_mul_f32 v[126:127], v[126:127], s[86:87] op_sel:[0,1] op_sel_hi:[1,1]\n"
      "v_cvt_pk_bf16_f32 v120, 0, v120\n"
      "v_cvt_pk_bf16_f32 v121, 0, v121\n"
      "v_cvt_pk_bf16_f32 v122, 0, v122\n"
      "v_cvt_pk_bf16_f32 v123, 0, v123\n"
      "v_cvt_pk_bf16_f32 v124, 0, v124\n"
      "v_cvt_pk_bf16_f32 v125, 0, v125\n"
      "v_cvt_pk_bf16_f32 v126, 0, v126\n"
      "v_cvt_pk_bf16_f32 v127, 0, v127\n"
      "s_mul_i32 s84, s80, 14336\n"
      "s_add_u32 s84, s84, 32768\n"
      "v_add_u32 v128, s84, v242\n"
      "v_xor_b32 v129, 16, v128\n"
      "v_xor_b32 v130, 32, v128\n"
      "v_xor_b32 v131, 48, v128\n"
      "v_xor_b32 v132, 64, v128\n"
      "v_xor_b32 v133, 80, v128\n"
      "v_xor_b32 v134, 96, v128\n"
      "v_xor_b32 v135, 112, v128\n"
      "s_lshr_b64 exec, -1, 8\n"
      "s_mov_b32 s85, 0x40000\n"
      "GATH_%=:\n"
      "s_waitcnt vmcnt(0)\n"
      "v_xor_b32 v136, s53, v145\n"
      "v_bitop3_b32 v136, v147, v245, v136 bitop3:0xbe\n"
      "v_bitop3_b32 v136, v149, v245, v136 bitop3:0xbe\n"
      "v_bitop3_b32 v136, v151, v245, v136 bitop3:0xbe\n"
      "v_bitop3_b32 v136, v153, v245, v136 bitop3:0xbe\n"
      "v_bitop3_b32 v136, v155, v245, v136 bitop3:0xbe\n"
      "v_bitop3_b32 v136, v157, v245, v136 bitop3:0xbe\n"
      "v_bitop3_b32 v136, v159, v245, v136 bitop3:0xbe\n"
      "v_cmp_ne_u32 vcc, 0, v136\n"
      "s_cbranch_vccz GOT_%=\n"
      "s_sub_u32 s85, s85, 1\n"
      "s_cbranch_scc1 GOT_%=\n"
      "s_sleep 2\n"
      "buffer_load_dwordx4 v[144:147], v240, s[76:79], 0 offen offset:0 sc1\n"
      "buffer_load_dwordx4 v[148:151], v240, s[76:79], 0 offen offset:16 sc1\n"
      "buffer_load_dwordx4 v[152:155], v240, s[76:79], 0 offen offset:1792 sc1\n"
      "buffer_load_dwordx4 v[156:159], v240, s[76:79], 0 offen offset:1808 sc1\n"
      "s_branch GATH_%=\n"
      "GOT_%=:\n"
      "v_lshlrev_b32 v168, 16, v152\n"
      "v_and_b32 v169, v253, v152\n"
      "v_lshlrev_b32 v160, 16, v144\n"
      "v_and_b32 v161, v253, v144\n"
      "v_lshlrev_b32 v170, 16, v154\n"
      "v_and_b32 v171, v253, v154\n"
      "v_lshlrev_b32 v162, 16, v146\n"
      "v_and_b32 v163, v253, v146\n"
      "v_lshlrev_b32 v172, 16, v156\n"
      "v_and_b32 v173, v253, v156\n"
      "v_lshlrev_b32 v164, 16, v148\n"
      "v_and_b32 v165, v253, v148\n"
      "v_lshlrev_b32 v174, 16, v158\n"
      "v_and_b32 v175, v253, v158\n"
      "v_lshlrev_b32 v166, 16, v150\n"
      "v_and_b32 v167, v253, v150\n"
      "v_pk_mul_f32 v[0:1], v[0:1], v[168:169]\n"
      "v_pk_mul_f32 v[2:3], v[2:3], v[170:171]\n"
      "v_pk_mul_f32 v[4:5], v[4:5], v[172:173]\n"
      "v_pk_mul_f32 v[6:7], v[6:7], v[174:175]\n"
      "v_cvt_pk_bf16_f32 v0, 0, v0\n"
      "v_cvt_pk_bf16_f32 v1, 0, v1\n"
      "v_cvt_pk_bf16_f32 v2, 0, v2\n"
      "v_cvt_pk_bf16_f32 v3, 0, v3\n"
      "v_cvt_pk_bf16_f32 v4, 0, v4\n"
      "v_cvt_pk_bf16_f32 v5, 0, v5\n"
      "v_cvt_pk_bf16_f32 v6, 0, v6\n"
      "v_cvt_pk_bf16_f32 v7, 0, v7\n"
      "s_mov_b64 exec, -1\n"
      "s_add_u32 m0, s81, 131072\n"
      "s_nop 0\n"
      "buffer_load_dwordx4 v208, s[64:67], s91 offen offset:0 lds\n"
      "s_lshr_b64 exec, -1, 8\n"
      "v_pk_add_f32 v[0:1], v[0:1], v[160:161]\n"
      "v_pk_add_f32 v[2:3], v[2:3], v[162:163]\n"
      "v_pk_add_f32 v[4:5], v[4:5], v[164:165]\n"
      "v_pk_add_f32 v[6:7], v[6:7], v[166:167]\n"
      "v_cvt_pk_bf16_f32 v0, v0, v1\n"
      "v_cvt_pk_bf16_f32 v1, v2, v3\n"
      "v_cvt_pk_bf16_f32 v2, v4, v5\n"
      "v_cvt_pk_bf16_f32 v3, v6, v7\n"
      "ds_write_b128 v128, v[0:3] offset:0\n"
      "v_pk_mul_f32 v[8:9], v[8:9], v[168:169]\n"
      "v_pk_mul_f32 v[10:11], v[10:11], v[170:171]\n"
      "v_pk_mul_f32 v[12:13], v[12:13], v[172:173]\n"
      "v_pk_mul_f32 v[14:15], v[14:15], v[174:175]\n"
      "v_cvt_pk_bf16_f32 v8, 0, v8\n"
      "v_cvt_pk_bf16_f32 v9, 0, v9\n"
      "v_cvt_pk_bf16_f32 v10, 0, v10\n"
      "v_cvt_pk_bf16_f32 v11, 0, v11\n"
      "v_cvt_pk_bf16_f32 v12, 0, v12\n"
      "v_cvt_pk_bf16_f32 v13, 0, v13\n"
      "v_cvt_pk_bf16_f32 v14, 0, v14\n"
      "v_cvt_pk_bf16_f32 v15, 0, v15\n"
      "s_mov_b64 exec, -1\n"
      "buffer_load_dwordx4 v209, s[64:67], s91 offen offset:1024 lds\n"
      "s_lshr_b64 exec, -1, 8\n"
      "v_pk_add_f32 v[8:9], v[8:9], v[160:161]\n"
      "v_pk_add_f32 v[10:11], v[10:11], v[162:163]\n"
      "v_pk_add_f32 v[12:13], v[12:13], v[164:165]\n"
      "v_pk_add_f32 v[14:15], v[14:15], v[166:167]\n"
      "v_cvt_pk_bf16_f32 v8, v8, v9\n"
      "v_cvt_pk_bf16_f32 v9, v10, v11\n"
      "v_cvt_pk_bf16_f32 v10, v12, v13\n"
      "v_cvt_pk_bf16_f32 v11, v14, v15\n"
      "ds_write_b128 v128, v[8:11] offset:896\n"
      "v_pk_mul_f32 v[16:17], v[16:17], v[168:169]\n"
      "v_pk_mul_f32 v[18:19], v[18:19], v[170:171]\n"
      "v_pk_mul_f32 v[20:21], v[20:21], v[172:173]\n"
      "v_pk_mul_f32 v[22:23], v[22:23], v[174:175]\n"
      "v_cvt_pk_bf16_f32 v16, 0, v16\n"
      "v_cvt_pk_bf16_f32 v17, 0, v17\n"
      "v_cvt_pk_bf16_f32 v18, 0, v18\n"
      "v_cvt_pk_bf16_f32 v19, 0, v19\n"
      "v_cvt_pk_bf16_f32 v20, 0, v20\n"
      "v_cvt_pk_bf16_f32 v21, 0, v21\n"
      "v_cvt_pk_bf16_f32 v22, 0, v22\n"
      "v_cvt_pk_bf16_f32 v23, 0, v23\n"
      "s_mov_b64 exec, -1\n"
      "buffer_load_dwordx4 v210, s[64:67], s91 offen offset:2048 lds\n"
      "s_lshr_b64 exec, -1, 8\n"
      "v_pk_add_f32 v[16:17], v[16:17], v[160:161]\n"
      "v_pk_add_f32 v[18:19], v[18:19], v[162:163]\n"
      "v_pk_add_f32 v[20:21], v[20:21], v[164:165]\n"
      "v_pk_add_f32 v[22:23], v[22:23], v[166:167]\n"
      "v_cvt_pk_bf16_f32 v16, v16, v17\n"
      "v_cvt_pk_bf16_f32 v17, v18, v19\n"
      "v_cvt_pk_bf16_f32 v18, v20, v21\n"
      "v_cvt_pk_bf16_f32 v19, v22, v23\n"
      "ds_write_b128 v129, v[16:19] offset:1792\n"
      "v_pk_mul_f32 v[24:25], v[24:25], v[168:169]\n"
      "v_pk_mul_f32 v[26:27], v[26:27], v[170:171]\n"
      "v_pk_mul_f32 v[28:29], v[28:29], v[172:173]\n"
      "v_pk_mul_f32 v[30:31], v[30:31], v[174:175]\n"
      "v_cvt_pk_bf16_f32 v24, 0, v24\n"
      "v_cvt_pk_bf16_f32 v25, 0, v25\n"
      "v_cvt_pk_bf16_f32 v26, 0, v26\n"
      "v_cvt_pk_bf16_f32 v27, 0, v27\n"
      "v_cvt_pk_bf16_f32 v28, 0, v28\n"
      "v_cvt_pk_bf16_f32 v29, 0, v29\n"
      "v_cvt_pk_bf16_f32 v30, 0, v30\n"
      "v_cvt_pk_bf16_f32 v31, 0, v31\n"
      "s_mov_b64 exec, -1\n"
      "buffer_load_dwordx4 v211, s[64:67], s91 offen offset:3072 lds\n"
      "s_lshr_b64 exec, -1, 8\n"
      "v_pk_add_f32 v[24:25], v[24:25], v[160:161]\n"
      "v_pk_add_f32 v[26:27], v[26:27], v[162:163]\n"
      "v_pk_add_f32 v[28:29], v[28:29], v[164:165]\n"
      "v_pk_add_f32 v[30:31], v[30:31], v[166:167]\n"
      "v_cvt_pk_bf16_f32 v24, v24, v25\n"
      "v_cvt_pk_bf16_f32 v25, v26, v27\n"
      "v_cvt_pk_bf16_f32 v26, v28, v29\n"
      "v_cvt_pk_bf16_f32 v27, v30, v31\n"
      "ds_write_b128 v129, v[24:27] offset:2688\n"
      "v_pk_mul_f32 v[32:33], v[32:33], v[168:169]\n"
      "v_pk_mul_f32 v[34:35], v[34:35], v[170:171]\n"
      "v_pk_mul_f32 v[36:37], v[36:37], v[172:173]\n"
      "v_pk_mul_f32 v[38:39], v[38:39], v[174:175]\n"
      "v_cvt_pk_bf16_f32 v32, 0, v32\n"
      "v_cvt_pk_bf16_f32 v33, 0, v33\n"
      "v_cvt_pk_bf16_f32 v34, 0, v34\n"
      "v_cvt_pk_bf16_f32 v35, 0, v35\n"
      "v_cvt_pk_bf16_f32 v36, 0, v36\n"
      "v_cvt_pk_bf16_f32 v37, 0, v37\n"
      "v_cvt_pk_bf16_f32 v38, 0, v38\n"
      "v_cvt_pk_bf16_f32 v39, 0, v39\n"
      "s_mov_b64 exec, -1\n"
      "s_add_u32 m0, s81, 135168\n"
      "s_nop 0\n"
      "buffer_load_dwordx4 v212, s[64:67], s91 offen offset:0 lds\n"
      "s_lshr_b64 exec, -1, 8\n"
      "v_pk_add_f32 v[32:33], v[32:33], v[160:161]\n"
      "v_pk_add_f32 v[34:35], v[34:35], v[162:163]\n"
      "v_pk_add_f32 v[36:37], v[36:37], v[164:165]\n"
      "v_pk_add_f32 v[38:39], v[38:39], v[166:167]\n"
      "v_cvt_pk_bf16_f32 v32, v32, v33\n"
      "v_cvt_pk_bf16_f32 v33, v34, v35\n"
      "v_cvt_pk_bf16_f32 v34, v36, v37\n"
      "v_cvt_pk_bf16_f32 v35, v38, v39\n"
      "ds_write_b128 v130, v[32:35] offset:3584\n"
      "v_pk_mul_f32 v[40:41], v[40:41], v[168:169]\n"
      "v_pk_mul_f32 v[42:43], v[42:43], v[170:171]\n"
      "v_pk_mul_f32 v[44:45], v[44:45], v[172:173]\n"
      "v_pk_mul_f32 v[46:47], v[46:47], v[174:175]\n"
      "v_cvt_pk_bf16_f32 v40, 0, v40\n"
      "v_cvt_pk_bf16_f32 v41, 0, v41\n"
      "v_cvt_pk_bf16_f32 v42, 0, v42\n"
      "v_cvt_pk_bf16_f32 v43, 0, v43\n"
      "v_cvt_pk_bf16_f32 v44, 0, v44\n"
      "v_cvt_pk_bf16_f32 v45, 0, v45\n"
      "v_cvt_pk_bf16_f32 v46, 0, v46\n"
      "v_cvt_pk_bf16_f32 v47, 0, v47\n"
      "s_mov_b64 exec, -1\n"
      "buffer_load_dwordx4 v213, s[64:67], s91 offen offset:1024 lds\n"
      "s_lshr_b64 exec, -1, 8\n"
      "v_pk_add_f32 v[40:41], v[40:41], v[160:161]\n"
      "v_pk_add_f32 v[42:43], v[42:43], v[162:163]\n"
      "v_pk_add_f32 v[44:45], v[44:45], v[164:165]\n"
      "v_pk_add_f32 v[46:47], v[46:47], v[166:167]\n"
      "v_cvt_pk_bf16_f32 v40, v40, v41\n"
      "v_cvt_pk_bf16_f32 v41, v42, v43\n"
      "v_cvt_pk_bf16_f32 v42, v44, v45\n"
      "v_cvt_pk_bf16_f32 v43, v46, v47\n"
      "ds_write_b128 v130, v[40:43] offset:4480\n"
      "v_pk_mul_f32 v[48:49], v[48:49], v[168:169]\n"
      "v_pk_mul_f32 v[50:51], v[50:51], v[170:171]\n"
      "v_pk_mul_f32 v[52:53], v[52:53], v[172:173]\n"
      "v_pk_mul_f32 v[54:55], v[54:55], v[174:175]\n"
      "v_cvt_pk_bf16_f32 v48, 0, v48\n"
      "v_cvt_pk_bf16_f32 v49, 0, v49\n"
      "v_cvt_pk_bf16_f32 v50, 0, v50\n"
      "v_cvt_pk_bf16_f32 v51, 0, v51\n"
      "v_cvt_pk_bf16_f32 v52, 0, v52\n"
      "v_cvt_pk_bf16_f32 v53, 0, v53\n"
      "v_cvt_pk_bf16_f32 v54, 0, v54\n"
      "v_cvt_pk_bf16_f32 v55, 0, v55\n"
      "s_mov_b64 exec, -1\n"
      "buffer_load_dwordx4 v214, s[64:67], s91 offen offset:2048 lds\n"
      "s_lshr_b64 exec, -1, 8\n"
      "v_pk_add_f32 v[48:49], v[48:49], v[160:161]\n"
      "v_pk_add_f32 v[50:51], v[50:51], v[162:163]\n"
      "v_pk_add_f32 v[52:53], v[52:53], v[164:165]\n"
      "v_pk_add_f32 v[54:55], v[54:55], v[166:167]\n"
      "v_cvt_pk_bf16_f32 v48, v48, v49\n"
      "v_cvt_pk_bf16_f32 v49, v50, v51\n"
      "v_cvt_pk_bf16_f32 v50, v52, v53\n"
      "v_cvt_pk_bf16_f32 v51, v54, v55\n"
      "ds_write_b128 v131, v[48:51] offset:5376\n"
      "v_pk_mul_f32 v[56:57], v[56:57], v[168:169]\n"
      "v_pk_mul_f32 v[58:59], v[58:59], v[170:171]\n"
      "v_pk_mul_f32 v[60:61], v[60:61], v[172:173]\n"
      "v_pk_mul_f32 v[62:63], v[62:63], v[174:175]\n"
      "v_cvt_pk_bf16_f32 v56, 0, v56\n"
      "v_cvt_pk_bf16_f32 v57, 0, v57\n"
      "v_cvt_pk_bf16_f32 v58, 0, v58\n"
      "v_cvt_pk_bf16_f32 v59, 0, v59\n"
      "v_cvt_pk_bf16_f32 v60, 0, v60\n"
      "v_cvt_pk_bf16_f32 v61, 0, v61\n"
      "v_cvt_pk_bf16_f32 v62, 0, v62\n"
      "v_cvt_pk_bf16_f32 v63, 0, v63\n"
      "s_mov_b64 exec, -1\n"
      "buffer_load_dwordx4 v215, s[64:67], s91 offen offset:3072 lds\n"
      "s_add_u32 s91, s91, 262144\n"
      "s_cmp_ge_u32 s91, 3670016\n"
      "s_cselect_b32 s91, 0, s91\n"
      "s_lshr_b64 exec, -1, 8\n"
      "v_pk_add_f32 v[56:57], v[56:57], v[160:161]\n"
      "v_pk_add_f32 v[58:59], v[58:59], v[162:163]\n"
      "v_pk_add_f32 v[60:61], v[60:61], v[164:165]\n"
      "v_pk_add_f32 v[62:63], v[62:63], v[166:167]\n"
      "v_cvt_pk_bf16_f32 v56, v56, v57\n"
      "v_cvt_pk_bf16_f32 v57, v58, v59\n"
      "v_cvt_pk_bf16_f32 v58, v60, v61\n"
      "v_cvt_pk_bf16_f32 v59, v62, v63\n"
      "ds_write_b128 v131, v[56:59] offset:6272\n"
      "v_pk_mul_f32 v[64:65], v[64:65], v[168:169]\n"
      "v_pk_mul_f32 v[66:67], v[66:67], v[170:171]\n"
      "v_pk_mul_f32 v[68:69], v[68:69], v[172:173]\n"
      "v_pk_mul_f32 v[70:71], v[70:71], v[174:175]\n"
      "v_cvt_pk_bf16_f32 v64, 0, v64\n"
      "v_cvt_pk_bf16_f32 v65, 0, v65\n"
      "v_cvt_pk_bf16_f32 v66, 0, v66\n"
      "v_cvt_pk_bf16_f32 v67, 0, v67\n"
      "v_cvt_pk_bf16_f32 v68, 0, v68\n"
      "v_cvt_pk_bf16_f32 v69, 0, v69\n"
      "v_cvt_pk_bf16_f32 v70, 0, v70\n"
      "v_cvt_pk_bf16_f32 v71, 0, v71\n"
      "s_mov_b64 exec, -1\n"
      "s_add_u32 m0, s81, 0\n"
      "s_nop 0\n"
      "buffer_load_dwordx4 v208, s[64:67], s91 offen offset:0 lds\n"
      "s_lshr_b64 exec, -1, 8\n"
      "v_pk_add_f32 v[64:65], v[64:65], v[160:161]\n"
      "v_pk_add_f32 v[66:67], v[66:67], v[162:163]\n"
      "v_pk_add_f32 v[68:69], v[68:69], v[164:165]\n"
      "v_pk_add_f32 v[70:71], v[70:71], v[166:167]\n"
      "v_cvt_pk_bf16_f32 v64, v64, v65\n"
      "v_cvt_pk_bf16_f32 v65, v66, v67\n"
      "v_cvt_pk_bf16_f32 v66, v68, v69\n"
      "v_cvt_pk_bf16_f32 v67, v70, v71\n"
      "ds_write_b128 v132, v[64:67] offset:7168\n"
      "v_pk_mul_f32 v[72:73], v[72:73], v[168:169]\n"
      "v_pk_mul_f32 v[74:75], v[74:75], v[170:171]\n"
      "v_pk_mul_f32 v[76:77], v[76:77], v[172:173]\n"
      "v_pk_mul_f32 v[78:79], v[78:79], v[174:175]\n"
      "v_cvt_pk_bf16_f32 v72, 0, v72\n"
      "v_cvt_pk_bf16_f32 v73, 0, v73\n"
      "v_cvt_pk_bf16_f32 v74, 0, v74\n"
      "v_cvt_pk_bf16_f32 v75, 0, v75\n"
      "v_cvt_pk_bf16_f32 v76, 0, v76\n"
      "v_cvt_pk_bf16_f32 v77, 0, v77\n"
      "v_cvt_pk_bf16_f32 v78, 0, v78\n"
      "v_cvt_pk_bf16_f32 v79, 0, v79\n"
      "s_mov_b64 exec, -1\n"
      "buffer_load_dwordx4 v209, s[64:67], s91 offen offset:1024 lds\n"
      "s_lshr_b64 exec, -1, 8\n"
      "v_pk_add_f32 v[72:73], v[72:73], v[160:161]\n"
      "v_pk_add_f32 v[74:75], v[74:75], v[162:163]\n"
      "v_pk_add_f32 v[76:77], v[76:77], v[164:165]\n"
      "v_pk_add_f32 v[78:79], v[78:79], v[166:167]\n"
      "v_cvt_pk_bf16_f32 v72, v72, v73\n"
      "v_cvt_pk_bf16_f32 v73, v74, v75\n"
      "v_cvt_pk_bf16_f32 v74, v76, v77\n"
      "v_cvt_pk_bf16_f32 v75, v78, v79\n"
      "ds_write_b128 v132, v[72:75] offset:8064\n"
      "v_pk_mul_f32 v[80:81], v[80:81], v[168:169]\n"
      "v_pk_mul_f32 v[82:83], v[82:83], v[170:171]\n"
      "v_pk_mul_f32 v[84:85], v[84:85], v[172:173]\n"
      "v_pk_mul_f32 v[86:87], v[86:87], v[174:175]\n"
      "v_cvt_pk_bf16_f32 v80, 0, v80\n"
      "v_cvt_pk_bf16_f32 v81, 0, v81\n"
      "v_cvt_pk_bf16_f32 v82, 0, v82\n"
      "v_cvt_pk_bf16_f32 v83, 0, v83\n"
      "v_cvt_pk_bf16_f32 v84, 0, v84\n"
      "v_cvt_pk_bf16_f32 v85, 0, v85\n"
      "v_cvt_pk_bf16_f32 v86, 0, v86\n"
      "v_cvt_pk_bf16_f32 v87, 0, v87\n"
      "s_mov_b64 exec, -1\n"
      "buffer_load_dwordx4 v210, s[64:67], s91 offen offset:2048 lds\n"
      "s_lshr_b64 exec, -1, 8\n"
      "v_pk_add_f32 v[80:81], v[80:81], v[160:161]\n"
      "v_pk_add_f32 v[82:83], v[82:83], v[162:163]\n"
      "v_pk_add_f32 v[84:85], v[84:85], v[164:165]\n"
      "v_pk_add_f32 v[86:87], v[86:87], v[166:167]\n"
      "v_cvt_pk_bf16_f32 v80, v80, v81\n"
      "v_cvt_pk_bf16_f32 v81, v82, v83\n"
      "v_cvt_pk_bf16_f32 v82, v84, v85\n"
      "v_cvt_pk_bf16_f32 v83, v86, v87\n"
      "ds_write_b128 v133, v[80:83] offset:8960\n"
      "v_pk_mul_f32 v[88:89], v[88:89], v[168:169]\n"
      "v_pk_mul_f32 v[90:91], v[90:91], v[170:171]\n"
      "v_pk_mul_f32 v[92:93], v[92:93], v[172:173]\n"
      "v_pk_mul_f32 v[94:95], v[94:95], v[174:175]\n"
      "v_cvt_pk_bf16_f32 v88, 0, v88\n"
      "v_cvt_pk_bf16_f32 v89, 0, v89\n"
      "v_cvt_pk_bf16_f32 v90, 0, v90\n"
      "v_cvt_pk_bf16_f32 v91, 0, v91\n"
      "v_cvt_pk_bf16_f32 v92, 0, v92\n"
      "v_cvt_pk_bf16_f32 v93, 0, v93\n"
      "v_cvt_pk_bf16_f32 v94, 0, v94\n"
      "v_cvt_pk_bf16_f32 v95, 0, v95\n"
      "s_mov_b64 exec, -1\n"
      "buffer_load_dwordx4 v211, s[64:67], s91 offen offset:3072 lds\n"
      "s_lshr_b64 exec, -1, 8\n"
      "v_pk_add_f32 v[88:89], v[88:89], v[160:161]\n"
      "v_pk_add_f32 v[90:91], v[90:91], v[162:163]\n"
      "v_pk_add_f32 v[92:93], v[92:93], v[164:165]\n"
      "v_pk_add_f32 v[94:95], v[94:95], v[166:167]\n"
      "v_cvt_pk_bf16_f32 v88, v88, v89\n"
      "v_cvt_pk_bf16_f32 v89, v90, v91\n"
      "v_cvt_pk_bf16_f32 v90, v92, v93\n"
      "v_cvt_pk_bf16_f32 v91, v94, v95\n"
      "ds_write_b128 v133, v[88:91] offset:9856\n"
      "v_pk_mul_f32 v[96:97], v[96:97], v[168:169]\n"
      "v_pk_mul_f32 v[98:99], v[98:99], v[170:171]\n"
      "v_pk_mul_f32 v[100:101], v[100:101], v[172:173]\n"
      "v_pk_mul_f32 v[102:103], v[102:103], v[174:175]\n"
      "v_cvt_pk_bf16_f32 v96, 0, v96\n"
      "v_cvt_pk_bf16_f32 v97, 0, v97\n"
      "v_cvt_pk_bf16_f32 v98, 0, v98\n"
      "v_cvt_pk_bf16_f32 v99, 0, v99\n"
      "v_cvt_pk_bf16_f32 v100, 0, v100\n"
      "v_cvt_pk_bf16_f32 v101, 0, v101\n"
      "v_cvt_pk_bf16_f32 v102, 0, v102\n"
      "v_cvt_pk_bf16_f32 v103, 0, v103\n"
      "s_mov_b64 exec, -1\n"
      "s_add_u32 m0, s81, 4096\n"
      "s_nop 0\n"
      "buffer_load_dwordx4 v212, s[64:67], s91 offen offset:0 lds\n"
      "s_lshr_b64 exec, -1, 8\n"
      "v_pk_add_f32 v[96:97], v[96:97], v[160:161]\n"
      "v_pk_add_f32 v[98:99], v[98:99], v[162:163]\n"
      "v_pk_add_f32 v[100:101], v[100:101], v[164:165]\n"
      "v_pk_add_f32 v[102:103], v[102:103], v[166:167]\n"
      "v_cvt_pk_bf16_f32 v96, v96, v97\n"
      "v_cvt_pk_bf16_f32 v97, v98, v99\n"
      "v_cvt_pk_bf16_f32 v98, v100, v101\n"
      "v_cvt_pk_bf16_f32 v99, v102, v103\n"
      "ds_write_b128 v134, v[96:99] offset:10752\n"
      "v_pk_mul_f32 v[104:105], v[104:105], v[168:169]\n"
      "v_pk_mul_f32 v[106:107], v[106:107], v[170:171]\n"
      "v_pk_mul_f32 v[108:109], v[108:109], v[172:173]\n"
      "v_pk_mul_f32 v[110:111], v[110:111], v[174:175]\n"
      "v_cvt_pk_bf16_f32 v104, 0, v104\n"
      "v_cvt_pk_bf16_f32 v105, 0, v105\n"
      "v_cvt_pk_bf16_f32 v106, 0, v106\n"
      "v_cvt_pk_bf16_f32 v107, 0, v107\n"
      "v_cvt_pk_bf16_f32 v108, 0, v108\n"
      "v_cvt_pk_bf16_f32 v109, 0, v109\n"
      "v_cvt_pk_bf16_f32 v110, 0, v110\n"
      "v_cvt_pk_bf16_f32 v111, 0, v111\n"
      "s_mov_b64 exec, -1\n"
      "buffer_load_dwordx4 v213, s[64:67], s91 offen offset:1024 lds\n"
      "s_lshr_b64 exec, -1, 8\n"
      "v_pk_add_f32 v[104:105], v[104:105], v[160:161]\n"
      "v_pk_add_f32 v[106:107], v[106:107], v[162:163]\n"
      "v_pk_add_f32 v[108:109], v[108:109], v[164:165]\n"
      "v_pk_add_f32 v[110:111], v[110:111], v[166:167]\n"
      "v_cvt_pk_bf16_f32 v104, v104, v105\n"
      "v_cvt_pk_bf16_f32 v105, v106, v107\n"
      "v_cvt_pk_bf16_f32 v106, v108, v109\n"
      "v_cvt_pk_bf16_f32 v107, v110, v111\n"
      "ds_write_b128 v134, v[104:107] offset:11648\n"
      "v_pk_mul_f32 v[112:113], v[112:113], v[168:169]\n"
      "v_pk_mul_f32 v[114:115], v[114:115], v[170:171]\n"
      "v_pk_mul_f32 v[116:117], v[116:117], v[172:173]\n"
      "v_pk_mul_f32 v[118:119], v[118:119], v[174:175]\n"
      "v_cvt_pk_bf16_f32 v112, 0, v112\n"
      "v_cvt_pk_bf16_f32 v113, 0, v113\n"
      "v_cvt_pk_bf16_f32 v114, 0, v114\n"
      "v_cvt_pk_bf16_f32 v115, 0, v115\n"
      "v_cvt_pk_bf16_f32 v116, 0, v116\n"
      "v_cvt_pk_bf16_f32 v117, 0, v117\n"
      "v_cvt_pk_bf16_f32 v118, 0, v118\n"
      "v_cvt_pk_bf16_f32 v119, 0, v119\n"
      "s_mov_b64 exec, -1\n"
      "buffer_load_dwordx4 v214, s[64:67], s91 offen offset:2048 lds\n"
      "s_lshr_b64 exec, -1, 8\n"
      "v_pk_add_f32 v[112:113], v[112:113], v[160:161]\n"
      "v_pk_add_f32 v[114:115], v[114:115], v[162:163]\n"
      "v_pk_add_f32 v[116:117], v[116:117], v[164:165]\n"
      "v_pk_add_f32 v[118:119], v[118:119], v[166:167]\n"
      "v_cvt_pk_bf16_f32 v112, v112, v113\n"
      "v_cvt_pk_bf16_f32 v113, v114, v115\n"
      "v_cvt_pk_bf16_f32 v114, v116, v117\n"
      "v_cvt_pk_bf16_f32 v115, v118, v119\n"
      "ds_write_b128 v135, v[112:115] offset:12544\n"
      "v_pk_mul_f32 v[120:121], v[120:121], v[168:169]\n"
      "v_pk_mul_f32 v[122:123], v[122:123], v[170:171]\n"
      "v_pk_mul_f32 v[124:125], v[124:125], v[172:173]\n"
      "v_pk_mul_f32 v[126:127], v[126:127], v[174:175]\n"
      "v_cvt_pk_bf16_f32 v120, 0, v120\n"
      "v_cvt_pk_bf16_f32 v121, 0, v121\n"
      "v_cvt_pk_bf16_f32 v122, 0, v122\n"
      "v_cvt_pk_bf16_f32 v123, 0, v123\n"
      "v_cvt_pk_bf16_f32 v124, 0, v124\n"
      "v_cvt_pk_bf16_f32 v125, 0, v125\n"
      "v_cvt_pk_bf16_f32 v126, 0, v126\n"
      "v_cvt_pk_bf16_f32 v127, 0, v127\n"
      "s_mov_b64 exec, -1\n"
      "buffer_load_dwordx4 v215, s[64:67], s91 offen offset:3072 lds\n"
      "s_add_u32 s91, s91, 262144\n"
      "s_cmp_ge_u32 s91, 3670016\n"
      "s_cselect_b32 s91, 0, s91\n"
      "s_lshr_b64 exec, -1, 8\n"
      "v_pk_add_f32 v[120:121], v[120:121], v[160:161]\n"
      "v_pk_add_f32 v[122:123], v[122:123], v[162:163]\n"
      "v_pk_add_f32 v[124:125], v[124:125], v[164:165]\n"
      "v_pk_add_f32 v[126:127], v[126:127], v[166:167]\n"
      "v_cvt_pk_bf16_f32 v120, v120, v121\n"
      "v_cvt_pk_bf16_f32 v121, v122, v123\n"
      "v_cvt_pk_bf16_f32 v122, v124, v125\n"
      "v_cvt_pk_bf16_f32 v123, v126, v127\n"
      "ds_write_b128 v135, v[120:123] offset:13440\n"
      "s_mov_b64 exec, -1\n"
      "v_and_b32 v244, 15, v254\n"
      "v_lshrrev_b32 v245, 1, v244\n"
      "v_xor_b32 v245, v245, v252\n"
      "v_mul_u32_u24 v244, 896, v244\n"
      "v_lshl_add_u32 v244, v245, 4, v244\n"
      "v_add_u32 v243, 32768, v244\n"
      "v_xor_b32 v253, 64, v243\n"
      "s_waitcnt lgkmcnt(0)\n"
      "s_barrier\n"
      "ds_read_b128 a[0:3], v243 offset:0\n"
      "ds_read_b128 a[4:7], v243 offset:14336\n"
      "ds_read_b128 a[8:11], v243 offset:28672\n"
      "ds_read_b128 a[12:15], v243 offset:43008\n"
      "ds_read_b64_tr_b16 v[128:129], v200 offset:0\n"
      "ds_read_b64_tr_b16 v[130:131], v200 offset:1024\n"
      "ds_read_b64_tr_b16 v[132:133], v201 offset:0\n"
      "ds_read_b64_tr_b16 v[134:135], v201 offset:1024\n"
      "ds_read_b64_tr_b16 v[136:137], v202 offset:0\n"
      "ds_read_b64_tr_b16 v[138:139], v202 offset:1024\n"
      "ds_read_b64_tr_b16 v[140:141], v203 offset:0\n"
      "ds_read_b64_tr_b16 v[142:143], v203 offset:1024\n"
      "ds_read_b64_tr_b16 v[144:145], v204 offset:0\n"
      "ds_read_b64_tr_b16 v[146:147], v204 offset:1024\n"
      "ds_read_b64_tr_b16 v[148:149], v205 offset:0\n"
      "ds_read_b64_tr_b16 v[150:151], v205 offset:1024\n"
      "ds_read_b64_tr_b16 v[152:153], v206 offset:0\n"
      "ds_read_b64_tr_b16 v[154:155], v206 offset:1024\n"
      "ds_read_b64_tr_b16 v[156:157], v207 offset:0\n"
      "ds_read_b64_tr_b16 v[158:159], v207 offset:1024\n"
      "s_waitcnt lgkmcnt(0)\n"
      "v_mfma_f32_16x16x32_bf16 v[0:3], v[128:131], a[0:3], 0\n"
      "ds_read_b128 a[16:19], v253 offset:0\n"
      "v_mfma_f32_16x16x32_bf16 v[4:7], v[128:131], a[4:7], 0\n"
      "s_add_u32 m0, s81, 98304\n"
      "ds_read_b128 a[20:23], v253 offset:14336\n"
      "v_mfma_f32_16x16x32_bf16 v[8:11], v[128:131], a[8:11], 0\n"
      "buffer_load_dwordx4 v208, s[64:67], s91 offen offset:0 lds\n"
      "v_mfma_f32_16x16x32_bf16 v[12:15], v[128:131], a[12:15], 0\n"
      "s_waitcnt vmcnt(9)\n"
      "ds_read_b128 a[24:27], v253 offset:28672\n"
      "v_mfma_f32_16x16x32_bf16 v[16:19], v[132:135], a[0:3], 0\n"
      "s_mov_b32 s89, 0xc2180000\n"
      "ds_read_b128 a[28:31], v253 offset:43008\n"
      "v_mfma_f32_16x16x32_bf16 v[20:23], v[132:135], a[4:7], 0\n"
      "buffer_load_dwordx4 v209, s[64:67], s91 offen offset:1024 lds\n"
      "v_mfma_f32_16x16x32_bf16 v[24:27], v[132:135], a[8:11], 0\n"
      "s_mov_b32 s94, 0x3db8aa3b\n"
      "ds_read_b64_tr_b16 v[160:161], v200 offset:32768\n"
      "v_mfma_f32_16x16x32_bf16 v[28:31], v[132:135], a[12:15], 0\n"
      "s_mov_b32 s88, 1\n"
      "ds_read_b64_tr_b16 v[162:163], v200 offset:33792\n"
      "v_mfma_f32_16x16x32_bf16 v[32:35], v[136:139], a[0:3], 0\n"
      "buffer_load_dwordx4 v210, s[64:67], s91 offen offset:2048 lds\n"
      "v_mfma_f32_16x16x32_bf16 v[36:39], v[136:139], a[4:7], 0\n"
      "ds_read_b64_tr_b16 v[164:165], v201 offset:32768\n"
      "v_lshrrev_b32 v244, 5, v254\n"
      "v_mfma_f32_16x16x32_bf16 v[40:43], v[136:139], a[8:11], 0\n"
      "ds_read_b64_tr_b16 v[166:167], v201 offset:33792\n"
      "v_and_b32 v245, 31, v254\n"
      "v_mfma_f32_16x16x32_bf16 v[44:47], v[136:139], a[12:15], 0\n"
      "buffer_load_dwordx4 v211, s[64:67], s91 offen offset:3072 lds\n"
      "v_mfma_f32_16x16x32_bf16 v[48:51], v[140:143], a[0:3], 0\n"
      "ds_read_b64_tr_b16 v[168:169], v202 offset:32768\n"
      "v_and_b32 v247, 16, v254\n"
      "v_mfma_f32_16x16x32_bf16 v[52:55], v[140:143], a[4:7], 0\n"
      "s_add_u32 m0, s81, 102400\n"
      "ds_read_b64_tr_b16 v[170:171], v202 offset:33792\n"
      "v_lshlrev_b32 v247, 10, v247\n"
      "v_mfma_f32_16x16x32_bf16 v[56:59], v[140:143], a[8:11], 0\n"
      "buffer_load_dwordx4 v212, s[64:67], s91 offen offset:0 lds\n"
      "v_mfma_f32_16x16x32_bf16 v[60:63], v[140:143], a[12:15], 0\n"
      "ds_read_b64_tr_b16 v[172:173], v203 offset:32768\n"
      "v_lshl_add_u32 v246, v244, 1, 0\n"
      "v_mfma_f32_16x16x32_bf16 v[64:67], v[144:147], a[0:3], 0\n"
      "ds_read_b64_tr_b16 v[174:175], v203 offset:33792\n"
      "v_xor_b32 v246, v246, v245\n"
      "v_mfma_f32_16x16x32_bf16 v[68:71], v[144:147], a[4:7], 0\n"
      "buffer_load_dwordx4 v213, s[64:67], s91 offen offset:1024 lds\n"
      "v_mfma_f32_16x16x32_bf16 v[72:75], v[144:147], a[8:11], 0\n"
      "ds_read_b64_tr_b16 v[176:177], v204 offset:32768\n"
      "v_lshlrev_b32 v246, 4, v246\n"
      "v_mfma_f32_16x16x32_bf16 v[76:79], v[144:147], a[12:15], 0\n"
      "ds_read_b64_tr_b16 v[178:179], v204 offset:33792\n"
      "v_lshl_add_u32 v246, v244, 9, v246\n"
      "v_mfma_f32_16x16x32_bf16 v[80:83], v[148:151], a[0:3], 0\n"
      "buffer_load_dwordx4 v214, s[64:67], s91 offen offset:2048 lds\n"
      "v_mfma_f32_16x16x32_bf16 v[84:87], v[148:151], a[4:7], 0\n"
      "ds_read_b64_tr_b16 v[180:181], v205 offset:32768\n"
      "v_add_u32 v230, s81, v246\n"
      "v_mfma_f32_16x16x32_bf16 v[88:91], v[148:151], a[8:11], 0\n"
      "ds_read_b64_tr_b16 v[182:183], v205 offset:33792\n"
      "v_xor_b32 v234, v230, v247\n"
      "v_mfma_f32_16x16x32_bf16 v[92:95], v[148:151], a[12:15], 0\n"
      "buffer_load_dwordx4 v215, s[64:67], s91 offen offset:3072 lds\n"
      "v_mfma_f32_16x16x32_bf16 v[96:99], v[152:155], a[0:3], 0\n"
      "ds_read_b64_tr_b16 v[184:185], v206 offset:32768\n"
      "v_lshl_add_u32 v246, v244, 1, 4\n"
      "v_mfma_f32_16x16x32_bf16 v[100:103], v[152:155], a[4:7], 0\n"
      "ds_read_b64_tr_b16 v[186:187], v206 offset:33792\n"
      "v_xor_b32 v246, v246, v245\n"
      "v_mfma_f32_16x16x32_bf16 v[104:107], v[152:155], a[8:11], 0\n"
      "ds_read_b64_tr_b16 v[188:189], v207 offset:32768\n"
      "v_lshlrev_b32 v246, 4, v246\n"
      "v_mfma_f32_16x16x32_bf16 v[108:111], v[152:155], a[12:15], 0\n"
      "ds_read_b64_tr_b16 v[190:191], v207 offset:33792\n"
      "v_lshl_add_u32 v246, v244, 9, v246\n"
      "v_mfma_f32_16x16x32_bf16 v[112:115], v[156:159], a[0:3], 0\n"
      "ds_read_b128 a[32:35], v243 offset:128\n"
      "v_add_u32 v231, s81, v246\n"
      "v_mfma_f32_16x16x32_bf16 v[116:119], v[156:159], a[4:7], 0\n"
      "s_add_u32 s91, s91, 262144\n"
      "ds_read_b128 a[36:39], v243 offset:14464\n"
      "v_mfma_f32_16x16x32_bf16 v[120:123], v[156:159], a[8:11], 0\n"
      "s_cmp_ge_u32 s91, 3670016\n"
      "s_cselect_b32 s91, 0, s91\n"
      "ds_read_b128 a[40:43], v243 offset:28800\n"
      "v_mfma_f32_16x16x32_bf16 v[124:127], v[156:159], a[12:15], 0\n"
      "s_waitcnt lgkmcnt(11)\n"
      "ds_read_b128 a[44:47], v243 offset:43136\n"
      "v_mfma_f32_16x16x32_bf16 v[0:3], v[160:163], a[16:19], v[0:3]\n"
      "s_waitcnt lgkmcnt(4)\n"
      "ds_read_b128 a[48:51], v253 offset:128\n"
      "v_xor_b32 v235, v231, v247\n"
      "v_mfma_f32_16x16x32_bf16 v[4:7], v[160:163], a[20:23], v[4:7]\n"
      "s_add_u32 m0, s81, 131072\n"
      "ds_read_b128 a[52:55], v253 offset:14464\n"
      "v_lshl_add_u32 v246, v244, 1, 8\n"
      "v_mfma_f32_16x16x32_bf16 v[8:11], v[160:163], a[24:27], v[8:11]\n"
      "buffer_load_dwordx4 v208, s[64:67], s91 offen offset:0 lds\n"
      "v_mfma_f32_16x16x32_bf16 v[12:15], v[160:163], a[28:31], v[12:15]\n"
      "s_waitcnt vmcnt(9)\n"
      "ds_read_b128 a[56:59], v253 offset:28800\n"
      "v_xor_b32 v246, v246, v245\n"
      "v_mfma_f32_16x16x32_bf16 v[16:19], v[164:167], a[16:19], v[16:19]\n"
      "ds_read_b128 a[60:63], v253 offset:43136\n"
      "v_lshlrev_b32 v246, 4, v246\n"
      "v_mfma_f32_16x16x32_bf16 v[20:23], v[164:167], a[20:23], v[20:23]\n"
      "buffer_load_dwordx4 v209, s[64:67], s91 offen offset:1024 lds\n"
      "v_mfma_f32_16x16x32_bf16 v[24:27], v[164:167], a[24:27], v[24:27]\n"
      "ds_read_b64_tr_b16 v[128:129], v192 offset:0\n"
      "v_lshl_add_u32 v246, v244, 9, v246\n"
      "v_mfma_f32_16x16x32_bf16 v[28:31], v[164:167], a[28:31], v[28:31]\n"
      "ds_read_b64_tr_b16 v[130:131], v192 offset:1024\n"
      "v_add_u32 v232, s81, v246\n"
      "v_mfma_f32_16x16x32_bf16 v[32:35], v[168:171], a[16:19], v[32:35]\n"
      "buffer_load_dwordx4 v210, s[64:67], s91 offen offset:2048 lds\n"
      "v_mfma_f32_16x16x32_bf16 v[36:39], v[168:171], a[20:23], v[36:39]\n"
      "ds_read_b64_tr_b16 v[132:133], v193 offset:0\n"
      "v_xor_b32 v236, v232, v247\n"
      "v_mfma_f32_16x16x32_bf16 v[40:43], v[168:171], a[24:27], v[40:43]\n"
      "ds_read_b64_tr_b16 v[134:135], v193 offset:1024\n"
      "v_lshl_add_u32 v246, v244, 1, 12\n"
      "v_mfma_f32_16x16x32_bf16 v[44:47], v[168:171], a[28:31], v[44:47]\n"
      "buffer_load_dwordx4 v211, s[64:67], s91 offen offset:3072 lds\n"
      "v_mfma_f32_16x16x32_bf16 v[48:51], v[172:175], a[16:19], v[48:51]\n"
      "ds_read_b64_tr_b16 v[136:137], v194 offset:0\n"
      "v_xor_b32 v246, v246, v245\n"
      "v_mfma_f32_16x16x32_bf16 v[52:55], v[172:175], a[20:23], v[52:55]\n"
      "s_add_u32 m0, s81, 135168\n"
      "ds_read_b64_tr_b16 v[138:139], v194 offset:1024\n"
      "v_lshlrev_b32 v246, 4, v246\n"
      "v_mfma_f32_16x16x32_bf16 v[56:59], v[172:175], a[24:27], v[56:59]\n"
      "buffer_load_dwordx4 v212, s[64:67], s91 offen offset:0 lds\n"
      "v_mfma_f32_16x16x32_bf16 v[60:63], v[172:175], a[28:31], v[60:63]\n"
      "ds_read_b64_tr_b16 v[140:141], v195 offset:0\n"
      "v_lshl_add_u32 v246, v244, 9, v246\n"
      "v_mfma_f32_16x16x32_bf16 v[64:67], v[176:179], a[16:19], v[64:67]\n"
      "ds_read_b64_tr_b16 v[142:143], v195 offset:1024\n"
      "v_add_u32 v233, s81, v246\n"
      "v_mfma_f32_16x16x32_bf16 v[68:71], v[176:179], a[20:23], v[68:71]\n"
      "buffer_load_dwordx4 v213, s[64:67], s91 offen offset:1024 lds\n"
      "v_mfma_f32_16x16x32_bf16 v[72:75], v[176:179], a[24:27], v[72:75]\n"
      "ds_read_b64_tr_b16 v[144:145], v196 offset:0\n"
      "v_xor_b32 v237, v233, v247\n"
      "v_mfma_f32_16x16x32_bf16 v[76:79], v[176:179], a[28:31], v[76:79]\n"
      "s_and_b32 s84, s80, 1\n"
      "ds_read_b64_tr_b16 v[146:147], v196 offset:1024\n"
      "v_mfma_f32_16x16x32_bf16 v[80:83], v[180:183], a[16:19], v[80:83]\n"
      "buffer_load_dwordx4 v214, s[64:67], s91 offen offset:2048 lds\n"
      "v_mfma_f32_16x16x32_bf16 v[84:87], v[180:183], a[20:23], v[84:87]\n"
      "s_lshl_b32 s85, s84, 14\n"
      "ds_read_b64_tr_b16 v[148:149], v197 offset:0\n"
      "v_mfma_f32_16x16x32_bf16 v[88:91], v[180:183], a[24:27], v[88:91]\n"
      "ds_read_b64_tr_b16 v[150:151], v197 offset:1024\n"
      "v_and_b32 v244, 15, v254\n"
      "v_mfma_f32_16x16x32_bf16 v[92:95], v[180:183], a[28:31], v[92:95]\n"
      "buffer_load_dwordx4 v215, s[64:67], s91 offen offset:3072 lds\n"
      "v_mfma_f32_16x16x32_bf16 v[96:99], v[184:187], a[16:19], v[96:99]\n"
      "ds_read_b64_tr_b16 v[152:153], v198 offset:0\n"
      "v_and_b32 v245, 7, v254\n"
      "v_mfma_f32_16x16x32_bf16 v[100:103], v[184:187], a[20:23], v[100:103]\n"
      "ds_read_b64_tr_b16 v[154:155], v198 offset:1024\n"
      "v_lshlrev_b32 v245, 1, v245\n"
      "v_mfma_f32_16x16x32_bf16 v[104:107], v[184:187], a[24:27], v[104:107]\n"
      "ds_read_b64_tr_b16 v[156:157], v199 offset:0\n"
      "v_xor_b32 v245, v245, v252\n"
      "v_mfma_f32_16x16x32_bf16 v[108:111], v[184:187], a[28:31], v[108:111]\n"
      "ds_read_b64_tr_b16 v[158:159], v199 offset:1024\n"
      "v_lshlrev_b32 v245, 4, v245\n"
      "v_mfma_f32_16x16x32_bf16 v[112:115], v[188:191], a[16:19], v[112:115]\n"
      "ds_read_b128 a[64:67], v243 offset:256\n"
      "v_lshl_add_u32 v245, v244, 9, v245\n"
      "v_mfma_f32_16x16x32_bf16 v[116:119], v[188:191], a[20:23], v[116:119]\n"
      "s_add_u32 s91, s91, 262144\n"
      "ds_read_b128 a[68:71], v243 offset:14592\n"
      "v_mfma_f32_16x16x32_bf16 v[120:123], v[188:191], a[24:27], v[120:123]\n"
      "s_cmp_ge_u32 s91, 3670016\n"
      "s_cselect_b32 s91, 0, s91\n"
      "ds_read_b128 a[72:75], v243 offset:28928\n"
      "v_mfma_f32_16x16x32_bf16 v[124:127], v[188:191], a[28:31], v[124:127]\n"
      "s_waitcnt lgkmcnt(11)\n"
      "ds_read_b128 a[76:79], v243 offset:43264\n"
      "v_mfma_f32_16x16x32_bf16 v[0:3], v[128:131], a[32:35], v[0:3]\n"
      "s_waitcnt lgkmcnt(4)\n"
      "ds_read_b128 a[80:83], v253 offset:256\n"
      "v_add_u32 v218, s85, v245\n"
      "v_mfma_f32_16x16x32_bf16 v[4:7], v[128:131], a[36:39], v[4:7]\n"
      "s_add_u32 m0, s81, 0\n"
      "ds_read_b128 a[84:87], v253 offset:14592\n"
      "v_xor_b32 v219, 64, v218\n"
      "v_mfma_f32_16x16x32_bf16 v[8:11], v[128:131], a[40:43], v[8:11]\n"
      "buffer_load_dwordx4 v208, s[64:67], s91 offen offset:0 lds\n"
      "v_mfma_f32_16x16x32_bf16 v[12:15], v[128:131], a[44:47], v[12:15]\n"
      "s_waitcnt vmcnt(9)\n"
      "ds_read_b128 a[88:91], v253 offset:28928\n"
      "v_xor_b32 v220, 128, v218\n"
      "v_mfma_f32_16x16x32_bf16 v[16:19], v[132:135], a[32:35], v[16:19]\n"
      "ds_read_b128 a[92:95], v253 offset:43264\n"
      "v_xor_b32 v221, 192, v218\n"
      "v_mfma_f32_16x16x32_bf16 v[20:23], v[132:135], a[36:39], v[20:23]\n"
      "buffer_load_dwordx4 v209, s[64:67], s91 offen offset:1024 lds\n"
      "v_mfma_f32_16x16x32_bf16 v[24:27], v[132:135], a[40:43], v[24:27]\n"
      "s_lshl_b32 s85, s84, 8\n"
      "ds_read_b64_tr_b16 v[160:161], v200 offset:0\n"
      "v_mfma_f32_16x16x32_bf16 v[28:31], v[132:135], a[44:47], v[28:31]\n"
      "s_add_u32 s85, s85, 65536\n"
      "ds_read_b64_tr_b16 v[162:163], v200 offset:1024\n"
      "v_mfma_f32_16x16x32_bf16 v[32:35], v[136:139], a[32:35], v[32:35]\n"
      "buffer_load_dwordx4 v210, s[64:67], s91 offen offset:2048 lds\n"
      "v_mfma_f32_16x16x32_bf16 v[36:39], v[136:139], a[36:39], v[36:39]\n"
      "ds_read_b64_tr_b16 v[164:165], v201 offset:0\n"
      "v_lshrrev_b32 v244, 2, v254\n"
      "v_mfma_f32_16x16x32_bf16 v[40:43], v[136:139], a[40:43], v[40:43]\n"
      "ds_read_b64_tr_b16 v[166:167], v201 offset:1024\n"
      "v_and_b32 v245, 7, v244\n"
      "v_mfma_f32_16x16x32_bf16 v[44:47], v[136:139], a[44:47], v[44:47]\n"
      "buffer_load_dwordx4 v211, s[64:67], s91 offen offset:3072 lds\n"
      "v_mfma_f32_16x16x32_bf16 v[48:51], v[140:143], a[32:35], v[48:51]\n"
      "ds_read_b64_tr_b16 v[168:169], v202 offset:0\n"
      "v_lshlrev_b32 v245, 5, v245\n"
      "v_mfma_f32_16x16x32_bf16 v[52:55], v[140:143], a[36:39], v[52:55]\n"
      "s_add_u32 m0, s81, 4096\n"
      "ds_read_b64_tr_b16 v[170:171], v202 offset:1024\n"
      "v_and_b32 v246, 3, v254\n"
      "v_mfma_f32_16x16x32_bf16 v[56:59], v[140:143], a[40:43], v[56:59]\n"
      "buffer_load_dwordx4 v212, s[64:67], s91 offen offset:0 lds\n"
      "v_mfma_f32_16x16x32_bf16 v[60:63], v[140:143], a[44:47], v[60:63]\n"
      "ds_read_b64_tr_b16 v[172:173], v203 offset:0\n"
      "v_lshl_add_u32 v245, v246, 3, v245\n"
      "v_mfma_f32_16x16x32_bf16 v[64:67], v[144:147], a[32:35], v[64:67]\n"
      "ds_read_b64_tr_b16 v[174:175], v203 offset:1024\n"
      "v_lshl_add_u32 v246, v244, 9, v245\n"
      "v_mfma_f32_16x16x32_bf16 v[68:71], v[144:147], a[36:39], v[68:71]\n"
      "buffer_load_dwordx4 v213, s[64:67], s91 offen offset:1024 lds\n"
      "v_mfma_f32_16x16x32_bf16 v[72:75], v[144:147], a[40:43], v[72:75]\n"
      "ds_read_b64_tr_b16 v[176:177], v204 offset:0\n"
      "v_add_u32 v222, s85, v246\n"
      "v_mfma_f32_16x16x32_bf16 v[76:79], v[144:147], a[44:47], v[76:79]\n"
      "ds_read_b64_tr_b16 v[178:179], v204 offset:1024\n"
      "v_xor_b32 v223, 32, v222\n"
      "v_mfma_f32_16x16x32_bf16 v[80:83], v[148:151], a[32:35], v[80:83]\n"
      "buffer_load_dwordx4 v214, s[64:67], s91 offen offset:2048 lds\n"
      "v_mfma_f32_16x16x32_bf16 v[84:87], v[148:151], a[36:39], v[84:87]\n"
      "ds_read_b64_tr_b16 v[180:181], v205 offset:0\n"
      "v_xor_b32 v224, 64, v222\n"
      "v_mfma_f32_16x16x32_bf16 v[88:91], v[148:151], a[40:43], v[88:91]\n"
      "ds_read_b64_tr_b16 v[182:183], v205 offset:1024\n"
      "v_xor_b32 v225, 96, v222\n"
      "v_mfma_f32_16x16x32_bf16 v[92:95], v[148:151], a[44:47], v[92:95]\n"
      "buffer_load_dwordx4 v215, s[64:67], s91 offen offset:3072 lds\n"
      "v_mfma_f32_16x16x32_bf16 v[96:99], v[152:155], a[32:35], v[96:99]\n"
      "ds_read_b64_tr_b16 v[184:185], v206 offset:0\n"
      "v_xor_b32 v226, 128, v222\n"
      "v_mfma_f32_16x16x32_bf16 v[100:103], v[152:155], a[36:39], v[100:103]\n"
      "ds_read_b64_tr_b16 v[186:187], v206 offset:1024\n"
      "v_xor_b32 v227, 160, v222\n"
      "v_mfma_f32_16x16x32_bf16 v[104:107], v[152:155], a[40:43], v[104:107]\n"
      "ds_read_b64_tr_b16 v[188:189], v207 offset:0\n"
      "v_xor_b32 v228, 192, v222\n"
      "v_mfma_f32_16x16x32_bf16 v[108:111], v[152:155], a[44:47], v[108:111]\n"
      "ds_read_b64_tr_b16 v[190:191], v207 offset:1024\n"
      "v_xor_b32 v229, 224, v222\n"
      "v_mfma_f32_16x16x32_bf16 v[112:115], v[156:159], a[32:35], v[112:115]\n"
      "s_lshl_b32 s84, s80, 12\n"
      "ds_read_b128 a[96:99], v243 offset:384\n"
      "v_mfma_f32_16x16x32_bf16 v[116:119], v[156:159], a[36:39], v[116:119]\n"
      "s_add_u32 s91, s91, 262144\n"
      "ds_read_b128 a[100:103], v243 offset:14720\n"
      "v_mfma_f32_16x16x32_bf16 v[120:123], v[156:159], a[40:43], v[120:123]\n"
      "s_cmp_ge_u32 s91, 3670016\n"
      "s_cselect_b32 s91, 0, s91\n"
      "ds_read_b128 a[104:107], v243 offset:29056\n"
      "v_mfma_f32_16x16x32_bf16 v[124:127], v[156:159], a[44:47], v[124:127]\n"
      "s_waitcnt lgkmcnt(11)\n"
      "ds_read_b128 a[108:111], v243 offset:43392\n"
      "v_mfma_f32_16x16x32_bf16 v[0:3], v[160:163], a[48:51], v[0:3]\n"
      "s_waitcnt lgkmcnt(4)\n"
      "ds_read_b128 a[112:115], v253 offset:384\n"
      "v_mfma_f32_16x16x32_bf16 v[4:7], v[160:163], a[52:55], v[4:7]\n"
      "s_add_u32 m0, s81, 98304\n"
      "ds_read_b128 a[116:119], v253 offset:14720\n"
      "v_mfma_f32_16x16x32_bf16 v[8:11], v[160:163], a[56:59], v[8:11]\n"
      "buffer_load_dwordx4 v208, s[64:67], s91 offen offset:0 lds\n"
      "v_mfma_f32_16x16x32_bf16 v[12:15], v[160:163], a[60:63], v[12:15]\n"
      "s_waitcnt vmcnt(9)\n"
      "ds_read_b128 a[120:123], v253 offset:29056\n"
      "v_mfma_f32_16x16x32_bf16 v[16:19], v[164:167], a[48:51], v[16:19]\n"
      "s_add_u32 s85, s84, 131072\n"
      "ds_read_b128 a[124:127], v253 offset:43392\n"
      "v_mfma_f32_16x16x32_bf16 v[20:23], v[164:167], a[52:55], v[20:23]\n"
      "buffer_load_dwordx4 v209, s[64:67], s91 offen offset:1024 lds\n"
      "v_mfma_f32_16x16x32_bf16 v[24:27], v[164:167], a[56:59], v[24:27]\n"
      "ds_read_b64_tr_b16 v[128:129], v200 offset:32768\n"
      "v_add_u32 v238, s85, v242\n"
      "v_mfma_f32_16x16x32_bf16 v[28:31], v[164:167], a[60:63], v[28:31]\n"
      "ds_read_b64_tr_b16 v[130:131], v200 offset:33792\n"
      "v_xor_b32 v239, 4096, v238\n"
      "v_mfma_f32_16x16x32_bf16 v[32:35], v[168:171], a[48:51], v[32:35]\n"
      "buffer_load_dwordx4 v210, s[64:67], s91 offen offset:2048 lds\n"
      "v_mfma_f32_16x16x32_bf16 v[36:39], v[168:171], a[52:55], v[36:39]\n"
      "s_lshr_b32 s85, s80, 1\n"
      "ds_read_b64_tr_b16 v[132:133], v201 offset:32768\n"
      "v_mfma_f32_16x16x32_bf16 v[40:43], v[168:171], a[56:59], v[40:43]\n"
      "s_lshl_b32 s85, s85, 3\n"
      "ds_read_b64_tr_b16 v[134:135], v201 offset:33792\n"
      "v_mfma_f32_16x16x32_bf16 v[44:47], v[168:171], a[60:63], v[44:47]\n"
      "buffer_load_dwordx4 v211, s[64:67], s91 offen offset:3072 lds\n"
      "v_mfma_f32_16x16x32_bf16 v[48:51], v[172:175], a[48:51], v[48:51]\n"
      "s_add_u32 s85, s85, 147456\n"
      "ds_read_b64_tr_b16 v[136:137], v202 offset:32768\n"
      "v_mfma_f32_16x16x32_bf16 v[52:55], v[172:175], a[52:55], v[52:55]\n"
      "s_add_u32 m0, s81, 102400\n"
      "ds_read_b64_tr_b16 v[138:139], v202 offset:33792\n"
      "v_mov_b32 v241, s85\n"
      "v_mfma_f32_16x16x32_bf16 v[56:59], v[172:175], a[56:59], v[56:59]\n"
      "buffer_load_dwordx4 v212, s[64:67], s91 offen offset:0 lds\n"
      "v_mfma_f32_16x16x32_bf16 v[60:63], v[172:175], a[60:63], v[60:63]\n"
      "s_lshl_b32 s99, s80, 11\n"
      "ds_read_b64_tr_b16 v[140:141], v203 offset:32768\n"
      "v_mfma_f32_16x16x32_bf16 v[64:67], v[176:179], a[48:51], v[64:67]\n"
      "s_xor_b32 s97, s99, 2048\n"
      "ds_read_b64_tr_b16 v[142:143], v203 offset:33792\n"
      "v_mfma_f32_16x16x32_bf16 v[68:71], v[176:179], a[52:55], v[68:71]\n"
      "buffer_load_dwordx4 v213, s[64:67], s91 offen offset:1024 lds\n"
      "v_mfma_f32_16x16x32_bf16 v[72:75], v[176:179], a[56:59], v[72:75]\n"
      "s_add_u32 s99, s99, 148480\n"
      "ds_read_b64_tr_b16 v[144:145], v204 offset:32768\n"
      "v_mfma_f32_16x16x32_bf16 v[76:79], v[176:179], a[60:63], v[76:79]\n"
      "s_add_u32 s97, s97, 148480\n"
      "ds_read_b64_tr_b16 v[146:147], v204 offset:33792\n"
      "v_mfma_f32_16x16x32_bf16 v[80:83], v[180:183], a[48:51], v[80:83]\n"
      "buffer_load_dwordx4 v214, s[64:67], s91 offen offset:2048 lds\n"
      "v_mfma_f32_16x16x32_bf16 v[84:87], v[180:183], a[52:55], v[84:87]\n"
      "s_and_b32 s73, s73, 0xffff\n"
      "ds_read_b64_tr_b16 v[148:149], v205 offset:32768\n"
      "v_mfma_f32_16x16x32_bf16 v[88:91], v[180:183], a[56:59], v[88:91]\n"
      "s_cmp_lt_u32 s92, 24\n"
      "s_cselect_b32 s74, 1605632, 0\n"
      "ds_read_b64_tr_b16 v[150:151], v205 offset:33792\n"
      "v_mfma_f32_16x16x32_bf16 v[92:95], v[180:183], a[60:63], v[92:95]\n"
      "buffer_load_dwordx4 v215, s[64:67], s91 offen offset:3072 lds\n"
      "v_mfma_f32_16x16x32_bf16 v[96:99], v[184:187], a[48:51], v[96:99]\n"
      "s_mul_i32 s84, s93, 448\n"
      "ds_read_b64_tr_b16 v[152:153], v206 offset:32768\n"
      "v_mfma_f32_16x16x32_bf16 v[100:103], v[184:187], a[52:55], v[100:103]\n"
      "s_andn2_b32 s84, s84, 127\n"
      "ds_read_b64_tr_b16 v[154:155], v206 offset:33792\n"
      "v_mfma_f32_16x16x32_bf16 v[104:107], v[184:187], a[56:59], v[104:107]\n"
      "s_lshl_b32 s85, s93, 7\n"
      "ds_read_b64_tr_b16 v[156:157], v207 offset:32768\n"
      "v_mfma_f32_16x16x32_bf16 v[108:111], v[184:187], a[60:63], v[108:111]\n"
      "s_cmp_lt_u32 s92, 16\n"
      "s_cselect_b32 s76, s84, s85\n"
      "s_cselect_b32 s85, 4294966528, 0\n"
      "ds_read_b64_tr_b16 v[158:159], v207 offset:33792\n"
      "v_mfma_f32_16x16x32_bf16 v[112:115], v[188:191], a[48:51], v[112:115]\n"
      "s_and_b32 s84, s92, 7\n"
      "ds_read_b128 a[128:131], v243 offset:512\n"
      "v_mfma_f32_16x16x32_bf16 v[116:119], v[188:191], a[52:55], v[116:119]\n"
      "s_add_u32 s91, s91, 262144\n"
      "ds_read_b128 a[132:135], v243 offset:14848\n"
      "v_mfma_f32_16x16x32_bf16 v[120:123], v[188:191], a[56:59], v[120:123]\n"
      "s_cmp_ge_u32 s91, 3670016\n"
      "s_cselect_b32 s91, 0, s91\n"
      "ds_read_b128 a[136:139], v243 offset:29184\n"
      "v_mfma_f32_16x16x32_bf16 v[124:127], v[188:191], a[60:63], v[124:127]\n"
      "s_waitcnt lgkmcnt(11)\n"
      "ds_read_b128 a[140:143], v243 offset:43520\n"
      "v_mfma_f32_16x16x32_bf16 v[0:3], v[128:131], a[64:67], v[0:3]\n"
      "s_waitcnt lgkmcnt(4)\n"
      "ds_read_b128 a[144:147], v253 offset:512\n"
      "v_lshl_add_u32 v244, s84, 4, v254\n"
      "v_mfma_f32_16x16x32_bf16 v[4:7], v[128:131], a[68:71], v[4:7]\n"
      "s_add_u32 m0, s81, 131072\n"
      "ds_read_b128 a[148:151], v253 offset:14848\n"
      "v_mul_u32_u24 v244, 896, v244\n"
      "v_mfma_f32_16x16x32_bf16 v[8:11], v[128:131], a[72:75], v[8:11]\n"
      "buffer_load_dwordx4 v208, s[64:67], s91 offen offset:0 lds\n"
      "v_mfma_f32_16x16x32_bf16 v[12:15], v[128:131], a[76:79], v[12:15]\n"
      "s_waitcnt vmcnt(9)\n"
      "ds_read_b128 a[152:155], v253 offset:29184\n"
      "v_and_b32 v245, 3, v254\n"
      "v_mfma_f32_16x16x32_bf16 v[16:19], v[132:135], a[64:67], v[16:19]\n"
      "ds_read_b128 a[156:159], v253 offset:43520\n"
      "v_mul_lo_u32 v245, v245, s85\n"
      "v_mfma_f32_16x16x32_bf16 v[20:23], v[132:135], a[68:71], v[20:23]\n"
      "buffer_load_dwordx4 v209, s[64:67], s91 offen offset:1024 lds\n"
      "v_mfma_f32_16x16x32_bf16 v[24:27], v[132:135], a[72:75], v[24:27]\n"
      "ds_read_b64_tr_b16 v[160:161], v192 offset:0\n"
      "v_add_u32 v244, v244, v245\n"
      "v_mfma_f32_16x16x32_bf16 v[28:31], v[132:135], a[76:79], v[28:31]\n"
      "s_mov_b32 s77, 114688\n"
      "ds_read_b64_tr_b16 v[162:163], v192 offset:1024\n"
      "v_mfma_f32_16x16x32_bf16 v[32:35], v[136:139], a[64:67], v[32:35]\n"
      "buffer_load_dwordx4 v210, s[64:67], s91 offen offset:2048 lds\n"
      "v_mfma_f32_16x16x32_bf16 v[36:39], v[136:139], a[68:71], v[36:39]\n"
      "s_cmp_eq_u32 s80, 2\n"
      "s_cselect_b64 s[72:73], s[68:69], s[72:73]\n"
      "s_cselect_b32 s74, s70, s74\n"
      "s_cselect_b32 s84, 3, 4\n"
      "ds_read_b64_tr_b16 v[164:165], v193 offset:0\n"
      "v_mfma_f32_16x16x32_bf16 v[40:43], v[136:139], a[72:75], v[40:43]\n"
      "s_cmp_eq_u32 s80, 2\n"
      "s_cselect_b32 s76, 0, s76\n"
      "s_cselect_b32 s77, 32768, s77\n"
      "ds_read_b64_tr_b16 v[166:167], v193 offset:1024\n"
      "v_mfma_f32_16x16x32_bf16 v[44:47], v[136:139], a[76:79], v[44:47]\n"
      "buffer_load_dwordx4 v211, s[64:67], s91 offen offset:3072 lds\n"
      "v_mfma_f32_16x16x32_bf16 v[48:51], v[140:143], a[64:67], v[48:51]\n"
      "s_cmp_eq_u32 s80, 1\n"
      "s_cselect_b64 s[72:73], s[60:61], s[72:73]\n"
      "ds_read_b64_tr_b16 v[168:169], v194 offset:0\n"
      "v_mfma_f32_16x16x32_bf16 v[52:55], v[140:143], a[68:71], v[52:55]\n"
      "s_add_u32 m0, s81, 135168\n"
      "ds_read_b64_tr_b16 v[170:171], v194 offset:1024\n"
      "v_mfma_f32_16x16x32_bf16 v[56:59], v[140:143], a[72:75], v[56:59]\n"
      "buffer_load_dwordx4 v212, s[64:67], s91 offen offset:0 lds\n"
      "v_mfma_f32_16x16x32_bf16 v[60:63], v[140:143], a[76:79], v[60:63]\n"
      "s_cmp_eq_u32 s80, 0\n"
      "s_cselect_b64 s[72:73], s[56:57], s[72:73]\n"
      "ds_read_b64_tr_b16 v[172:173], v195 offset:0\n"
      "v_mfma_f32_16x16x32_bf16 v[64:67], v[144:147], a[64:67], v[64:67]\n"
      "s_cmp_lt_u32 s80, 2\n"
      "s_cselect_b32 s74, s58, s74\n"
      "s_cselect_b32 s76, 163840, s76\n"
      "s_cselect_b32 s77, 65536, s77\n"
      "ds_read_b64_tr_b16 v[174:175], v195 offset:1024\n"
      "v_mfma_f32_16x16x32_bf16 v[68:71], v[144:147], a[68:71], v[68:71]\n"
      "buffer_load_dwordx4 v213, s[64:67], s91 offen offset:1024 lds\n"
      "v_mfma_f32_16x16x32_bf16 v[72:75], v[144:147], a[72:75], v[72:75]\n"
      "s_lshl_b32 s85, s92, s84\n"
      "ds_read_b64_tr_b16 v[176:177], v196 offset:0\n"
      "v_mfma_f32_16x16x32_bf16 v[76:79], v[144:147], a[76:79], v[76:79]\n"
      "ds_read_b64_tr_b16 v[178:179], v196 offset:1024\n"
      "v_add_lshl_u32 v245, s85, v254, 7\n"
      "v_mfma_f32_16x16x32_bf16 v[80:83], v[148:151], a[64:67], v[80:83]\n"
      "buffer_load_dwordx4 v214, s[64:67], s91 offen offset:2048 lds\n"
      "v_mfma_f32_16x16x32_bf16 v[84:87], v[148:151], a[68:71], v[84:87]\n"
      "s_cmp_eq_u32 s80, 3\n"
      "s_cselect_b64 vcc, -1, 0\n"
      "ds_read_b64_tr_b16 v[180:181], v197 offset:0\n"
      "v_mfma_f32_16x16x32_bf16 v[88:91], v[148:151], a[72:75], v[88:91]\n"
      "ds_read_b64_tr_b16 v[182:183], v197 offset:1024\n"
      "v_lshrrev_b32 v246, s84, v254\n"
      "v_mfma_f32_16x16x32_bf16 v[92:95], v[148:151], a[76:79], v[92:95]\n"
      "buffer_load_dwordx4 v215, s[64:67], s91 offen offset:3072 lds\n"
      "v_mfma_f32_16x16x32_bf16 v[96:99], v[152:155], a[64:67], v[96:99]\n"
      "ds_read_b64_tr_b16 v[184:185], v198 offset:0\n"
      "v_cndmask_b32 v244, v245, v244, vcc\n"
      "v_mfma_f32_16x16x32_bf16 v[100:103], v[152:155], a[68:71], v[100:103]\n"
      "ds_read_b64_tr_b16 v[186:187], v198 offset:1024\n"
      "v_min_u32 v246, 1, v246\n"
      "v_mfma_f32_16x16x32_bf16 v[104:107], v[152:155], a[72:75], v[104:107]\n"
      "ds_read_b64_tr_b16 v[188:189], v199 offset:0\n"
      "v_lshl_add_u32 v255, v246, 30, v244\n"
      "v_mfma_f32_16x16x32_bf16 v[108:111], v[152:155], a[76:79], v[108:111]\n"
      "ds_read_b64_tr_b16 v[190:191], v199 offset:1024\n"
      "v_mfma_f32_16x16x32_bf16 v[112:115], v[156:159], a[64:67], v[112:115]\n"
      "ds_read_b128 a[160:163], v243 offset:640\n"
      "v_mfma_f32_16x16x32_bf16 v[116:119], v[156:159], a[68:71], v[116:119]\n"
      "s_add_u32 s91, s91, 262144\n"
      "ds_read_b128 a[164:167], v243 offset:14976\n"
      "v_mfma_f32_16x16x32_bf16 v[120:123], v[156:159], a[72:75], v[120:123]\n"
      "s_cmp_ge_u32 s91, 3670016\n"
      "s_cselect_b32 s91, 0, s91\n"
      "ds_read_b128 a[168:171], v243 offset:29312\n"
      "v_mfma_f32_16x16x32_bf16 v[124:127], v[156:159], a[76:79], v[124:127]\n"
      "s_waitcnt lgkmcnt(11)\n"
      "ds_read_b128 a[172:175], v243 offset:43648\n"
      "v_mfma_f32_16x16x32_bf16 v[0:3], v[160:163], a[80:83], v[0:3]\n"
      "s_waitcnt lgkmcnt(4)\n"
      "ds_read_b128 a[176:179], v253 offset:640\n"
      "v_mfma_f32_16x16x32_bf16 v[4:7], v[160:163], a[84:87], v[4:7]\n"
      "s_add_u32 m0, s81, 0\n"
      "ds_read_b128 a[180:183], v253 offset:14976\n"
      "v_mfma_f32_16x16x32_bf16 v[8:11], v[160:163], a[88:91], v[8:11]\n"
      "buffer_load_dwordx4 v208, s[64:67], s91 offen offset:0 lds\n"
      "v_mfma_f32_16x16x32_bf16 v[12:15], v[160:163], a[92:95], v[12:15]\n"
      "s_waitcnt vmcnt(9)\n"
      "ds_read_b128 a[184:187], v253 offset:29312\n"
      "v_mfma_f32_16x16x32_bf16 v[16:19], v[164:167], a[80:83], v[16:19]\n"
      "ds_read_b128 a[188:191], v253 offset:43648\n"
      "v_mfma_f32_16x16x32_bf16 v[20:23], v[164:167], a[84:87], v[20:23]\n"
      "buffer_load_dwordx4 v209, s[64:67], s91 offen offset:1024 lds\n"
      "v_mfma_f32_16x16x32_bf16 v[24:27], v[164:167], a[88:91], v[24:27]\n"
      "ds_read_b64_tr_b16 v[128:129], v200 offset:0\n"
      "v_mfma_f32_16x16x32_bf16 v[28:31], v[164:167], a[92:95], v[28:31]\n"
      "ds_read_b64_tr_b16 v[130:131], v200 offset:1024\n"
      "v_mfma_f32_16x16x32_bf16 v[32:35], v[168:171], a[80:83], v[32:35]\n"
      "buffer_load_dwordx4 v210, s[64:67], s91 offen offset:2048 lds\n"
      "v_mfma_f32_16x16x32_bf16 v[36:39], v[168:171], a[84:87], v[36:39]\n"
      "ds_read_b64_tr_b16 v[132:133], v201 offset:0\n"
      "v_mfma_f32_16x16x32_bf16 v[40:43], v[168:171], a[88:91], v[40:43]\n"
      "ds_read_b64_tr_b16 v[134:135], v201 offset:1024\n"
      "v_mfma_f32_16x16x32_bf16 v[44:47], v[168:171], a[92:95], v[44:47]\n"
      "buffer_load_dwordx4 v211, s[64:67], s91 offen offset:3072 lds\n"
      "v_mfma_f32_16x16x32_bf16 v[48:51], v[172:175], a[80:83], v[48:51]\n"
      "ds_read_b64_tr_b16 v[136:137], v202 offset:0\n"
      "v_mfma_f32_16x16x32_bf16 v[52:55], v[172:175], a[84:87], v[52:55]\n"
      "s_add_u32 m0, s81, 4096\n"
      "ds_read_b64_tr_b16 v[138:139], v202 offset:1024\n"
      "v_mfma_f32_16x16x32_bf16 v[56:59], v[172:175], a[88:91], v[56:59]\n"
      "buffer_load_dwordx4 v212, s[64:67], s91 offen offset:0 lds\n"
      "v_mfma_f32_16x16x32_bf16 v[60:63], v[172:175], a[92:95], v[60:63]\n"
      "ds_read_b64_tr_b16 v[140:141], v203 offset:0\n"
      "v_mfma_f32_16x16x32_bf16 v[64:67], v[176:179], a[80:83], v[64:67]\n"
      "ds_read_b64_tr_b16 v[142:143], v203 offset:1024\n"
      "v_mfma_f32_16x16x32_bf16 v[68:71], v[176:179], a[84:87], v[68:71]\n"
      "buffer_load_dwordx4 v213, s[64:67], s91 offen offset:1024 lds\n"
      "v_mfma_f32_16x16x32_bf16 v[72:75], v[176:179], a[88:91], v[72:75]\n"
      "ds_read_b64_tr_b16 v[144:145], v204 offset:0\n"
      "v_mfma_f32_16x16x32_bf16 v[76:79], v[176:179], a[92:95], v[76:79]\n"
      "ds_read_b64_tr_b16 v[146:147], v204 offset:1024\n"
      "v_mfma_f32_16x16x32_bf16 v[80:83], v[180:183], a[80:83], v[80:83]\n"
      "buffer_load_dwordx4 v214, s[64:67], s91 offen offset:2048 lds\n"
      "v_mfma_f32_16x16x32_bf16 v[84:87], v[180:183], a[84:87], v[84:87]\n"
      "ds_read_b64_tr_b16 v[148:149], v205 offset:0\n"
      "v_mfma_f32_16x16x32_bf16 v[88:91], v[180:183], a[88:91], v[88:91]\n"
      "ds_read_b64_tr_b16 v[150:151], v205 offset:1024\n"
      "v_mfma_f32_16x16x32_bf16 v[92:95], v[180:183], a[92:95], v[92:95]\n"
      "buffer_load_dwordx4 v215, s[64:67], s91 offen offset:3072 lds\n"
      "v_mfma_f32_16x16x32_bf16 v[96:99], v[184:187], a[80:83], v[96:99]\n"
      "ds_read_b64_tr_b16 v[152:153], v206 offset:0\n"
      "v_mfma_f32_16x16x32_bf16 v[100:103], v[184:187], a[84:87], v[100:103]\n"
      "ds_read_b64_tr_b16 v[154:155], v206 offset:1024\n"
      "v_mfma_f32_16x16x32_bf16 v[104:107], v[184:187], a[88:91], v[104:107]\n"
      "ds_read_b64_tr_b16 v[156:157], v207 offset:0\n"
      "v_mfma_f32_16x16x32_bf16 v[108:111], v[184:187], a[92:95], v[108:111]\n"
      "ds_read_b64_tr_b16 v[158:159], v207 offset:1024\n"
      "v_mfma_f32_16x16x32_bf16 v[112:115], v[188:191], a[80:83], v[112:115]\n"
      "ds_read_b128 a[192:195], v243 offset:768\n"
      "v_mfma_f32_16x16x32_bf16 v[116:119], v[188:191], a[84:87], v[116:119]\n"
      "s_add_u32 s91, s91, 262144\n"
      "ds_read_b128 a[196:199], v243 offset:15104\n"
      "v_mfma_f32_16x16x32_bf16 v[120:123], v[188:191], a[88:91], v[120:123]\n"
      "s_cmp_ge_u32 s91, 3670016\n"
      "s_cselect_b32 s91, 0, s91\n"
      "ds_read_b128 a[200:203], v243 offset:29440\n"
      "v_mfma_f32_16x16x32_bf16 v[124:127], v[188:191], a[92:95], v[124:127]\n"
      "s_waitcnt lgkmcnt(11)\n"
      "ds_read_b128 a[204:207], v243 offset:43776\n"
      "v_mfma_f32_16x16x32_bf16 v[0:3], v[128:131], a[96:99], v[0:3]\n"
      "s_waitcnt lgkmcnt(4)\n"
      "ds_read_b128 a[208:211], v253 offset:768\n"
      "v_mfma_f32_16x16x32_bf16 v[4:7], v[128:131], a[100:103], v[4:7]\n"
      "s_add_u32 m0, s81, 98304\n"
      "ds_read_b128 a[212:215], v253 offset:15104\n"
      "v_mfma_f32_16x16x32_bf16 v[8:11], v[128:131], a[104:107], v[8:11]\n"
      "buffer_load_dwordx4 v208, s[64:67], s91 offen offset:0 lds\n"
      "v_mfma_f32_16x16x32_bf16 v[12:15], v[128:131], a[108:111], v[12:15]\n"
      "s_waitcnt vmcnt(9)\n"
      "ds_read_b128 a[216:219], v253 offset:29440\n"
      "v_mfma_f32_16x16x32_bf16 v[16:19], v[132:135], a[96:99], v[16:19]\n"
      "ds_read_b128 a[220:223], v253 offset:43776\n"
      "v_mfma_f32_16x16x32_bf16 v[20:23], v[132:135], a[100:103], v[20:23]\n"
      "buffer_load_dwordx4 v209, s[64:67], s91 offen offset:1024 lds\n"
      "v_mfma_f32_16x16x32_bf16 v[24:27], v[132:135], a[104:107], v[24:27]\n"
      "ds_read_b64_tr_b16 v[160:161], v200 offset:32768\n"
      "v_mfma_f32_16x16x32_bf16 v[28:31], v[132:135], a[108:111], v[28:31]\n"
      "ds_read_b64_tr_b16 v[162:163], v200 offset:33792\n"
      "v_mfma_f32_16x16x32_bf16 v[32:35], v[136:139], a[96:99], v[32:35]\n"
      "buffer_load_dwordx4 v210, s[64:67], s91 offen offset:2048 lds\n"
      "v_mfma_f32_16x16x32_bf16 v[36:39], v[136:139], a[100:103], v[36:39]\n"
      "ds_read_b64_tr_b16 v[164:165], v201 offset:32768\n"
      "v_mfma_f32_16x16x32_bf16 v[40:43], v[136:139], a[104:107], v[40:43]\n"
      "ds_read_b64_tr_b16 v[166:167], v201 offset:33792\n"
      "v_mfma_f32_16x16x32_bf16 v[44:47], v[136:139], a[108:111], v[44:47]\n"
      "buffer_load_dwordx4 v211, s[64:67], s91 offen offset:3072 lds\n"
      "v_mfma_f32_16x16x32_bf16 v[48:51], v[140:143], a[96:99], v[48:51]\n"
      "ds_read_b64_tr_b16 v[168:169], v202 offset:32768\n"
      "v_mfma_f32_16x16x32_bf16 v[52:55], v[140:143], a[100:103], v[52:55]\n"
      "s_add_u32 m0, s81, 102400\n"
      "ds_read_b64_tr_b16 v[170:171], v202 offset:33792\n"
      "v_mfma_f32_16x16x32_bf16 v[56:59], v[140:143], a[104:107], v[56:59]\n"
      "buffer_load_dwordx4 v212, s[64:67], s91 offen offset:0 lds\n"
      "v_mfma_f32_16x16x32_bf16 v[60:63], v[140:143], a[108:111], v[60:63]\n"
      "ds_read_b64_tr_b16 v[172:173], v203 offset:32768\n"
      "v_mfma_f32_16x16x32_bf16 v[64:67], v[144:147], a[96:99], v[64:67]\n"
      "ds_read_b64_tr_b16 v[174:175], v203 offset:33792\n"
      "v_mfma_f32_16x16x32_bf16 v[68:71], v[144:147], a[100:103], v[68:71]\n"
      "buffer_load_dwordx4 v213, s[64:67], s91 offen offset:1024 lds\n"
      "v_mfma_f32_16x16x32_bf16 v[72:75], v[144:147], a[104:107], v[72:75]\n"
      "ds_read_b64_tr_b16 v[176:177], v204 offset:32768\n"
      "v_mfma_f32_16x16x32_bf16 v[76:79], v[144:147], a[108:111], v[76:79]\n"
      "ds_read_b64_tr_b16 v[178:179], v204 offset:33792\n"
      "v_mfma_f32_16x16x32_bf16 v[80:83], v[148:151], a[96:99], v[80:83]\n"
      "buffer_load_dwordx4 v214, s[64:67], s91 offen offset:2048 lds\n"
      "v_mfma_f32_16x16x32_bf16 v[84:87], v[148:151], a[100:103], v[84:87]\n"
      "ds_read_b64_tr_b16 v[180:181], v205 offset:32768\n"
      "v_mfma_f32_16x16x32_bf16 v[88:91], v[148:151], a[104:107], v[88:91]\n"
      "ds_read_b64_tr_b16 v[182:183], v205 offset:33792\n"
      "v_mfma_f32_16x16x32_bf16 v[92:95], v[148:151], a[108:111], v[92:95]\n"
      "buffer_load_dwordx4 v215, s[64:67], s91 offen offset:3072 lds\n"
      "v_mfma_f32_16x16x32_bf16 v[96:99], v[152:155], a[96:99], v[96:99]\n"
      "ds_read_b64_tr_b16 v[184:185], v206 offset:32768\n"
      "v_mfma_f32_16x16x32_bf16 v[100:103], v[152:155], a[100:103], v[100:103]\n"
      "ds_read_b64_tr_b16 v[186:187], v206 offset:33792\n"
      "v_mfma_f32_16x16x32_bf16 v[104:107], v[152:155], a[104:107], v[104:107]\n"
      "ds_read_b64_tr_b16 v[188:189], v207 offset:32768\n"
      "v_mfma_f32_16x16x32_bf16 v[108:111], v[152:155], a[108:111], v[108:111]\n"
      "ds_read_b64_tr_b16 v[190:191], v207 offset:33792\n"
      "v_mfma_f32_16x16x32_bf16 v[112:115], v[156:159], a[96:99], v[112:115]\n"
      "v_mfma_f32_16x16x32_bf16 v[116:119], v[156:159], a[100:103], v[116:119]\n"
      "s_add_u32 s91, s91, 262144\n"
      "v_mfma_f32_16x16x32_bf16 v[120:123], v[156:159], a[104:107], v[120:123]\n"
      "s_cmp_ge_u32 s91, 3670016\n"
      "s_cselect_b32 s91, 0, s91\n"
      "v_mfma_f32_16x16x32_bf16 v[124:127], v[156:159], a[108:111], v[124:127]\n"
      "s_waitcnt lgkmcnt(8)\n"
      "v_mfma_f32_16x16x32_bf16 v[0:3], v[160:163], a[112:115], v[0:3]\n"
      "s_waitcnt lgkmcnt(0)\n"
      "v_mfma_f32_16x16x32_bf16 v[4:7], v[160:163], a[116:119], v[4:7]\n"
      "s_add_u32 m0, s81, 131072\n"
      "v_mfma_f32_16x16x32_bf16 v[8:11], v[160:163], a[120:123], v[8:11]\n"
      "buffer_load_dwordx4 v208, s[64:67], s91 offen offset:0 lds\n"
      "v_mfma_f32_16x16x32_bf16 v[12:15], v[160:163], a[124:127], v[12:15]\n"
      "s_waitcnt vmcnt(9)\n"
      "ds_read_b64_tr_b16 v[128:129], v192 offset:0\n"
      "v_mfma_f32_16x16x32_bf16 v[16:19], v[164:167], a[112:115], v[16:19]\n"
      "ds_read_b64_tr_b16 v[130:131], v192 offset:1024\n"
      "v_mfma_f32_16x16x32_bf16 v[20:23], v[164:167], a[116:119], v[20:23]\n"
      "buffer_load_dwordx4 v209, s[64:67], s91 offen offset:1024 lds\n"
      "v_mfma_f32_16x16x32_bf16 v[24:27], v[164:167], a[120:123], v[24:27]\n"
      "ds_read_b64_tr_b16 v[132:133], v193 offset:0\n"
      "v_mfma_f32_16x16x32_bf16 v[28:31], v[164:167], a[124:127], v[28:31]\n"
      "ds_read_b64_tr_b16 v[134:135], v193 offset:1024\n"
      "v_mfma_f32_16x16x32_bf16 v[32:35], v[168:171], a[112:115], v[32:35]\n"
      "buffer_load_dwordx4 v210, s[64:67], s91 offen offset:2048 lds\n"
      "v_mfma_f32_16x16x32_bf16 v[36:39], v[168:171], a[116:119], v[36:39]\n"
      "ds_read_b64_tr_b16 v[136:137], v194 offset:0\n"
      "v_mfma_f32_16x16x32_bf16 v[40:43], v[168:171], a[120:123], v[40:43]\n"
      "v_mfma_f32_16x16x32_bf16 v[44:47], v[168:171], a[124:127], v[44:47]\n"
      "buffer_load_dwordx4 v211, s[64:67], s91 offen offset:3072 lds\n"
      "v_mfma_f32_16x16x32_bf16 v[48:51], v[172:175], a[112:115], v[48:51]\n"
      "ds_read_b64_tr_b16 v[138:139], v194 offset:1024\n"
      "v_mfma_f32_16x16x32_bf16 v[52:55], v[172:175], a[116:119], v[52:55]\n"
      "s_add_u32 m0, s81, 135168\n"
      "ds_read_b64_tr_b16 v[140:141], v195 offset:0\n"
      "v_mfma_f32_16x16x32_bf16 v[56:59], v[172:175], a[120:123], v[56:59]\n"
      "buffer_load_dwordx4 v212, s[64:67], s91 offen offset:0 lds\n"
      "v_mfma_f32_16x16x32_bf16 v[60:63], v[172:175], a[124:127], v[60:63]\n"
      "ds_read_b64_tr_b16 v[142:143], v195 offset:1024\n"
      "v_mfma_f32_16x16x32_bf16 v[64:67], v[176:179], a[112:115], v[64:67]\n"
      "ds_read_b64_tr_b16 v[144:145], v196 offset:0\n"
      "v_mfma_f32_16x16x32_bf16 v[68:71], v[176:179], a[116:119], v[68:71]\n"
      "buffer_load_dwordx4 v213, s[64:67], s91 offen offset:1024 lds\n"
      "v_mfma_f32_16x16x32_bf16 v[72:75], v[176:179], a[120:123], v[72:75]\n"
      "ds_read_b64_tr_b16 v[146:147], v196 offset:1024\n"
      "v_mfma_f32_16x16x32_bf16 v[76:79], v[176:179], a[124:127], v[76:79]\n"
      "ds_read_b64_tr_b16 v[148:149], v197 offset:0\n"
      "v_mfma_f32_16x16x32_bf16 v[80:83], v[180:183], a[112:115], v[80:83]\n"
      "buffer_load_dwordx4 v214, s[64:67], s91 offen offset:2048 lds\n"
      "v_mfma_f32_16x16x32_bf16 v[84:87], v[180:183], a[116:119], v[84:87]\n"
      "ds_read_b64_tr_b16 v[150:151], v197 offset:1024\n"
      "v_mfma_f32_16x16x32_bf16 v[88:91], v[180:183], a[120:123], v[88:91]\n"
      "ds_read_b64_tr_b16 v[152:153], v198 offset:0\n"
      "v_mfma_f32_16x16x32_bf16 v[92:95], v[180:183], a[124:127], v[92:95]\n"
      "buffer_load_dwordx4 v215, s[64:67], s91 offen offset:3072 lds\n"
      "v_mfma_f32_16x16x32_bf16 v[96:99], v[184:187], a[112:115], v[96:99]\n"
      "ds_read_b64_tr_b16 v[154:155], v198 offset:1024\n"
      "v_mfma_f32_16x16x32_bf16 v[100:103], v[184:187], a[116:119], v[100:103]\n"
      "ds_read_b64_tr_b16 v[156:157], v199 offset:0\n"
      "v_mfma_f32_16x16x32_bf16 v[104:107], v[184:187], a[120:123], v[104:107]\n"
      "v_mfma_f32_16x16x32_bf16 v[108:111], v[184:187], a[124:127], v[108:111]\n"
      "ds_read_b64_tr_b16 v[158:159], v199 offset:1024\n"
      "v_mfma_f32_16x16x32_bf16 v[112:115], v[188:191], a[112:115], v[112:115]\n"
      "v_mfma_f32_16x16x32_bf16 v[116:119], v[188:191], a[116:119], v[116:119]\n"
      "s_add_u32 s91, s91, 262144\n"
      "v_mfma_f32_16x16x32_bf16 v[120:123], v[188:191], a[120:123], v[120:123]\n"
      "s_cmp_ge_u32 s91, 3670016\n"
      "s_cselect_b32 s91, 0, s91\n"
      "v_mfma_f32_16x16x32_bf16 v[124:127], v[188:191], a[124:127], v[124:127]\n"
      "s_waitcnt lgkmcnt(0)\n"
      "v_mfma_f32_16x16x32_bf16 v[0:3], v[128:131], a[128:131], v[0:3]\n"
      "s_barrier\n"
      "v_mfma_f32_16x16x32_bf16 v[4:7], v[128:131], a[132:135], v[4:7]\n"
      "s_add_u32 m0, s81, 0\n"
      "v_mfma_f32_16x16x32_bf16 v[8:11], v[128:131], a[136:139], v[8:11]\n"
      "buffer_load_dwordx4 v208, s[64:67], s91 offen offset:0 lds\n"
      "v_mfma_f32_16x16x32_bf16 v[12:15], v[128:131], a[140:143], v[12:15]\n"
      "s_waitcnt vmcnt(9)\n"
      "ds_read_b64_tr_b16 v[160:161], v200 offset:0\n"
      "v_mfma_f32_16x16x32_bf16 v[16:19], v[132:135], a[128:131], v[16:19]\n"
      "ds_read_b64_tr_b16 v[162:163], v200 offset:1024\n"
      "v_mfma_f32_16x16x32_bf16 v[20:23], v[132:135], a[132:135], v[20:23]\n"
      "buffer_load_dwordx4 v209, s[64:67], s91 offen offset:1024 lds\n"
      "v_mfma_f32_16x16x32_bf16 v[24:27], v[132:135], a[136:139], v[24:27]\n"
      "ds_read_b64_tr_b16 v[164:165], v201 offset:0\n"
      "v_mfma_f32_16x16x32_bf16 v[28:31], v[132:135], a[140:143], v[28:31]\n"
      "ds_read_b64_tr_b16 v[166:167], v201 offset:1024\n"
      "v_mfma_f32_16x16x32_bf16 v[32:35], v[136:139], a[128:131], v[32:35]\n"
      "buffer_load_dwordx4 v210, s[64:67], s91 offen offset:2048 lds\n"
      "v_mfma_f32_16x16x32_bf16 v[36:39], v[136:139], a[132:135], v[36:39]\n"
      "ds_read_b64_tr_b16 v[168:169], v202 offset:0\n"
      "v_mfma_f32_16x16x32_bf16 v[40:43], v[136:139], a[136:139], v[40:43]\n"
      "v_mfma_f32_16x16x32_bf16 v[44:47], v[136:139], a[140:143], v[44:47]\n"
      "buffer_load_dwordx4 v211, s[64:67], s91 offen offset:3072 lds\n"
      "v_mfma_f32_16x16x32_bf16 v[48:51], v[140:143], a[128:131], v[48:51]\n"
      "ds_read_b64_tr_b16 v[170:171], v202 offset:1024\n"
      "v_mfma_f32_16x16x32_bf16 v[52:55], v[140:143], a[132:135], v[52:55]\n"
      "s_add_u32 m0, s81, 4096\n"
      "ds_read_b64_tr_b16 v[172:173], v203 offset:0\n"
      "v_mfma_f32_16x16x32_bf16 v[56:59], v[140:143], a[136:139], v[56:59]\n"
      "buffer_load_dwordx4 v212, s[64:67], s91 offen offset:0 lds\n"
      "v_mfma_f32_16x16x32_bf16 v[60:63], v[140:143], a[140:143], v[60:63]\n"
      "ds_read_b64_tr_b16 v[174:175], v203 offset:1024\n"
      "v_mfma_f32_16x16x32_bf16 v[64:67], v[144:147], a[128:131], v[64:67]\n"
      "ds_read_b64_tr_b16 v[176:177], v204 offset:0\n"
      "v_mfma_f32_16x16x32_bf16 v[68:71], v[144:147], a[132:135], v[68:71]\n"
      "buffer_load_dwordx4 v213, s[64:67], s91 offen offset:1024 lds\n"
      "v_mfma_f32_16x16x32_bf16 v[72:75], v[144:147], a[136:139], v[72:75]\n"
      "ds_read_b64_tr_b16 v[178:179], v204 offset:1024\n"
      "v_mfma_f32_16x16x32_bf16 v[76:79], v[144:147], a[140:143], v[76:79]\n"
      "ds_read_b64_tr_b16 v[180:181], v205 offset:0\n"
      "v_mfma_f32_16x16x32_bf16 v[80:83], v[148:151], a[128:131], v[80:83]\n"
      "buffer_load_dwordx4 v214, s[64:67], s91 offen offset:2048 lds\n"
      "v_mfma_f32_16x16x32_bf16 v[84:87], v[148:151], a[132:135], v[84:87]\n"
      "ds_read_b64_tr_b16 v[182:183], v205 offset:1024\n"
      "v_mfma_f32_16x16x32_bf16 v[88:91], v[148:151], a[136:139], v[88:91]\n"
      "ds_read_b64_tr_b16 v[184:185], v206 offset:0\n"
      "v_mfma_f32_16x16x32_bf16 v[92:95], v[148:151], a[140:143], v[92:95]\n"
      "buffer_load_dwordx4 v215, s[64:67], s91 offen offset:3072 lds\n"
      "v_mfma_f32_16x16x32_bf16 v[96:99], v[152:155], a[128:131], v[96:99]\n"
      "ds_read_b64_tr_b16 v[186:187], v206 offset:1024\n"
      "v_mfma_f32_16x16x32_bf16 v[100:103], v[152:155], a[132:135], v[100:103]\n"
      "ds_read_b64_tr_b16 v[188:189], v207 offset:0\n"
      "v_mfma_f32_16x16x32_bf16 v[104:107], v[152:155], a[136:139], v[104:107]\n"
      "v_mfma_f32_16x16x32_bf16 v[108:111], v[152:155], a[140:143], v[108:111]\n"
      "ds_read_b64_tr_b16 v[190:191], v207 offset:1024\n"
      "v_mfma_f32_16x16x32_bf16 v[112:115], v[156:159], a[128:131], v[112:115]\n"
      "v_mfma_f32_16x16x32_bf16 v[116:119], v[156:159], a[132:135], v[116:119]\n"
      "s_add_u32 s91, s91, 262144\n"
      "v_mfma_f32_16x16x32_bf16 v[120:123], v[156:159], a[136:139], v[120:123]\n"
      "s_cmp_ge_u32 s91, 3670016\n"
      "s_cselect_b32 s91, 0, s91\n"
      "v_mfma_f32_16x16x32_bf16 v[124:127], v[156:159], a[140:143], v[124:127]\n"
      "s_waitcnt lgkmcnt(8)\n"
      "v_mfma_f32_16x16x32_bf16 v[0:3], v[160:163], a[144:147], v[0:3]\n"
      "s_waitcnt lgkmcnt(0)\n"
      "v_mfma_f32_16x16x32_bf16 v[4:7], v[160:163], a[148:151], v[4:7]\n"
      "s_add_u32 m0, s81, 98304\n"
      "v_mfma_f32_16x16x32_bf16 v[8:11], v[160:163], a[152:155], v[8:11]\n"
      "buffer_load_dwordx4 v208, s[64:67], s91 offen offset:0 lds\n"
      "v_mfma_f32_16x16x32_bf16 v[12:15], v[160:163], a[156:159], v[12:15]\n"
      "s_waitcnt vmcnt(9)\n"
      "ds_read_b64_tr_b16 v[128:129], v200 offset:32768\n"
      "v_mfma_f32_16x16x32_bf16 v[16:19], v[164:167], a[144:147], v[16:19]\n"
      "ds_read_b64_tr_b16 v[130:131], v200 offset:33792\n"
      "v_mfma_f32_16x16x32_bf16 v[20:23], v[164:167], a[148:151], v[20:23]\n"
      "buffer_load_dwordx4 v209, s[64:67], s91 offen offset:1024 lds\n"
      "v_mfma_f32_16x16x32_bf16 v[24:27], v[164:167], a[152:155], v[24:27]\n"
      "ds_read_b64_tr_b16 v[132:133], v201 offset:32768\n"
      "v_mfma_f32_16x16x32_bf16 v[28:31], v[164:167], a[156:159], v[28:31]\n"
      "ds_read_b64_tr_b16 v[134:135], v201 offset:33792\n"
      "v_mfma_f32_16x16x32_bf16 v[32:35], v[168:171], a[144:147], v[32:35]\n"
      "buffer_load_dwordx4 v210, s[64:67], s91 offen offset:2048 lds\n"
      "v_mfma_f32_16x16x32_bf16 v[36:39], v[168:171], a[148:151], v[36:39]\n"
      "ds_read_b64_tr_b16 v[136:137], v202 offset:32768\n"
      "v_mfma_f32_16x16x32_bf16 v[40:43], v[168:171], a[152:155], v[40:43]\n"
      "v_mfma_f32_16x16x32_bf16 v[44:47], v[168:171], a[156:159], v[44:47]\n"
      "buffer_load_dwordx4 v211, s[64:67], s91 offen offset:3072 lds\n"
      "v_mfma_f32_16x16x32_bf16 v[48:51], v[172:175], a[144:147], v[48:51]\n"
      "ds_read_b64_tr_b16 v[138:139], v202 offset:33792\n"
      "v_mfma_f32_16x16x32_bf16 v[52:55], v[172:175], a[148:151], v[52:55]\n"
      "s_add_u32 m0, s81, 102400\n"
      "ds_read_b64_tr_b16 v[140:141], v203 offset:32768\n"
      "v_mfma_f32_16x16x32_bf16 v[56:59], v[172:175], a[152:155], v[56:59]\n"
      "buffer_load_dwordx4 v212, s[64:67], s91 offen offset:0 lds\n"
      "v_mfma_f32_16x16x32_bf16 v[60:63], v[172:175], a[156:159], v[60:63]\n"
      "ds_read_b64_tr_b16 v[142:143], v203 offset:33792\n"
      "v_mfma_f32_16x16x32_bf16 v[64:67], v[176:179], a[144:147], v[64:67]\n"
      "ds_read_b64_tr_b16 v[144:145], v204 offset:32768\n"
      "v_mfma_f32_16x16x32_bf16 v[68:71], v[176:179], a[148:151], v[68:71]\n"
      "buffer_load_dwordx4 v213, s[64:67], s91 offen offset:1024 lds\n"
      "v_mfma_f32_16x16x32_bf16 v[72:75], v[176:179], a[152:155], v[72:75]\n"
      "ds_read_b64_tr_b16 v[146:147], v204 offset:33792\n"
      "v_mfma_f32_16x16x32_bf16 v[76:79], v[176:179], a[156:159], v[76:79]\n"
      "ds_read_b64_tr_b16 v[148:149], v205 offset:32768\n"
      "v_mfma_f32_16x16x32_bf16 v[80:83], v[180:183], a[144:147], v[80:83]\n"
      "buffer_load_dwordx4 v214, s[64:67], s91 offen offset:2048 lds\n"
      "v_mfma_f32_16x16x32_bf16 v[84:87], v[180:183], a[148:151], v[84:87]\n"
      "ds_read_b64_tr_b16 v[150:151], v205 offset:33792\n"
      "v_mfma_f32_16x16x32_bf16 v[88:91], v[180:183], a[152:155], v[88:91]\n"
      "ds_read_b64_tr_b16 v[152:153], v206 offset:32768\n"
      "v_mfma_f32_16x16x32_bf16 v[92:95], v[180:183], a[156:159], v[92:95]\n"
      "buffer_load_dwordx4 v215, s[64:67], s91 offen offset:3072 lds\n"
      "v_mfma_f32_16x16x32_bf16 v[96:99], v[184:187], a[144:147], v[96:99]\n"
      "ds_read_b64_tr_b16 v[154:155], v206 offset:33792\n"
      "v_mfma_f32_16x16x32_bf16 v[100:103], v[184:187], a[148:151], v[100:103]\n"
      "ds_read_b64_tr_b16 v[156:157], v207 offset:32768\n"
      "v_mfma_f32_16x16x32_bf16 v[104:107], v[184:187], a[152:155], v[104:107]\n"
      "v_mfma_f32_16x16x32_bf16 v[108:111], v[184:187], a[156:159], v[108:111]\n"
      "ds_read_b64_tr_b16 v[158:159], v207 offset:33792\n"
      "v_mfma_f32_16x16x32_bf16 v[112:115], v[188:191], a[144:147], v[112:115]\n"
      "v_mfma_f32_16x16x32_bf16 v[116:119], v[188:191], a[148:151], v[116:119]\n"
      "s_add_u32 s91, s91, 262144\n"
      "v_mfma_f32_16x16x32_bf16 v[120:123], v[188:191], a[152:155], v[120:123]\n"
      "s_cmp_ge_u32 s91, 3670016\n"
      "s_cselect_b32 s91, 0, s91\n"
      "v_mfma_f32_16x16x32_bf16 v[124:127], v[188:191], a[156:159], v[124:127]\n"
      "s_waitcnt lgkmcnt(8)\n"
      "v_mfma_f32_16x16x32_bf16 v[0:3], v[128:131], a[160:163], v[0:3]\n"
      "s_waitcnt lgkmcnt(0)\n"
      "v_mfma_f32_16x16x32_bf16 v[4:7], v[128:131], a[164:167], v[4:7]\n"
      "s_add_u32 m0, s81, 131072\n"
      "v_mfma_f32_16x16x32_bf16 v[8:11], v[128:131], a[168:171], v[8:11]\n"
      "buffer_load_dwordx4 v208, s[64:67], s91 offen offset:0 lds\n"
      "v_mfma_f32_16x16x32_bf16 v[12:15], v[128:131], a[172:175], v[12:15]\n"
      "s_waitcnt vmcnt(9)\n"
      "ds_read_b64_tr_b16 v[160:161], v192 offset:0\n"
      "v_mfma_f32_16x16x32_bf16 v[16:19], v[132:135], a[160:163], v[16:19]\n"
      "ds_read_b64_tr_b16 v[162:163], v192 offset:1024\n"
      "v_mfma_f32_16x16x32_bf16 v[20:23], v[132:135], a[164:167], v[20:23]\n"
      "buffer_load_dwordx4 v209, s[64:67], s91 offen offset:1024 lds\n"
      "v_mfma_f32_16x16x32_bf16 v[24:27], v[132:135], a[168:171], v[24:27]\n"
      "ds_read_b64_tr_b16 v[164:165], v193 offset:0\n"
      "v_mfma_f32_16x16x32_bf16 v[28:31], v[132:135], a[172:175], v[28:31]\n"
      "ds_read_b64_tr_b16 v[166:167], v193 offset:1024\n"
      "v_mfma_f32_16x16x32_bf16 v[32:35], v[136:139], a[160:163], v[32:35]\n"
      "buffer_load_dwordx4 v210, s[64:67], s91 offen offset:2048 lds\n"
      "v_mfma_f32_16x16x32_bf16 v[36:39], v[136:139], a[164:167], v[36:39]\n"
      "ds_read_b64_tr_b16 v[168:169], v194 offset:0\n"
      "v_mfma_f32_16x16x32_bf16 v[40:43], v[136:139], a[168:171], v[40:43]\n"
      "v_mfma_f32_16x16x32_bf16 v[44:47], v[136:139], a[172:175], v[44:47]\n"
      "buffer_load_dwordx4 v211, s[64:67], s91 offen offset:3072 lds\n"
      "v_mfma_f32_16x16x32_bf16 v[48:51], v[140:143], a[160:163], v[48:51]\n"
      "ds_read_b64_tr_b16 v[170:171], v194 offset:1024\n"
      "v_mfma_f32_16x16x32_bf16 v[52:55], v[140:143], a[164:167], v[52:55]\n"
      "s_add_u32 m0, s81, 135168\n"
      "ds_read_b64_tr_b16 v[172:173], v195 offset:0\n"
      "v_mfma_f32_16x16x32_bf16 v[56:59], v[140:143], a[168:171], v[56:59]\n"
      "buffer_load_dwordx4 v212, s[64:67], s91 offen offset:0 lds\n"
      "v_mfma_f32_16x16x32_bf16 v[60:63], v[140:143], a[172:175], v[60:63]\n"
      "ds_read_b64_tr_b16 v[174:175], v195 offset:1024\n"
      "v_mfma_f32_16x16x32_bf16 v[64:67], v[144:147], a[160:163], v[64:67]\n"
      "ds_read_b64_tr_b16 v[176:177], v196 offset:0\n"
      "v_mfma_f32_16x16x32_bf16 v[68:71], v[144:147], a[164:167], v[68:71]\n"
      "buffer_load_dwordx4 v213, s[64:67], s91 offen offset:1024 lds\n"
      "v_mfma_f32_16x16x32_bf16 v[72:75], v[144:147], a[168:171], v[72:75]\n"
      "ds_read_b64_tr_b16 v[178:179], v196 offset:1024\n"
      "v_mfma_f32_16x16x32_bf16 v[76:79], v[144:147], a[172:175], v[76:79]\n"
      "ds_read_b64_tr_b16 v[180:181], v197 offset:0\n"
      "v_mfma_f32_16x16x32_bf16 v[80:83], v[148:151], a[160:163], v[80:83]\n"
      "buffer_load_dwordx4 v214, s[64:67], s91 offen offset:2048 lds\n"
      "v_mfma_f32_16x16x32_bf16 v[84:87], v[148:151], a[164:167], v[84:87]\n"
      "ds_read_b64_tr_b16 v[182:183], v197 offset:1024\n"
      "v_mfma_f32_16x16x32_bf16 v[88:91], v[148:151], a[168:171], v[88:91]\n"
      "ds_read_b64_tr_b16 v[184:185], v198 offset:0\n"
      "v_mfma_f32_16x16x32_bf16 v[92:95], v[148:151], a[172:175], v[92:95]\n"
      "buffer_load_dwordx4 v215, s[64:67], s91 offen offset:3072 lds\n"
      "v_mfma_f32_16x16x32_bf16 v[96:99], v[152:155], a[160:163], v[96:99]\n"
      "ds_read_b64_tr_b16 v[186:187], v198 offset:1024\n"
      "v_mfma_f32_16x16x32_bf16 v[100:103], v[152:155], a[164:167], v[100:103]\n"
      "ds_read_b64_tr_b16 v[188:189], v199 offset:0\n"
      "v_mfma_f32_16x16x32_bf16 v[104:107], v[152:155], a[168:171], v[104:107]\n"
      "v_mfma_f32_16x16x32_bf16 v[108:111], v[152:155], a[172:175], v[108:111]\n"
      "ds_read_b64_tr_b16 v[190:191], v199 offset:1024\n"
      "v_mfma_f32_16x16x32_bf16 v[112:115], v[156:159], a[160:163], v[112:115]\n"
      "v_mfma_f32_16x16x32_bf16 v[116:119], v[156:159], a[164:167], v[116:119]\n"
      "s_add_u32 s91, s91, 262144\n"
      "v_mfma_f32_16x16x32_bf16 v[120:123], v[156:159], a[168:171], v[120:123]\n"
      "s_cmp_ge_u32 s91, 3670016\n"
      "s_cselect_b32 s91, 0, s91\n"
      "v_mfma_f32_16x16x32_bf16 v[124:127], v[156:159], a[172:175], v[124:127]\n"
      "s_waitcnt lgkmcnt(8)\n"
      "v_mfma_f32_16x16x32_bf16 v[0:3], v[160:163], a[176:179], v[0:3]\n"
      "s_waitcnt lgkmcnt(0)\n"
      "v_mfma_f32_16x16x32_bf16 v[4:7], v[160:163], a[180:183], v[4:7]\n"
      "s_add_u32 m0, s81, 32768\n"
      "v_mfma_f32_16x16x32_bf16 v[8:11], v[160:163], a[184:187], v[8:11]\n"
      "buffer_load_dwordx4 v230, s[56:59], s82 offen offset:0 lds\n"
      "v_mfma_f32_16x16x32_bf16 v[12:15], v[160:163], a[188:191], v[12:15]\n"
      "s_waitcnt vmcnt(9)\n"
      "ds_read_b64_tr_b16 v[128:129], v200 offset:0\n"
      "v_mfma_f32_16x16x32_bf16 v[16:19], v[164:167], a[176:179], v[16:19]\n"
      "ds_read_b64_tr_b16 v[130:131], v200 offset:1024\n"
      "v_mfma_f32_16x16x32_bf16 v[20:23], v[164:167], a[180:183], v[20:23]\n"
      "buffer_load_dwordx4 v231, s[56:59], s82 offen offset:1024 lds\n"
      "v_mfma_f32_16x16x32_bf16 v[24:27], v[164:167], a[184:187], v[24:27]\n"
      "ds_read_b64_tr_b16 v[132:133], v201 offset:0\n"
      "v_mfma_f32_16x16x32_bf16 v[28:31], v[164:167], a[188:191], v[28:31]\n"
      "ds_read_b64_tr_b16 v[134:135], v201 offset:1024\n"
      "v_mfma_f32_16x16x32_bf16 v[32:35], v[168:171], a[176:179], v[32:35]\n"
      "buffer_load_dwordx4 v232, s[56:59], s82 offen offset:2048 lds\n"
      "v_mfma_f32_16x16x32_bf16 v[36:39], v[168:171], a[180:183], v[36:39]\n"
      "ds_read_b64_tr_b16 v[136:137], v202 offset:0\n"
      "v_mfma_f32_16x16x32_bf16 v[40:43], v[168:171], a[184:187], v[40:43]\n"
      "s_add_u32 s96, s82, 4096\n"
      "v_mfma_f32_16x16x32_bf16 v[44:47], v[168:171], a[188:191], v[44:47]\n"
      "buffer_load_dwordx4 v233, s[56:59], s82 offen offset:3072 lds\n"
      "v_mfma_f32_16x16x32_bf16 v[48:51], v[172:175], a[176:179], v[48:51]\n"
      "ds_read_b64_tr_b16 v[138:139], v202 offset:1024\n"
      "v_mfma_f32_16x16x32_bf16 v[52:55], v[172:175], a[180:183], v[52:55]\n"
      "s_add_u32 m0, s81, 36864\n"
      "ds_read_b64_tr_b16 v[140:141], v203 offset:0\n"
      "v_mfma_f32_16x16x32_bf16 v[56:59], v[172:175], a[184:187], v[56:59]\n"
      "buffer_load_dwordx4 v230, s[56:59], s96 offen offset:0 lds\n"
      "v_mfma_f32_16x16x32_bf16 v[60:63], v[172:175], a[188:191], v[60:63]\n"
      "ds_read_b64_tr_b16 v[142:143], v203 offset:1024\n"
      "v_mfma_f32_16x16x32_bf16 v[64:67], v[176:179], a[176:179], v[64:67]\n"
      "ds_read_b64_tr_b16 v[144:145], v204 offset:0\n"
      "v_mfma_f32_16x16x32_bf16 v[68:71], v[176:179], a[180:183], v[68:71]\n"
      "buffer_load_dwordx4 v231, s[56:59], s96 offen offset:1024 lds\n"
      "v_mfma_f32_16x16x32_bf16 v[72:75], v[176:179], a[184:187], v[72:75]\n"
      "ds_read_b64_tr_b16 v[146:147], v204 offset:1024\n"
      "v_mfma_f32_16x16x32_bf16 v[76:79], v[176:179], a[188:191], v[76:79]\n"
      "ds_read_b64_tr_b16 v[148:149], v205 offset:0\n"
      "v_mfma_f32_16x16x32_bf16 v[80:83], v[180:183], a[176:179], v[80:83]\n"
      "buffer_load_dwordx4 v232, s[56:59], s96 offen offset:2048 lds\n"
      "v_mfma_f32_16x16x32_bf16 v[84:87], v[180:183], a[180:183], v[84:87]\n"
      "ds_read_b64_tr_b16 v[150:151], v205 offset:1024\n"
      "v_mfma_f32_16x16x32_bf16 v[88:91], v[180:183], a[184:187], v[88:91]\n"
      "ds_read_b64_tr_b16 v[152:153], v206 offset:0\n"
      "v_mfma_f32_16x16x32_bf16 v[92:95], v[180:183], a[188:191], v[92:95]\n"
      "buffer_load_dwordx4 v233, s[56:59], s96 offen offset:3072 lds\n"
      "v_mfma_f32_16x16x32_bf16 v[96:99], v[184:187], a[176:179], v[96:99]\n"
      "ds_read_b64_tr_b16 v[154:155], v206 offset:1024\n"
      "v_mfma_f32_16x16x32_bf16 v[100:103], v[184:187], a[180:183], v[100:103]\n"
      "ds_read_b64_tr_b16 v[156:157], v207 offset:0\n"
      "v_mfma_f32_16x16x32_bf16 v[104:107], v[184:187], a[184:187], v[104:107]\n"
      "v_mfma_f32_16x16x32_bf16 v[108:111], v[184:187], a[188:191], v[108:111]\n"
      "ds_read_b64_tr_b16 v[158:159], v207 offset:1024\n"
      "v_mfma_f32_16x16x32_bf16 v[112:115], v[188:191], a[176:179], v[112:115]\n"
      "v_mfma_f32_16x16x32_bf16 v[116:119], v[188:191], a[180:183], v[116:119]\n"
      "v_mfma_f32_16x16x32_bf16 v[120:123], v[188:191], a[184:187], v[120:123]\n"
      "v_mfma_f32_16x16x32_bf16 v[124:127], v[188:191], a[188:191], v[124:127]\n"
      "s_waitcnt lgkmcnt(8)\n"
      "v_mfma_f32_16x16x32_bf16 v[0:3], v[128:131], a[192:195], v[0:3]\n"
      "s_waitcnt lgkmcnt(0)\n"
      "v_lshrrev_b32 v244, 5, v254\n"
      "v_mfma_f32_16x16x32_bf16 v[4:7], v[128:131], a[196:199], v[4:7]\n"
      "s_add_u32 m0, s81, 0\n"
      "v_and_b32 v245, 15, v254\n"
      "v_mfma_f32_16x16x32_bf16 v[8:11], v[128:131], a[200:203], v[8:11]\n"
      "buffer_load_dwordx4 v230, s[56:59], s83 offen offset:0 lds\n"
      "v_mfma_f32_16x16x32_bf16 v[12:15], v[128:131], a[204:207], v[12:15]\n"
      "s_waitcnt vmcnt(9)\n"
      "ds_read_b64_tr_b16 v[160:161], v200 offset:32768\n"
      "v_lshl_add_u32 v244, v244, 4, v245\n"
      "v_mfma_f32_16x16x32_bf16 v[16:19], v[132:135], a[192:195], v[16:19]\n"
      "ds_read_b64_tr_b16 v[162:163], v200 offset:33792\n"
      "v_lshlrev_b32 v244, 4, v244\n"
      "v_mfma_f32_16x16x32_bf16 v[20:23], v[132:135], a[196:199], v[20:23]\n"
      "buffer_load_dwordx4 v231, s[56:59], s83 offen offset:1024 lds\n"
      "v_mfma_f32_16x16x32_bf16 v[24:27], v[132:135], a[200:203], v[24:27]\n"
      "ds_read_b64_tr_b16 v[164:165], v201 offset:32768\n"
      "v_bfe_u32 v245, v254, 4, 1\n"
      "v_mfma_f32_16x16x32_bf16 v[28:31], v[132:135], a[204:207], v[28:31]\n"
      "ds_read_b64_tr_b16 v[166:167], v201 offset:33792\n"
      "v_lshl_add_u32 v244, v245, 9, v244\n"
      "v_mfma_f32_16x16x32_bf16 v[32:35], v[136:139], a[192:195], v[32:35]\n"
      "buffer_load_dwordx4 v232, s[56:59], s83 offen offset:2048 lds\n"
      "v_mfma_f32_16x16x32_bf16 v[36:39], v[136:139], a[196:199], v[36:39]\n"
      "s_add_u32 s84, s81, 98304\n"
      "ds_read_b64_tr_b16 v[168:169], v202 offset:32768\n"
      "v_mfma_f32_16x16x32_bf16 v[40:43], v[136:139], a[200:203], v[40:43]\n"
      "s_add_u32 s96, s83, 4096\n"
      "v_add_u32 v243, s84, v244\n"
      "v_mfma_f32_16x16x32_bf16 v[44:47], v[136:139], a[204:207], v[44:47]\n"
      "buffer_load_dwordx4 v233, s[56:59], s83 offen offset:3072 lds\n"
      "v_mfma_f32_16x16x32_bf16 v[48:51], v[140:143], a[192:195], v[48:51]\n"
      "s_and_b32 s84, s80, 2\n"
      "ds_read_b64_tr_b16 v[170:171], v202 offset:33792\n"
      "v_mfma_f32_16x16x32_bf16 v[52:55], v[140:143], a[196:199], v[52:55]\n"
      "s_add_u32 m0, s81, 4096\n"
      "ds_read_b64_tr_b16 v[172:173], v203 offset:32768\n"
      "v_mfma_f32_16x16x32_bf16 v[56:59], v[140:143], a[200:203], v[56:59]\n"
      "buffer_load_dwordx4 v230, s[56:59], s96 offen offset:0 lds\n"
      "v_mfma_f32_16x16x32_bf16 v[60:63], v[140:143], a[204:207], v[60:63]\n"
      "s_lshl_b32 s84, s84, 13\n"
      "ds_read_b64_tr_b16 v[174:175], v203 offset:33792\n"
      "v_mfma_f32_16x16x32_bf16 v[64:67], v[144:147], a[192:195], v[64:67]\n"
      "s_add_u32 s84, s84, 98304\n"
      "ds_read_b64_tr_b16 v[176:177], v204 offset:32768\n"
      "v_mfma_f32_16x16x32_bf16 v[68:71], v[144:147], a[196:199], v[68:71]\n"
      "buffer_load_dwordx4 v231, s[56:59], s96 offen offset:1024 lds\n"
      "v_mfma_f32_16x16x32_bf16 v[72:75], v[144:147], a[200:203], v[72:75]\n"
      "ds_read_b64_tr_b16 v[178:179], v204 offset:33792\n"
      "v_add_u32 v252, s84, v242\n"
      "v_mfma_f32_16x16x32_bf16 v[76:79], v[144:147], a[204:207], v[76:79]\n"
      "ds_read_b64_tr_b16 v[180:181], v205 offset:32768\n"
      "v_mfma_f32_16x16x32_bf16 v[80:83], v[148:151], a[192:195], v[80:83]\n"
      "buffer_load_dwordx4 v232, s[56:59], s96 offen offset:2048 lds\n"
      "v_mfma_f32_16x16x32_bf16 v[84:87], v[148:151], a[196:199], v[84:87]\n"
      "ds_read_b64_tr_b16 v[182:183], v205 offset:33792\n"
      "v_mfma_f32_16x16x32_bf16 v[88:91], v[148:151], a[200:203], v[88:91]\n"
      "ds_read_b64_tr_b16 v[184:185], v206 offset:32768\n"
      "v_mfma_f32_16x16x32_bf16 v[92:95], v[148:151], a[204:207], v[92:95]\n"
      "buffer_load_dwordx4 v233, s[56:59], s96 offen offset:3072 lds\n"
      "v_mfma_f32_16x16x32_bf16 v[96:99], v[152:155], a[192:195], v[96:99]\n"
      "ds_read_b64_tr_b16 v[186:187], v206 offset:33792\n"
      "v_mfma_f32_16x16x32_bf16 v[100:103], v[152:155], a[196:199], v[100:103]\n"
      "ds_read_b64_tr_b16 v[188:189], v207 offset:32768\n"
      "v_mfma_f32_16x16x32_bf16 v[104:107], v[152:155], a[200:203], v[104:107]\n"
      "v_mfma_f32_16x16x32_bf16 v[108:111], v[152:155], a[204:207], v[108:111]\n"
      "ds_read_b64_tr_b16 v[190:191], v207 offset:33792\n"
      "v_mfma_f32_16x16x32_bf16 v[112:115], v[156:159], a[192:195], v[112:115]\n"
      "v_mfma_f32_16x16x32_bf16 v[116:119], v[156:159], a[196:199], v[116:119]\n"
      "v_mfma_f32_16x16x32_bf16 v[120:123], v[156:159], a[200:203], v[120:123]\n"
      "v_mfma_f32_16x16x32_bf16 v[124:127], v[156:159], a[204:207], v[124:127]\n"
      "s_waitcnt lgkmcnt(8)\n"
      "v_mfma_f32_16x16x32_bf16 v[0:3], v[160:163], a[208:211], v[0:3]\n"
      "s_waitcnt lgkmcnt(0)\n"
      "v_mfma_f32_16x16x32_bf16 v[4:7], v[160:163], a[212:215], v[4:7]\n"
      "v_mfma_f32_16x16x32_bf16 v[8:11], v[160:163], a[216:219], v[8:11]\n"
      "v_mfma_f32_16x16x32_bf16 v[12:15], v[160:163], a[220:223], v[12:15]\n"
      "v_mfma_f32_16x16x32_bf16 v[16:19], v[164:167], a[208:211], v[16:19]\n"
      "v_mfma_f32_16x16x32_bf16 v[20:23], v[164:167], a[212:215], v[20:23]\n"
      "v_mfma_f32_16x16x32_bf16 v[24:27], v[164:167], a[216:219], v[24:27]\n"
      "v_mfma_f32_16x16x32_bf16 v[28:31], v[164:167], a[220:223], v[28:31]\n"
      "v_mfma_f32_16x16x32_bf16 v[32:35], v[168:171], a[208:211], v[32:35]\n"
      "v_mfma_f32_16x16x32_bf16 v[36:39], v[168:171], a[212:215], v[36:39]\n"
      "v_mfma_f32_16x16x32_bf16 v[40:43], v[168:171], a[216:219], v[40:43]\n"
      "v_mfma_f32_16x16x32_bf16 v[44:47], v[168:171], a[220:223], v[44:47]\n"
      "v_mfma_f32_16x16x32_bf16 v[48:51], v[172:175], a[208:211], v[48:51]\n"
      "v_mfma_f32_16x16x32_bf16 v[52:55], v[172:175], a[212:215], v[52:55]\n"
      "v_mfma_f32_16x16x32_bf16 v[56:59], v[172:175], a[216:219], v[56:59]\n"
      "v_mfma_f32_16x16x32_bf16 v[60:63], v[172:175], a[220:223], v[60:63]\n"
      "v_mfma_f32_16x16x32_bf16 v[64:67], v[176:179], a[208:211], v[64:67]\n"
      "v_mfma_f32_16x16x32_bf16 v[68:71], v[176:179], a[212:215], v[68:71]\n"
      "v_mfma_f32_16x16x32_bf16 v[72:75], v[176:179], a[216:219], v[72:75]\n"
      "v_mfma_f32_16x16x32_bf16 v[76:79], v[176:179], a[220:223], v[76:79]\n"
      "v_mfma_f32_16x16x32_bf16 v[80:83], v[180:183], a[208:211], v[80:83]\n"
      "v_mfma_f32_16x16x32_bf16 v[84:87], v[180:183], a[212:215], v[84:87]\n"
      "v_mfma_f32_16x16x32_bf16 v[88:91], v[180:183], a[216:219], v[88:91]\n"
      "v_mfma_f32_16x16x32_bf16 v[92:95], v[180:183], a[220:223], v[92:95]\n"
      "v_mfma_f32_16x16x32_bf16 v[96:99], v[184:187], a[208:211], v[96:99]\n"
      "v_mfma_f32_16x16x32_bf16 v[100:103], v[184:187], a[212:215], v[100:103]\n"
      "v_mfma_f32_16x16x32_bf16 v[104:107], v[184:187], a[216:219], v[104:107]\n"
      "v_mfma_f32_16x16x32_bf16 v[108:111], v[184:187], a[220:223], v[108:111]\n"
      "v_mfma_f32_16x16x32_bf16 v[112:115], v[188:191], a[208:211], v[112:115]\n"
      "v_mfma_f32_16x16x32_bf16 v[116:119], v[188:191], a[212:215], v[116:119]\n"
      "v_mfma_f32_16x16x32_bf16 v[120:123], v[188:191], a[216:219], v[120:123]\n"
      "v_mfma_f32_16x16x32_bf16 v[124:127], v[188:191], a[220:223], v[124:127]\n"
      "v_pk_mul_f32 v[0:1], v[0:1], s[94:95] op_sel_hi:[1,0]\n"
      "v_pk_mul_f32 v[2:3], v[2:3], s[94:95] op_sel_hi:[1,0]\n"
      "v_pk_mul_f32 v[16:17], v[16:17], s[94:95] op_sel_hi:[1,0]\n"
      "v_pk_mul_f32 v[18:19], v[18:19], s[94:95] op_sel_hi:[1,0]\n"
      "v_cvt_pk_bf16_f32 v0, v0, v1\n"
      "v_cvt_pk_bf16_f32 v1, v2, v3\n"
      "v_cvt_pk_bf16_f32 v2, v16, v17\n"
      "v_cvt_pk_bf16_f32 v3, v18, v19\n"
      "v_pk_mul_f32 v[4:5], v[4:5], s[94:95] op_sel_hi:[1,0]\n"
      "v_pk_mul_f32 v[6:7], v[6:7], s[94:95] op_sel_hi:[1,0]\n"
      "v_pk_mul_f32 v[20:21], v[20:21], s[94:95] op_sel_hi:[1,0]\n"
      "v_pk_mul_f32 v[22:23], v[22:23], s[94:95] op_sel_hi:[1,0]\n"
      "v_cvt_pk_bf16_f32 v4, v4, v5\n"
      "v_cvt_pk_bf16_f32 v5, v6, v7\n"
      "v_cvt_pk_bf16_f32 v6, v20, v21\n"
      "v_cvt_pk_bf16_f32 v7, v22, v23\n"
      "v_permlane16_swap_b32 v0, v2\n"
      "v_permlane16_swap_b32 v1, v3\n"
      "ds_write_b128 v243, v[0:3] offset:0\n"
      "v_pk_mul_f32 v[8:9], v[8:9], s[94:95] op_sel_hi:[1,0]\n"
      "v_pk_mul_f32 v[10:11], v[10:11], s[94:95] op_sel_hi:[1,0]\n"
      "v_pk_mul_f32 v[24:25], v[24:25], s[94:95] op_sel_hi:[1,0]\n"
      "v_pk_mul_f32 v[26:27], v[26:27], s[94:95] op_sel_hi:[1,0]\n"
      "v_cvt_pk_bf16_f32 v8, v8, v9\n"
      "v_cvt_pk_bf16_f32 v9, v10, v11\n"
      "v_cvt_pk_bf16_f32 v10, v24, v25\n"
      "v_cvt_pk_bf16_f32 v11, v26, v27\n"
      "v_permlane16_swap_b32 v4, v6\n"
      "v_permlane16_swap_b32 v5, v7\n"
      "ds_write_b128 v243, v[4:7] offset:4096\n"
      "v_pk_mul_f32 v[12:13], v[12:13], s[94:95] op_sel_hi:[1,0]\n"
      "v_pk_mul_f32 v[14:15], v[14:15], s[94:95] op_sel_hi:[1,0]\n"
      "v_pk_mul_f32 v[28:29], v[28:29], s[94:95] op_sel_hi:[1,0]\n"
      "v_pk_mul_f32 v[30:31], v[30:31], s[94:95] op_sel_hi:[1,0]\n"
      "v_cvt_pk_bf16_f32 v12, v12, v13\n"
      "v_cvt_pk_bf16_f32 v13, v14, v15\n"
      "v_cvt_pk_bf16_f32 v14, v28, v29\n"
      "v_cvt_pk_bf16_f32 v15, v30, v31\n"
      "v_permlane16_swap_b32 v8, v10\n"
      "v_permlane16_swap_b32 v9, v11\n"
      "ds_write_b128 v243, v[8:11] offset:32768\n"
      "v_pk_mul_f32 v[32:33], v[32:33], s[94:95] op_sel_hi:[1,0]\n"
      "v_pk_mul_f32 v[34:35], v[34:35], s[94:95] op_sel_hi:[1,0]\n"
      "v_pk_mul_f32 v[48:49], v[48:49], s[94:95] op_sel_hi:[1,0]\n"
      "v_pk_mul_f32 v[50:51], v[50:51], s[94:95] op_sel_hi:[1,0]\n"
      "v_cvt_pk_bf16_f32 v32, v32, v33\n"
      "v_cvt_pk_bf16_f32 v33, v34, v35\n"
      "v_cvt_pk_bf16_f32 v34, v48, v49\n"
      "v_cvt_pk_bf16_f32 v35, v50, v51\n"
      "v_permlane16_swap_b32 v12, v14\n"
      "v_permlane16_swap_b32 v13, v15\n"
      "ds_write_b128 v243, v[12:15] offset:36864\n"
      "v_pk_mul_f32 v[36:37], v[36:37], s[94:95] op_sel_hi:[1,0]\n"
      "v_pk_mul_f32 v[38:39], v[38:39], s[94:95] op_sel_hi:[1,0]\n"
      "v_pk_mul_f32 v[52:53], v[52:53], s[94:95] op_sel_hi:[1,0]\n"
      "v_pk_mul_f32 v[54:55], v[54:55], s[94:95] op_sel_hi:[1,0]\n"
      "v_cvt_pk_bf16_f32 v36, v36, v37\n"
      "v_cvt_pk_bf16_f32 v37, v38, v39\n"
      "v_cvt_pk_bf16_f32 v38, v52, v53\n"
      "v_cvt_pk_bf16_f32 v39, v54, v55\n"
      "v_permlane16_swap_b32 v32, v34\n"
      "v_permlane16_swap_b32 v33, v35\n"
      "ds_write_b128 v243, v[32:35] offset:1024\n"
      "v_pk_mul_f32 v[40:41], v[40:41], s[94:95] op_sel_hi:[1,0]\n"
      "v_pk_mul_f32 v[42:43], v[42:43], s[94:95] op_sel_hi:[1,0]\n"
      "v_pk_mul_f32 v[56:57], v[56:57], s[94:95] op_sel_hi:[1,0]\n"
      "v_pk_mul_f32 v[58:59], v[58:59], s[94:95] op_sel_hi:[1,0]\n"
      "v_cvt_pk_bf16_f32 v40, v40, v41\n"
      "v_cvt_pk_bf16_f32 v41, v42, v43\n"
      "v_cvt_pk_bf16_f32 v42, v56, v57\n"
      "v_cvt_pk_bf16_f32 v43, v58, v59\n"
      "v_permlane16_swap_b32 v36, v38\n"
      "v_permlane16_swap_b32 v37, v39\n"
      "ds_write_b128 v243, v[36:39] offset:5120\n"
      "v_pk_mul_f32 v[44:45], v[44:45], s[94:95] op_sel_hi:[1,0]\n"
      "v_pk_mul_f32 v[46:47], v[46:47], s[94:95] op_sel_hi:[1,0]\n"
      "v_pk_mul_f32 v[60:61], v[60:61], s[94:95] op_sel_hi:[1,0]\n"
      "v_pk_mul_f32 v[62:63], v[62:63], s[94:95] op_sel_hi:[1,0]\n"
      "v_cvt_pk_bf16_f32 v44, v44, v45\n"
      "v_cvt_pk_bf16_f32 v45, v46, v47\n"
      "v_cvt_pk_bf16_f32 v46, v60, v61\n"
      "v_cvt_pk_bf16_f32 v47, v62, v63\n"
      "v_permlane16_swap_b32 v40, v42\n"
      "v_permlane16_swap_b32 v41, v43\n"
      "ds_write_b128 v243, v[40:43] offset:33792\n"
      "v_pk_mul_f32 v[64:65], v[64:65], s[94:95] op_sel_hi:[1,0]\n"
      "v_pk_mul_f32 v[66:67], v[66:67], s[94:95] op_sel_hi:[1,0]\n"
      "v_pk_mul_f32 v[80:81], v[80:81], s[94:95] op_sel_hi:[1,0]\n"
      "v_pk_mul_f32 v[82:83], v[82:83], s[94:95] op_sel_hi:[1,0]\n"
      "v_cvt_pk_bf16_f32 v64, v64, v65\n"
      "v_cvt_pk_bf16_f32 v65, v66, v67\n"
      "v_cvt_pk_bf16_f32 v66, v80, v81\n"
      "v_cvt_pk_bf16_f32 v67, v82, v83\n"
      "v_permlane16_swap_b32 v44, v46\n"
      "v_permlane16_swap_b32 v45, v47\n"
      "ds_write_b128 v243, v[44:47] offset:37888\n"
      "v_pk_mul_f32 v[68:69], v[68:69], s[94:95] op_sel_hi:[1,0]\n"
      "v_pk_mul_f32 v[70:71], v[70:71], s[94:95] op_sel_hi:[1,0]\n"
      "v_pk_mul_f32 v[84:85], v[84:85], s[94:95] op_sel_hi:[1,0]\n"
      "v_pk_mul_f32 v[86:87], v[86:87], s[94:95] op_sel_hi:[1,0]\n"
      "v_cvt_pk_bf16_f32 v68, v68, v69\n"
      "v_cvt_pk_bf16_f32 v69, v70, v71\n"
      "v_cvt_pk_bf16_f32 v70, v84, v85\n"
      "v_cvt_pk_bf16_f32 v71, v86, v87\n"
      "v_permlane16_swap_b32 v64, v66\n"
      "v_permlane16_swap_b32 v65, v67\n"
      "ds_write_b128 v243, v[64:67] offset:2048\n"
      "v_pk_mul_f32 v[72:73], v[72:73], s[94:95] op_sel_hi:[1,0]\n"
      "v_pk_mul_f32 v[74:75], v[74:75], s[94:95] op_sel_hi:[1,0]\n"
      "v_pk_mul_f32 v[88:89], v[88:89], s[94:95] op_sel_hi:[1,0]\n"
      "v_pk_mul_f32 v[90:91], v[90:91], s[94:95] op_sel_hi:[1,0]\n"
      "v_cvt_pk_bf16_f32 v72, v72, v73\n"
      "v_cvt_pk_bf16_f32 v73, v74, v75\n"
      "v_cvt_pk_bf16_f32 v74, v88, v89\n"
      "v_cvt_pk_bf16_f32 v75, v90, v91\n"
      "v_permlane16_swap_b32 v68, v70\n"
      "v_permlane16_swap_b32 v69, v71\n"
      "ds_write_b128 v243, v[68:71] offset:6144\n"
      "v_pk_mul_f32 v[76:77], v[76:77], s[94:95] op_sel_hi:[1,0]\n"
      "v_pk_mul_f32 v[78:79], v[78:79], s[94:95] op_sel_hi:[1,0]\n"
      "v_pk_mul_f32 v[92:93], v[92:93], s[94:95] op_sel_hi:[1,0]\n"
      "v_pk_mul_f32 v[94:95], v[94:95], s[94:95] op_sel_hi:[1,0]\n"
      "v_cvt_pk_bf16_f32 v76, v76, v77\n"
      "v_cvt_pk_bf16_f32 v77, v78, v79\n"
      "v_cvt_pk_bf16_f32 v78, v92, v93\n"
      "v_cvt_pk_bf16_f32 v79, v94, v95\n"
      "v_permlane16_swap_b32 v72, v74\n"
      "v_permlane16_swap_b32 v73, v75\n"
      "ds_write_b128 v243, v[72:75] offset:34816\n"
      "v_pk_mul_f32 v[96:97], v[96:97], s[94:95] op_sel_hi:[1,0]\n"
      "v_pk_mul_f32 v[98:99], v[98:99], s[94:95] op_sel_hi:[1,0]\n"
      "v_pk_mul_f32 v[112:113], v[112:113], s[94:95] op_sel_hi:[1,0]\n"
      "v_pk_mul_f32 v[114:115], v[114:115], s[94:95] op_sel_hi:[1,0]\n"
      "v_cvt_pk_bf16_f32 v96, v96, v97\n"
      "v_cvt_pk_bf16_f32 v97, v98, v99\n"
      "v_cvt_pk_bf16_f32 v98, v112, v113\n"
      "v_cvt_pk_bf16_f32 v99, v114, v115\n"
      "v_permlane16_swap_b32 v76, v78\n"
      "v_permlane16_swap_b32 v77, v79\n"
      "ds_write_b128 v243, v[76:79] offset:38912\n"
      "v_pk_mul_f32 v[100:101], v[100:101], s[94:95] op_sel_hi:[1,0]\n"
      "v_pk_mul_f32 v[102:103], v[102:103], s[94:95] op_sel_hi:[1,0]\n"
      "v_pk_mul_f32 v[116:117], v[116:117], s[94:95] op_sel_hi:[1,0]\n"
      "v_pk_mul_f32 v[118:119], v[118:119], s[94:95] op_sel_hi:[1,0]\n"
      "v_cvt_pk_bf16_f32 v100, v100, v101\n"
      "v_cvt_pk_bf16_f32 v101, v102, v103\n"
      "v_cvt_pk_bf16_f32 v102, v116, v117\n"
      "v_cvt_pk_bf16_f32 v103, v118, v119\n"
      "v_permlane16_swap_b32 v96, v98\n"
      "v_permlane16_swap_b32 v97, v99\n"
      "ds_write_b128 v243, v[96:99] offset:3072\n"
      "v_pk_mul_f32 v[104:105], v[104:105], s[94:95] op_sel_hi:[1,0]\n"
      "v_pk_mul_f32 v[106:107], v[106:107], s[94:95] op_sel_hi:[1,0]\n"
      "v_pk_mul_f32 v[120:121], v[120:121], s[94:95] op_sel_hi:[1,0]\n"
      "v_pk_mul_f32 v[122:123], v[122:123], s[94:95] op_sel_hi:[1,0]\n"
      "v_cvt_pk_bf16_f32 v104, v104, v105\n"
      "v_cvt_pk_bf16_f32 v105, v106, v107\n"
      "v_cvt_pk_bf16_f32 v106, v120, v121\n"
      "v_cvt_pk_bf16_f32 v107, v122, v123\n"
      "v_permlane16_swap_b32 v100, v102\n"
      "v_permlane16_swap_b32 v101, v103\n"
      "ds_write_b128 v243, v[100:103] offset:7168\n"
      "v_pk_mul_f32 v[108:109], v[108:109], s[94:95] op_sel_hi:[1,0]\n"
      "v_pk_mul_f32 v[110:111], v[110:111], s[94:95] op_sel_hi:[1,0]\n"
      "v_pk_mul_f32 v[124:125], v[124:125], s[94:95] op_sel_hi:[1,0]\n"
      "v_pk_mul_f32 v[126:127], v[126:127], s[94:95] op_sel_hi:[1,0]\n"
      "v_cvt_pk_bf16_f32 v108, v108, v109\n"
      "v_cvt_pk_bf16_f32 v109, v110, v111\n"
      "v_cvt_pk_bf16_f32 v110, v124, v125\n"
      "v_cvt_pk_bf16_f32 v111, v126, v127\n"
      "v_permlane16_swap_b32 v104, v106\n"
      "v_permlane16_swap_b32 v105, v107\n"
      "ds_write_b128 v243, v[104:107] offset:35840\n"
      "s_nop 1\n"
      "v_permlane16_swap_b32 v108, v110\n"
      "v_permlane16_swap_b32 v109, v111\n"
      "ds_write_b128 v243, v[108:111] offset:39936\n"
      "s_waitcnt vmcnt(8) lgkmcnt(0)\n"
      "s_barrier\n"
      "ds_read_b128 a[128:131], v218 offset:32768\n"
      "ds_read_b128 a[132:135], v219 offset:32768\n"
      "ds_read_b128 a[136:139], v220 offset:32768\n"
      "ds_read_b128 a[140:143], v221 offset:32768\n"
      "ds_read_b128 a[144:147], v218 offset:33024\n"
      "ds_read_b128 a[148:151], v219 offset:33024\n"
      "ds_read_b128 a[152:155], v220 offset:33024\n"
      "ds_read_b128 a[156:159], v221 offset:33024\n"
      "ds_read_b128 a[0:3], v252 offset:0\n"
      "ds_read_b128 a[4:7], v252 offset:1024\n"
      "ds_read_b128 a[8:11], v252 offset:2048\n"
      "ds_read_b128 a[12:15], v252 offset:3072\n"
      "ds_read_b128 a[16:19], v252 offset:8192\n"
      "ds_read_b128 a[20:23], v252 offset:9216\n"
      "ds_read_b128 a[24:27], v252 offset:10240\n"
      "ds_read_b128 a[28:31], v252 offset:11264\n"
      "ds_read_b128 a[32:35], v252 offset:4096\n"
      "ds_read_b128 a[36:39], v252 offset:5120\n"
      "ds_read_b128 a[40:43], v252 offset:6144\n"
      "ds_read_b128 a[44:47], v252 offset:7168\n"
      "ds_read_b128 a[48:51], v252 offset:12288\n"
      "ds_read_b128 a[52:55], v252 offset:13312\n"
      "ds_read_b128 a[56:59], v252 offset:14336\n"
      "ds_read_b128 a[60:63], v252 offset:15360\n"
      "s_waitcnt lgkmcnt(8)\n"
      "v_mfma_f32_16x16x32_bf16 v[128:131], a[128:131], a[0:3], 0\n"
      "ds_read_b128 a[64:67], v252 offset:32768\n"
      "v_mov_b32 v0, 0\n"
      "v_mfma_f32_16x16x32_bf16 v[128:131], a[132:135], a[4:7], v[128:131]\n"
      "ds_read_b128 a[68:71], v252 offset:33792\n"
      "v_mov_b32 v1, 0\n"
      "v_mfma_f32_16x16x32_bf16 v[128:131], a[136:139], a[8:11], v[128:131]\n"
      "ds_read_b128 a[72:75], v252 offset:34816\n"
      "v_mov_b32 v2, 0\n"
      "v_mfma_f32_16x16x32_bf16 v[128:131], a[140:143], a[12:15], v[128:131]\n"
      "ds_read_b128 a[76:79], v252 offset:35840\n"
      "v_mov_b32 v3, 0\n"
      "v_mfma_f32_16x16x32_bf16 v[128:131], a[144:147], a[16:19], v[128:131]\n"
      "ds_read_b128 a[80:83], v252 offset:40960\n"
      "v_mov_b32 v4, 0\n"
      "v_mfma_f32_16x16x32_bf16 v[128:131], a[148:151], a[20:23], v[128:131]\n"
      "ds_read_b128 a[84:87], v252 offset:41984\n"
      "v_mov_b32 v5, 0\n"
      "v_mfma_f32_16x16x32_bf16 v[128:131], a[152:155], a[24:27], v[128:131]\n"
      "ds_read_b128 a[88:91], v252 offset:43008\n"
      "v_mov_b32 v6, 0\n"
      "v_mfma_f32_16x16x32_bf16 v[128:131], a[156:159], a[28:31], v[128:131]\n"
      "s_waitcnt lgkmcnt(7)\n"
      "ds_read_b128 a[92:95], v252 offset:44032\n"
      "v_mov_b32 v7, 0\n"
      "v_mfma_f32_16x16x32_bf16 v[132:135], a[128:131], a[32:35], 0\n"
      "ds_read_b128 a[96:99], v252 offset:36864\n"
      "v_mov_b32 v8, 0\n"
      "v_mfma_f32_16x16x32_bf16 v[132:135], a[132:135], a[36:39], v[132:135]\n"
      "ds_read_b128 a[100:103], v252 offset:37888\n"
      "v_mov_b32 v9, 0\n"
      "v_mfma_f32_16x16x32_bf16 v[132:135], a[136:139], a[40:43], v[132:135]\n"
      "ds_read_b128 a[104:107], v252 offset:38912\n"
      "v_mov_b32 v10, 0\n"
      "v_mfma_f32_16x16x32_bf16 v[132:135], a[140:143], a[44:47], v[132:135]\n"
      "ds_read_b128 a[108:111], v252 offset:39936\n"
      "v_mov_b32 v11, 0\n"
      "v_mfma_f32_16x16x32_bf16 v[132:135], a[144:147], a[48:51], v[132:135]\n"
      "ds_read_b128 a[112:115], v252 offset:45056\n"
      "v_mov_b32 v12, 0\n"
      "v_mfma_f32_16x16x32_bf16 v[132:135], a[148:151], a[52:55], v[132:135]\n"
      "ds_read_b128 a[116:119], v252 offset:46080\n"
      "v_mov_b32 v13, 0\n"
      "v_mfma_f32_16x16x32_bf16 v[132:135], a[152:155], a[56:59], v[132:135]\n"
      "ds_read_b128 a[120:123], v252 offset:47104\n"
      "v_mov_b32 v14, 0\n"
      "v_mfma_f32_16x16x32_bf16 v[132:135], a[156:159], a[60:63], v[132:135]\n"
      "s_waitcnt lgkmcnt(7)\n"
      "ds_read_b128 a[124:127], v252 offset:48128\n"
      "v_mov_b32 v15, 0\n"
      "v_mfma_f32_16x16x32_bf16 v[136:139], a[128:131], a[64:67], 0\n"
      "ds_read_b128 a[160:163], v218 offset:40960\n"
      "v_mov_b32 v16, 0\n"
      "v_mfma_f32_16x16x32_bf16 v[136:139], a[132:135], a[68:71], v[136:139]\n"
      "ds_read_b128 a[164:167], v219 offset:40960\n"
      "v_mov_b32 v17, 0\n"
      "v_mfma_f32_16x16x32_bf16 v[136:139], a[136:139], a[72:75], v[136:139]\n"
      "ds_read_b128 a[168:171], v220 offset:40960\n"
      "v_mov_b32 v18, 0\n"
      "v_mfma_f32_16x16x32_bf16 v[136:139], a[140:143], a[76:79], v[136:139]\n"
      "ds_read_b128 a[172:175], v221 offset:40960\n"
      "v_mov_b32 v19, 0\n"
      "v_mfma_f32_16x16x32_bf16 v[136:139], a[144:147], a[80:83], v[136:139]\n"
      "ds_read_b128 a[176:179], v218 offset:41216\n"
      "v_mov_b32 v20, 0\n"
      "v_mfma_f32_16x16x32_bf16 v[136:139], a[148:151], a[84:87], v[136:139]\n"
      "ds_read_b128 a[180:183], v219 offset:41216\n"
      "v_mov_b32 v21, 0\n"
      "v_mfma_f32_16x16x32_bf16 v[136:139], a[152:155], a[88:91], v[136:139]\n"
      "ds_read_b128 a[184:187], v220 offset:41216\n"
      "v_mov_b32 v22, 0\n"
      "v_mfma_f32_16x16x32_bf16 v[136:139], a[156:159], a[92:95], v[136:139]\n"
      "s_waitcnt lgkmcnt(7)\n"
      "ds_read_b128 a[188:191], v221 offset:41216\n"
      "v_mov_b32 v23, 0\n"
      "v_mfma_f32_16x16x32_bf16 v[140:143], a[128:131], a[96:99], 0\n"
      "s_add_u32 m0, s81, 65536\n"
      "v_mov_b32 v24, 0\n"
      "v_mov_b32 v25, 0\n"
      "v_mfma_f32_16x16x32_bf16 v[140:143], a[132:135], a[100:103], v[140:143]\n"
      "buffer_load_dwordx4 v234, s[60:63], s82 offen offset:0 lds\n"
      "v_mfma_f32_16x16x32_bf16 v[140:143], a[136:139], a[104:107], v[140:143]\n"
      "v_mov_b32 v26, 0\n"
      "v_mov_b32 v27, 0\n"
      "v_mfma_f32_16x16x32_bf16 v[140:143], a[140:143], a[108:111], v[140:143]\n"
      "v_mov_b32 v28, 0\n"
      "v_mov_b32 v29, 0\n"
      "v_mfma_f32_16x16x32_bf16 v[140:143], a[144:147], a[112:115], v[140:143]\n"
      "buffer_load_dwordx4 v235, s[60:63], s82 offen offset:1024 lds\n"
      "v_mfma_f32_16x16x32_bf16 v[140:143], a[148:151], a[116:119], v[140:143]\n"
      "v_mov_b32 v30, 0\n"
      "v_mov_b32 v31, 0\n"
      "v_mfma_f32_16x16x32_bf16 v[140:143], a[152:155], a[120:123], v[140:143]\n"
      "v_mov_b32 v32, 0\n"
      "v_mov_b32 v33, 0\n"
      "v_mfma_f32_16x16x32_bf16 v[140:143], a[156:159], a[124:127], v[140:143]\n"
      "s_waitcnt lgkmcnt(0)\n"
      "v_mov_b32 v34, 0\n"
      "v_mov_b32 v35, 0\n"
      "v_mfma_f32_16x16x32_bf16 v[144:147], a[160:163], a[0:3], 0\n"
      "s_barrier\n"
      "v_mov_b32 v36, 0\n"
      "v_mfma_f32_16x16x32_bf16 v[144:147], a[164:167], a[4:7], v[144:147]\n"
      "v_mov_b32 v252, 0\n"
      "v_mov_b32 v37, 0\n"
      "v_mfma_f32_16x16x32_bf16 v[144:147], a[168:171], a[8:11], v[144:147]\n"
      "v_lshrrev_b32 v253, 2, v242\n"
      "v_mov_b32 v38, 0\n"
      "v_mfma_f32_16x16x32_bf16 v[144:147], a[172:175], a[12:15], v[144:147]\n"
      "v_add_u32 v253, 147456, v253\n"
      "v_mov_b32 v39, 0\n"
      "v_mfma_f32_16x16x32_bf16 v[144:147], a[176:179], a[16:19], v[144:147]\n"
      "ds_write_b32 v253, v252\n"
      "v_mfma_f32_16x16x32_bf16 v[144:147], a[180:183], a[20:23], v[144:147]\n"
      "buffer_load_dwordx4 v236, s[60:63], s82 offen offset:2048 lds\n"
      "v_mfma_f32_16x16x32_bf16 v[144:147], a[184:187], a[24:27], v[144:147]\n"
      "v_mov_b32 v40, 0\n"
      "v_mov_b32 v41, 0\n"
      "v_mfma_f32_16x16x32_bf16 v[144:147], a[188:191], a[28:31], v[144:147]\n"
      "s_add_u32 s96, s82, 4096\n"
      "v_mov_b32 v42, 0\n"
      "v_mov_b32 v43, 0\n"
      "v_mfma_f32_16x16x32_bf16 v[148:151], a[160:163], a[32:35], 0\n"
      "buffer_load_dwordx4 v237, s[60:63], s82 offen offset:3072 lds\n"
      "v_mfma_f32_16x16x32_bf16 v[148:151], a[164:167], a[36:39], v[148:151]\n"
      "v_mov_b32 v44, 0\n"
      "v_mov_b32 v45, 0\n"
      "v_mfma_f32_16x16x32_bf16 v[148:151], a[168:171], a[40:43], v[148:151]\n"
      "s_add_u32 m0, s81, 69632\n"
      "v_mov_b32 v46, 0\n"
      "v_mov_b32 v47, 0\n"
      "v_mfma_f32_16x16x32_bf16 v[148:151], a[172:175], a[44:47], v[148:151]\n"
      "buffer_load_dwordx4 v234, s[60:63], s96 offen offset:0 lds\n"
      "v_mfma_f32_16x16x32_bf16 v[148:151], a[176:179], a[48:51], v[148:151]\n"
      "v_mov_b32 v48, 0\n"
      "v_mov_b32 v49, 0\n"
      "v_mfma_f32_16x16x32_bf16 v[148:151], a[180:183], a[52:55], v[148:151]\n"
      "v_mov_b32 v50, 0\n"
      "v_mov_b32 v51, 0\n"
      "v_mfma_f32_16x16x32_bf16 v[148:151], a[184:187], a[56:59], v[148:151]\n"
      "buffer_load_dwordx4 v235, s[60:63], s96 offen offset:1024 lds\n"
      "v_mfma_f32_16x16x32_bf16 v[148:151], a[188:191], a[60:63], v[148:151]\n"
      "v_mov_b32 v52, 0\n"
      "v_mov_b32 v53, 0\n"
      "v_mfma_f32_16x16x32_bf16 v[152:155], a[160:163], a[64:67], 0\n"
      "v_mov_b32 v54, 0\n"
      "v_mov_b32 v55, 0\n"
      "v_mfma_f32_16x16x32_bf16 v[152:155], a[164:167], a[68:71], v[152:155]\n"
      "buffer_load_dwordx4 v236, s[60:63], s96 offen offset:2048 lds\n"
      "v_mfma_f32_16x16x32_bf16 v[152:155], a[168:171], a[72:75], v[152:155]\n"
      "v_mov_b32 v56, 0\n"
      "v_mov_b32 v57, 0\n"
      "v_mfma_f32_16x16x32_bf16 v[152:155], a[172:175], a[76:79], v[152:155]\n"
      "v_mov_b32 v58, 0\n"
      "v_mov_b32 v59, 0\n"
      "v_mfma_f32_16x16x32_bf16 v[152:155], a[176:179], a[80:83], v[152:155]\n"
      "buffer_load_dwordx4 v237, s[60:63], s96 offen offset:3072 lds\n"
      "v_mfma_f32_16x16x32_bf16 v[152:155], a[180:183], a[84:87], v[152:155]\n"
      "v_mov_b32 v60, 0\n"
      "v_mov_b32 v61, 0\n"
      "v_mfma_f32_16x16x32_bf16 v[152:155], a[184:187], a[88:91], v[152:155]\n"
      "v_mov_b32 v62, 0\n"
      "v_mov_b32 v63, 0\n"
      "v_mfma_f32_16x16x32_bf16 v[152:155], a[188:191], a[92:95], v[152:155]\n"
      "v_mov_b32 v64, 0\n"
      "v_mov_b32 v65, 0\n"
      "v_mfma_f32_16x16x32_bf16 v[156:159], a[160:163], a[96:99], 0\n"
      "v_mov_b32 v66, 0\n"
      "v_mov_b32 v67, 0\n"
      "v_mfma_f32_16x16x32_bf16 v[156:159], a[164:167], a[100:103], v[156:159]\n"
      "v_mov_b32 v68, 0\n"
      "v_mov_b32 v69, 0\n"
      "v_mfma_f32_16x16x32_bf16 v[156:159], a[168:171], a[104:107], v[156:159]\n"
      "v_mov_b32 v70, 0\n"
      "v_mov_b32 v71, 0\n"
      "v_mfma_f32_16x16x32_bf16 v[156:159], a[172:175], a[108:111], v[156:159]\n"
      "v_mov_b32 v72, 0\n"
      "v_mov_b32 v73, 0\n"
      "v_mfma_f32_16x16x32_bf16 v[156:159], a[176:179], a[112:115], v[156:159]\n"
      "v_mov_b32 v74, 0\n"
      "v_mov_b32 v75, 0\n"
      "v_mfma_f32_16x16x32_bf16 v[156:159], a[180:183], a[116:119], v[156:159]\n"
      "v_mov_b32 v76, 0\n"
      "v_mov_b32 v77, 0\n"
      "v_mfma_f32_16x16x32_bf16 v[156:159], a[184:187], a[120:123], v[156:159]\n"
      "v_mov_b32 v78, 0\n"
      "v_mov_b32 v79, 0\n"
      "v_mfma_f32_16x16x32_bf16 v[156:159], a[188:191], a[124:127], v[156:159]\n"
      "v_mov_b32 v80, 0\n"
      "v_mov_b32 v81, 0\n"
      "v_mov_b32 v82, 0\n"
      "v_mov_b32 v83, 0\n"
      "v_mov_b32 v84, 0\n"
      "v_mov_b32 v85, 0\n"
      "v_mov_b32 v86, 0\n"
      "v_mov_b32 v87, 0\n"
      "v_mov_b32 v88, 0\n"
      "v_mov_b32 v89, 0\n"
      "v_max3_f32 v244, v128, v129, v130\n"
      "v_max3_f32 v244, v244, v131, v144\n"
      "v_max3_f32 v244, v244, v145, v146\n"
      "v_max_f32 v244, v244, v147\n"
      "v_max3_f32 v245, v132, v133, v134\n"
      "v_max3_f32 v245, v245, v135, v148\n"
      "v_max3_f32 v245, v245, v149, v150\n"
      "v_max_f32 v245, v245, v151\n"
      "v_max3_f32 v246, v136, v137, v138\n"
      "v_max3_f32 v246, v246, v139, v152\n"
      "v_max3_f32 v246, v246, v153, v154\n"
      "v_max_f32 v246, v246, v155\n"
      "v_max3_f32 v247, v140, v141, v142\n"
      "v_max3_f32 v247, v247, v143, v156\n"
      "v_max3_f32 v247, v247, v157, v158\n"
      "v_max_f32 v247, v247, v159\n"
      "v_mov_b32 v248, v244\n"
      "v_mov_b32 v249, v245\n"
      "v_mov_b32 v250, v246\n"
      "v_mov_b32 v251, v247\n"
      "s_nop 1\n"
      "v_permlane32_swap_b32 v244, v248\n"
      "v_permlane32_swap_b32 v245, v249\n"
      "v_permlane32_swap_b32 v246, v250\n"
      "v_permlane32_swap_b32 v247, v251\n"
      "s_nop 1\n"
      "v_max_f32 v244, v244, v248\n"
      "v_max_f32 v245, v245, v249\n"
      "v_max_f32 v246, v246, v250\n"
      "v_max_f32 v247, v247, v251\n"
      "v_mov_b32 v248, v244\n"
      "v_mov_b32 v249, v245\n"
      "v_mov_b32 v250, v246\n"
      "v_mov_b32 v251, v247\n"
      "s_nop 1\n"
      "v_permlane16_swap_b32 v244, v248\n"
      "v_permlane16_swap_b32 v245, v249\n"
      "v_permlane16_swap_b32 v246, v250\n"
      "v_permlane16_swap_b32 v247, v251\n"
      "s_nop 1\n"
      "v_max_f32 v244, v244, v248\n"
      "v_max_f32 v245, v245, v249\n"
      "v_max_f32 v246, v246, v250\n"
      "v_max_f32 v247, v247, v251\n"
      "v_ceil_f32 v244, v244\n"
      "v_add_f32 v244, 0x42cc0000, v244\n"
      "v_ceil_f32 v245, v245\n"
      "v_add_f32 v245, 0x42cc0000, v245\n"
      "v_ceil_f32 v246, v246\n"
      "v_add_f32 v246, 0x42cc0000, v246\n"
      "v_ceil_f32 v247, v247\n"
      "v_add_f32 v247, 0x42cc0000, v247\n"
      "v_add_u32 v243, s99, v242\n"
      "v_add_u32 v252, s97, v242\n"
      "ds_write_b128 v243, v[244:247] offset:1024\n"
      "s_waitcnt vmcnt(0) lgkmcnt(0)\n"
      "s_barrier\n"
      "ds_read_b128 v[248:251], v252 offset:1024\n"
      "ds_read_b128 a[128:131], v218 offset:0\n"
      "ds_read_b128 a[132:135], v219 offset:0\n"
      "ds_read_b128 a[136:139], v220 offset:0\n"
      "ds_read_b128 a[140:143], v221 offset:0\n"
      "ds_read_b128 a[144:147], v218 offset:256\n"
      "ds_read_b128 a[148:151], v219 offset:256\n"
      "ds_read_b128 a[152:155], v220 offset:256\n"
      "ds_read_b128 a[156:159], v221 offset:256\n"
      "ds_read_b128 a[160:163], v218 offset:8192\n"
      "ds_read_b128 a[164:167], v219 offset:8192\n"
      "ds_read_b128 a[168:171], v220 offset:8192\n"
      "ds_read_b128 a[172:175], v221 offset:8192\n"
      "ds_read_b128 a[176:179], v218 offset:8448\n"
      "ds_read_b128 a[180:183], v219 offset:8448\n"
      "ds_read_b128 a[184:187], v220 offset:8448\n"
      "ds_read_b128 a[188:191], v221 offset:8448\n"
      "v_mov_b32 v90, 0\n"
      "v_mov_b32 v91, 0\n"
      "v_mov_b32 v92, 0\n"
      "v_mov_b32 v93, 0\n"
      "v_mov_b32 v94, 0\n"
      "v_mov_b32 v95, 0\n"
      "v_mov_b32 v96, 0\n"
      "v_mov_b32 v97, 0\n"
      "v_mov_b32 v98, 0\n"
      "v_mov_b32 v99, 0\n"
      "v_mov_b32 v100, 0\n"
      "v_mov_b32 v101, 0\n"
      "v_mov_b32 v102, 0\n"
      "v_mov_b32 v103, 0\n"
      "v_mov_b32 v104, 0\n"
      "v_mov_b32 v105, 0\n"
      "v_mov_b32 v106, 0\n"
      "v_mov_b32 v107, 0\n"
      "v_mov_b32 v108, 0\n"
      "v_mov_b32 v109, 0\n"
      "v_mov_b32 v110, 0\n"
      "v_mov_b32 v111, 0\n"
      "v_mov_b32 v112, 0\n"
      "v_mov_b32 v113, 0\n"
      "v_mov_b32 v114, 0\n"
      "v_mov_b32 v115, 0\n"
      "v_mov_b32 v116, 0\n"
      "v_mov_b32 v117, 0\n"
      "v_mov_b32 v118, 0\n"
      "v_mov_b32 v119, 0\n"
      "v_mov_b32 v120, 0\n"
      "v_mov_b32 v121, 0\n"
      "v_mov_b32 v122, 0\n"
      "v_mov_b32 v123, 0\n"
      "v_mov_b32 v124, 0\n"
      "v_mov_b32 v125, 0\n"
      "v_mov_b32 v126, 0\n"
      "v_mov_b32 v127, 0\n"
      "v_mov_b32 v208, 0\n"
      "v_mov_b32 v209, 0\n"
      "v_mov_b32 v210, 0\n"
      "v_mov_b32 v211, 0\n"
      "v_mov_b32 v212, 0\n"
      "v_mov_b32 v213, 0\n"
      "v_mov_b32 v214, 0\n"
      "v_mov_b32 v215, 0\n"
      "s_waitcnt lgkmcnt(15)\n"
      "v_max_f32 v244, v244, v248\n"
      "v_sub_f32 v192, 0, v244\n"
      "v_sub_f32 v193, 0, v244\n"
      "v_sub_f32 v194, 0, v244\n"
      "v_sub_f32 v195, 0, v244\n"
      "v_sub_f32 v128, v128, v244\n"
      "v_sub_f32 v129, v129, v244\n"
      "v_sub_f32 v130, v130, v244\n"
      "v_sub_f32 v131, v131, v244\n"
      "v_sub_f32 v144, v144, v244\n"
      "v_sub_f32 v145, v145, v244\n"
      "v_sub_f32 v146, v146, v244\n"
      "v_sub_f32 v147, v147, v244\n"
      "v_max_f32 v245, v245, v249\n"
      "v_sub_f32 v196, 0, v245\n"
      "v_sub_f32 v197, 0, v245\n"
      "v_sub_f32 v198, 0, v245\n"
      "v_sub_f32 v199, 0, v245\n"
      "v_sub_f32 v132, v132, v245\n"
      "v_sub_f32 v133, v133, v245\n"
      "v_sub_f32 v134, v134, v245\n"
      "v_sub_f32 v135, v135, v245\n"
      "v_sub_f32 v148, v148, v245\n"
      "v_sub_f32 v149, v149, v245\n"
      "v_sub_f32 v150, v150, v245\n"
      "v_sub_f32 v151, v151, v245\n"
      "v_max_f32 v246, v246, v250\n"
      "v_sub_f32 v200, 0, v246\n"
      "v_sub_f32 v201, 0, v246\n"
      "v_sub_f32 v202, 0, v246\n"
      "v_sub_f32 v203, 0, v246\n"
      "v_sub_f32 v136, v136, v246\n"
      "v_sub_f32 v137, v137, v246\n"
      "v_sub_f32 v138, v138, v246\n"
      "v_sub_f32 v139, v139, v246\n"
      "v_sub_f32 v152, v152, v246\n"
      "v_sub_f32 v153, v153, v246\n"
      "v_sub_f32 v154, v154, v246\n"
      "v_sub_f32 v155, v155, v246\n"
      "v_max_f32 v247, v247, v251\n"
      "v_sub_f32 v204, 0, v247\n"
      "v_sub_f32 v205, 0, v247\n"
      "v_sub_f32 v206, 0, v247\n"
      "v_sub_f32 v207, 0, v247\n"
      "v_sub_f32 v140, v140, v247\n"
      "v_sub_f32 v141, v141, v247\n"
      "v_sub_f32 v142, v142, v247\n"
      "v_sub_f32 v143, v143, v247\n"
      "v_sub_f32 v156, v156, v247\n"
      "v_sub_f32 v157, v157, v247\n"
      "v_sub_f32 v158, v158, v247\n"
      "v_sub_f32 v159, v159, v247\n"
      "v_exp_f32 v128, v128\n"
      "s_add_u32 s82, s82, 65536\n"
      "s_add_u32 m0, s81, 32768\n"
      "ds_read_b64_tr_b16 a[192:193], v222 offset:0\n"
      "buffer_load_dwordx4 v230, s[56:59], s82 offen offset:0 lds\n"
      "ds_read_b64_tr_b16 a[194:195], v222 offset:8192\n"
      "v_add_f32 v208, v208, v128\n"
      "v_exp_f32 v129, v129\n"
      "ds_read_b64_tr_b16 a[196:197], v222 offset:16384\n"
      "v_add_f32 v208, v208, v129\n"
      "ds_read_b64_tr_b16 a[198:199], v222 offset:24576\n"
      "v_cvt_pk_bf16_f32 v128, v128, v129\n"
      "buffer_load_dwordx4 v231, s[56:59], s82 offen offset:1024 lds\n"
      "v_exp_f32 v130, v130\n"
      "ds_read_b64_tr_b16 a[200:201], v223 offset:0\n"
      "v_add_f32 v208, v208, v130\n"
      "v_exp_f32 v131, v131\n"
      "ds_read_b64_tr_b16 a[202:203], v223 offset:8192\n"
      "v_add_f32 v208, v208, v131\n"
      "buffer_load_dwordx4 v232, s[56:59], s82 offen offset:2048 lds\n"
      "ds_read_b64_tr_b16 a[204:205], v223 offset:16384\n"
      "v_cvt_pk_bf16_f32 v129, v130, v131\n"
      "v_exp_f32 v144, v144\n"
      "ds_read_b64_tr_b16 a[206:207], v223 offset:24576\n"
      "v_add_f32 v208, v208, v144\n"
      "s_add_u32 s96, s82, 4096\n"
      "v_exp_f32 v145, v145\n"
      "buffer_load_dwordx4 v233, s[56:59], s82 offen offset:3072 lds\n"
      "ds_read_b64_tr_b16 a[208:209], v224 offset:0\n"
      "v_add_f32 v208, v208, v145\n"
      "ds_read_b64_tr_b16 a[210:211], v224 offset:8192\n"
      "v_cvt_pk_bf16_f32 v130, v144, v145\n"
      "v_exp_f32 v146, v146\n"
      "s_add_u32 m0, s81, 36864\n"
      "ds_read_b64_tr_b16 a[212:213], v224 offset:16384\n"
      "v_add_f32 v208, v208, v146\n"
      "buffer_load_dwordx4 v230, s[56:59], s96 offen offset:0 lds\n"
      "v_exp_f32 v147, v147\n"
      "ds_read_b64_tr_b16 a[214:215], v224 offset:24576\n"
      "v_add_f32 v208, v208, v147\n"
      "v_exp_f32 v132, v132\n"
      "s_nop 0\n"
      "v_cvt_pk_bf16_f32 v131, v146, v147\n"
      "v_add_f32 v209, v209, v132\n"
      "buffer_load_dwordx4 v231, s[56:59], s96 offen offset:1024 lds\n"
      "v_exp_f32 v133, v133\n"
      "ds_read_b64_tr_b16 a[216:217], v225 offset:0\n"
      "v_add_f32 v209, v209, v133\n"
      "ds_read_b64_tr_b16 a[218:219], v225 offset:8192\n"
      "v_cvt_pk_bf16_f32 v132, v132, v133\n"
      "v_exp_f32 v134, v134\n"
      "buffer_load_dwordx4 v232, s[56:59], s96 offen offset:2048 lds\n"
      "ds_read_b64_tr_b16 a[220:221], v225 offset:16384\n"
      "v_add_f32 v209, v209, v134\n"
      "v_exp_f32 v135, v135\n"
      "ds_read_b64_tr_b16 a[222:223], v225 offset:24576\n"
      "v_add_f32 v209, v209, v135\n"
      "ds_read_b64_tr_b16 a[224:225], v226 offset:0\n"
      "v_cvt_pk_bf16_f32 v133, v134, v135\n"
      "buffer_load_dwordx4 v233, s[56:59], s96 offen offset:3072 lds\n"
      "v_exp_f32 v148, v148\n"
      "ds_read_b64_tr_b16 a[226:227], v226 offset:8192\n"
      "v_add_f32 v209, v209, v148\n"
      "v_exp_f32 v149, v149\n"
      "s_add_u32 m0, s81, 98304\n"
      "ds_read_b64_tr_b16 a[228:229], v226 offset:16384\n"
      "v_add_f32 v209, v209, v149\n"
      "buffer_load_dwordx4 v234, s[60:63], s83 offen offset:0 lds\n"
      "ds_read_b64_tr_b16 a[230:231], v226 offset:24576\n"
      "v_cvt_pk_bf16_f32 v134, v148, v149\n"
      "v_exp_f32 v150, v150\n"
      "ds_read_b64_tr_b16 a[232:233], v227 offset:0\n"
      "v_add_f32 v209, v209, v150\n"
      "v_exp_f32 v151, v151\n"
      "buffer_load_dwordx4 v235, s[60:63], s83 offen offset:1024 lds\n"
      "ds_read_b64_tr_b16 a[234:235], v227 offset:8192\n"
      "v_add_f32 v209, v209, v151\n"
      "s_waitcnt lgkmcnt(15)\n"
      "ds_read_b64_tr_b16 a[236:237], v227 offset:16384\n"
      "v_cvt_pk_bf16_f32 v135, v150, v151\n"
      "v_mfma_f32_16x16x32_bf16 v[160:163], a[128:131], a[0:3], v[192:195]\n"
      "v_exp_f32 v136, v136\n"
      "v_mfma_f32_16x16x32_bf16 v[160:163], a[132:135], a[4:7], v[160:163]\n"
      "ds_read_b64_tr_b16 a[238:239], v227 offset:24576\n"
      "v_add_f32 v210, v210, v136\n"
      "v_mfma_f32_16x16x32_bf16 v[160:163], a[136:139], a[8:11], v[160:163]\n"
      "buffer_load_dwordx4 v236, s[60:63], s83 offen offset:2048 lds\n"
      "v_mfma_f32_16x16x32_bf16 v[160:163], a[140:143], a[12:15], v[160:163]\n"
      "v_exp_f32 v137, v137\n"
      "v_mfma_f32_16x16x32_bf16 v[160:163], a[144:147], a[16:19], v[160:163]\n"
      "v_add_f32 v210, v210, v137\n"
      "v_cvt_pk_bf16_f32 v136, v136, v137\n"
      "v_mfma_f32_16x16x32_bf16 v[160:163], a[148:151], a[20:23], v[160:163]\n"
      "v_exp_f32 v138, v138\n"
      "v_mfma_f32_16x16x32_bf16 v[160:163], a[152:155], a[24:27], v[160:163]\n"
      "s_add_u32 s96, s83, 4096\n"
      "ds_read_b64_tr_b16 a[240:241], v228 offset:0\n"
      "v_add_f32 v210, v210, v138\n"
      "v_mfma_f32_16x16x32_bf16 v[160:163], a[156:159], a[28:31], v[160:163]\n"
      "buffer_load_dwordx4 v237, s[60:63], s83 offen offset:3072 lds\n"
      "v_mfma_f32_16x16x32_bf16 v[164:167], a[128:131], a[32:35], v[196:199]\n"
      "v_exp_f32 v139, v139\n"
      "v_mfma_f32_16x16x32_bf16 v[164:167], a[132:135], a[36:39], v[164:167]\n"
      "ds_read_b64_tr_b16 a[242:243], v228 offset:8192\n"
      "v_add_f32 v210, v210, v139\n"
      "v_mfma_f32_16x16x32_bf16 v[164:167], a[136:139], a[40:43], v[164:167]\n"
      "ds_read_b64_tr_b16 a[244:245], v228 offset:16384\n"
      "v_cvt_pk_bf16_f32 v137, v138, v139\n"
      "v_mfma_f32_16x16x32_bf16 v[164:167], a[140:143], a[44:47], v[164:167]\n"
      "s_add_u32 m0, s81, 102400\n"
      "v_exp_f32 v152, v152\n"
      "v_mfma_f32_16x16x32_bf16 v[164:167], a[144:147], a[48:51], v[164:167]\n"
      "buffer_load_dwordx4 v234, s[60:63], s96 offen offset:0 lds\n"
      "v_mfma_f32_16x16x32_bf16 v[164:167], a[148:151], a[52:55], v[164:167]\n"
      "ds_read_b64_tr_b16 a[246:247], v228 offset:24576\n"
      "v_add_f32 v210, v210, v152\n"
      "v_mfma_f32_16x16x32_bf16 v[164:167], a[152:155], a[56:59], v[164:167]\n"
      "v_exp_f32 v153, v153\n"
      "v_mfma_f32_16x16x32_bf16 v[164:167], a[156:159], a[60:63], v[164:167]\n"
      "ds_read_b64_tr_b16 a[248:249], v229 offset:0\n"
      "v_add_f32 v210, v210, v153\n"
      "v_mfma_f32_16x16x32_bf16 v[168:171], a[128:131], a[64:67], v[200:203]\n"
      "ds_read_b64_tr_b16 a[250:251], v229 offset:8192\n"
      "v_cvt_pk_bf16_f32 v138, v152, v153\n"
      "v_mfma_f32_16x16x32_bf16 v[168:171], a[132:135], a[68:71], v[168:171]\n"
      "buffer_load_dwordx4 v235, s[60:63], s96 offen offset:1024 lds\n"
      "v_mfma_f32_16x16x32_bf16 v[168:171], a[136:139], a[72:75], v[168:171]\n"
      "ds_read_b64_tr_b16 a[252:253], v229 offset:16384\n"
      "v_max3_f32 v216, v160, v161, v161\n"
      "v_mfma_f32_16x16x32_bf16 v[168:171], a[140:143], a[76:79], v[168:171]\n"
      "ds_read_b64_tr_b16 a[254:255], v229 offset:24576\n"
      "v_max3_f32 v216, v162, v163, v216\n"
      "v_mfma_f32_16x16x32_bf16 v[168:171], a[144:147], a[80:83], v[168:171]\n"
      "v_exp_f32 v154, v154\n"
      "v_mfma_f32_16x16x32_bf16 v[168:171], a[148:151], a[84:87], v[168:171]\n"
      "v_exp_f32 v155, v155\n"
      "v_mfma_f32_16x16x32_bf16 v[168:171], a[152:155], a[88:91], v[168:171]\n"
      "buffer_load_dwordx4 v236, s[60:63], s96 offen offset:2048 lds\n"
      "v_mfma_f32_16x16x32_bf16 v[168:171], a[156:159], a[92:95], v[168:171]\n"
      "v_add_f32 v210, v210, v154\n"
      "v_add_f32 v210, v210, v155\n"
      "v_mfma_f32_16x16x32_bf16 v[172:175], a[128:131], a[96:99], v[204:207]\n"
      "v_exp_f32 v140, v140\n"
      "v_mfma_f32_16x16x32_bf16 v[172:175], a[132:135], a[100:103], v[172:175]\n"
      "v_cvt_pk_bf16_f32 v139, v154, v155\n"
      "v_add_f32 v211, v211, v140\n"
      "v_mfma_f32_16x16x32_bf16 v[172:175], a[136:139], a[104:107], v[172:175]\n"
      "v_exp_f32 v141, v141\n"
      "v_mfma_f32_16x16x32_bf16 v[172:175], a[140:143], a[108:111], v[172:175]\n"
      "buffer_load_dwordx4 v237, s[60:63], s96 offen offset:3072 lds\n"
      "v_mfma_f32_16x16x32_bf16 v[172:175], a[144:147], a[112:115], v[172:175]\n"
      "v_add_f32 v211, v211, v141\n"
      "v_cvt_pk_bf16_f32 v140, v140, v141\n"
      "v_mfma_f32_16x16x32_bf16 v[172:175], a[148:151], a[116:119], v[172:175]\n"
      "v_exp_f32 v142, v142\n"
      "v_mfma_f32_16x16x32_bf16 v[172:175], a[152:155], a[120:123], v[172:175]\n"
      "v_exp_f32 v143, v143\n"
      "v_mfma_f32_16x16x32_bf16 v[172:175], a[156:159], a[124:127], v[172:175]\n"
      "v_add_f32 v211, v211, v142\n"
      "v_add_f32 v211, v211, v143\n"
      "v_mfma_f32_16x16x32_bf16 v[176:179], a[160:163], a[0:3], v[192:195]\n"
      "ds_write_b128 v238, v[128:131] offset:0\n"
      "v_mfma_f32_16x16x32_bf16 v[176:179], a[164:167], a[4:7], v[176:179]\n"
      "v_exp_f32 v156, v156\n"
      "v_mfma_f32_16x16x32_bf16 v[176:179], a[168:171], a[8:11], v[176:179]\n"
      "v_cvt_pk_bf16_f32 v141, v142, v143\n"
      "v_add_f32 v211, v211, v156\n"
      "v_mfma_f32_16x16x32_bf16 v[176:179], a[172:175], a[12:15], v[176:179]\n"
      "ds_write_b128 v238, v[132:135] offset:1024\n"
      "v_mfma_f32_16x16x32_bf16 v[176:179], a[176:179], a[16:19], v[176:179]\n"
      "v_exp_f32 v157, v157\n"
      "v_mfma_f32_16x16x32_bf16 v[176:179], a[180:183], a[20:23], v[176:179]\n"
      "v_add_f32 v211, v211, v157\n"
      "v_cvt_pk_bf16_f32 v142, v156, v157\n"
      "v_mfma_f32_16x16x32_bf16 v[176:179], a[184:187], a[24:27], v[176:179]\n"
      "ds_write_b128 v238, v[136:139] offset:2048\n"
      "v_mfma_f32_16x16x32_bf16 v[176:179], a[188:191], a[28:31], v[176:179]\n"
      "v_exp_f32 v158, v158\n"
      "v_mfma_f32_16x16x32_bf16 v[180:183], a[160:163], a[32:35], v[196:199]\n"
      "v_exp_f32 v159, v159\n"
      "v_mfma_f32_16x16x32_bf16 v[180:183], a[164:167], a[36:39], v[180:183]\n"
      "v_add_f32 v211, v211, v158\n"
      "v_add_f32 v211, v211, v159\n"
      "v_mfma_f32_16x16x32_bf16 v[180:183], a[168:171], a[40:43], v[180:183]\n"
      "v_cvt_pk_bf16_f32 v143, v158, v159\n"
      "v_max3_f32 v216, v164, v165, v216\n"
      "v_mfma_f32_16x16x32_bf16 v[180:183], a[172:175], a[44:47], v[180:183]\n"
      "ds_write_b128 v238, v[140:143] offset:3072\n"
      "v_mfma_f32_16x16x32_bf16 v[180:183], a[176:179], a[48:51], v[180:183]\n"
      "v_max3_f32 v216, v166, v167, v216\n"
      "v_max3_f32 v216, v168, v169, v216\n"
      "v_mfma_f32_16x16x32_bf16 v[180:183], a[180:183], a[52:55], v[180:183]\n"
      "v_max3_f32 v216, v170, v171, v216\n"
      "v_max3_f32 v216, v172, v173, v216\n"
      "v_mfma_f32_16x16x32_bf16 v[180:183], a[184:187], a[56:59], v[180:183]\n"
      "s_waitcnt lgkmcnt(0)\n"
      "s_barrier\n"
      "v_mfma_f32_16x16x32_bf16 v[180:183], a[188:191], a[60:63], v[180:183]\n"
      "ds_read_b32 v240, v241 offset:0\n"
      "v_max3_f32 v216, v174, v175, v216\n"
      "v_mfma_f32_16x16x32_bf16 v[184:187], a[160:163], a[64:67], v[200:203]\n"
      "ds_read_b128 v[144:147], v239 offset:0\n"
      "v_max3_f32 v216, v176, v177, v216\n"
      "v_mfma_f32_16x16x32_bf16 v[184:187], a[164:167], a[68:71], v[184:187]\n"
      "ds_read_b128 v[148:151], v239 offset:1024\n"
      "v_max3_f32 v216, v178, v179, v216\n"
      "v_mfma_f32_16x16x32_bf16 v[184:187], a[168:171], a[72:75], v[184:187]\n"
      "ds_read_b128 v[152:155], v239 offset:2048\n"
      "v_mfma_f32_16x16x32_bf16 v[184:187], a[172:175], a[76:79], v[184:187]\n"
      "ds_read_b128 v[156:159], v239 offset:3072\n"
      "v_mfma_f32_16x16x32_bf16 v[184:187], a[176:179], a[80:83], v[184:187]\n"
      "v_mfma_f32_16x16x32_bf16 v[184:187], a[180:183], a[84:87], v[184:187]\n"
      "s_waitcnt lgkmcnt(4)\n"
      "v_cmp_eq_u32 vcc, s88, v240\n"
      "v_mfma_f32_16x16x32_bf16 v[184:187], a[184:187], a[88:91], v[184:187]\n"
      "v_mfma_f32_16x16x32_bf16 v[184:187], a[188:191], a[92:95], v[184:187]\n"
      "v_max3_f32 v216, v180, v181, v216\n"
      "v_max3_f32 v216, v182, v183, v216\n"
      "v_mfma_f32_16x16x32_bf16 v[188:191], a[160:163], a[96:99], v[204:207]\n"
      "v_mfma_f32_16x16x32_bf16 v[188:191], a[164:167], a[100:103], v[188:191]\n"
      "v_mfma_f32_16x16x32_bf16 v[188:191], a[168:171], a[104:107], v[188:191]\n"
      "v_mfma_f32_16x16x32_bf16 v[188:191], a[172:175], a[108:111], v[188:191]\n"
      "v_mfma_f32_16x16x32_bf16 v[188:191], a[176:179], a[112:115], v[188:191]\n"
      "v_mfma_f32_16x16x32_bf16 v[188:191], a[180:183], a[116:119], v[188:191]\n"
      "v_mfma_f32_16x16x32_bf16 v[188:191], a[184:187], a[120:123], v[188:191]\n"
      "v_mfma_f32_16x16x32_bf16 v[188:191], a[188:191], a[124:127], v[188:191]\n"
      "s_waitcnt lgkmcnt(0)\n"
      "s_cbranch_vccnz R2_1_%=\n"
      "B2_2_%=:\n"
      "s_mov_b32 s90, 15\n"
      "LOOP_%=:\n"
      "v_mfma_f32_16x16x32_bf16 v[0:3], a[192:195], v[128:131], v[0:3]\n"
      "s_add_u32 s88, s88, 1\n"
      "v_max3_f32 v216, v184, v185, v216\n"
      "v_max3_f32 v216, v186, v187, v216\n"
      "v_mfma_f32_16x16x32_bf16 v[0:3], a[196:199], v[144:147], v[0:3]\n"
      "s_add_u32 s83, s83, 65536\n"
      "s_add_u32 m0, s81, 0\n"
      "v_mfma_f32_16x16x32_bf16 v[4:7], a[192:195], v[132:135], v[4:7]\n"
      "buffer_load_dwordx4 v230, s[56:59], s83 offen offset:0 lds\n"
      "v_mfma_f32_16x16x32_bf16 v[4:7], a[196:199], v[148:151], v[4:7]\n"
      "v_mfma_f32_16x16x32_bf16 v[8:11], a[192:195], v[136:139], v[8:11]\n"
      "v_max3_f32 v216, v188, v189, v216\n"
      "v_max3_f32 v216, v190, v191, v216\n"
      "v_mfma_f32_16x16x32_bf16 v[8:11], a[196:199], v[152:155], v[8:11]\n"
      "v_cmp_lt_f32 vcc, s89, v216\n"
      "v_mfma_f32_16x16x32_bf16 v[12:15], a[192:195], v[140:143], v[12:15]\n"
      "v_mfma_f32_16x16x32_bf16 v[12:15], a[196:199], v[156:159], v[12:15]\n"
      "s_cbranch_vccnz R1_3_%=\n"
      "B1_4_%=:\n"
      "v_mfma_f32_16x16x32_bf16 v[16:19], a[200:203], v[128:131], v[16:19]\n"
      "buffer_load_dwordx4 v231, s[56:59], s83 offen offset:1024 lds\n"
      "v_mfma_f32_16x16x32_bf16 v[16:19], a[204:207], v[144:147], v[16:19]\n"
      "v_exp_f32 v160, v160\n"
      "v_mfma_f32_16x16x32_bf16 v[20:23], a[200:203], v[132:135], v[20:23]\n"
      "v_exp_f32 v161, v161\n"
      "v_mfma_f32_16x16x32_bf16 v[20:23], a[204:207], v[148:151], v[20:23]\n"
      "v_add_f32 v208, v208, v160\n"
      "v_add_f32 v208, v208, v161\n"
      "v_mfma_f32_16x16x32_bf16 v[24:27], a[200:203], v[136:139], v[24:27]\n"
      "v_exp_f32 v162, v162\n"
      "v_mfma_f32_16x16x32_bf16 v[24:27], a[204:207], v[152:155], v[24:27]\n"
      "buffer_load_dwordx4 v232, s[56:59], s83 offen offset:2048 lds\n"
      "v_mfma_f32_16x16x32_bf16 v[28:31], a[200:203], v[140:143], v[28:31]\n"
      "s_waitcnt vmcnt(3)\n"
      "s_barrier\n"
      "v_mfma_f32_16x16x32_bf16 v[28:31], a[204:207], v[156:159], v[28:31]\n"
      "ds_read_b128 a[128:131], v218 offset:32768\n"
      "v_cvt_pk_bf16_f32 v160, v160, v161\n"
      "v_mfma_f32_16x16x32_bf16 v[32:35], a[208:211], v[128:131], v[32:35]\n"
      "ds_read_b128 a[132:135], v219 offset:32768\n"
      "v_add_f32 v208, v208, v162\n"
      "v_mfma_f32_16x16x32_bf16 v[32:35], a[212:215], v[144:147], v[32:35]\n"
      "s_add_u32 s96, s83, 4096\n"
      "v_exp_f32 v163, v163\n"
      "v_mfma_f32_16x16x32_bf16 v[36:39], a[208:211], v[132:135], v[36:39]\n"
      "buffer_load_dwordx4 v233, s[56:59], s83 offen offset:3072 lds\n"
      "v_mfma_f32_16x16x32_bf16 v[36:39], a[212:215], v[148:151], v[36:39]\n"
      "ds_read_b128 a[136:139], v220 offset:32768\n"
      "v_add_f32 v208, v208, v163\n"
      "v_mfma_f32_16x16x32_bf16 v[40:43], a[208:211], v[136:139], v[40:43]\n"
      "ds_read_b128 a[140:143], v221 offset:32768\n"
      "v_cvt_pk_bf16_f32 v161, v162, v163\n"
      "v_mfma_f32_16x16x32_bf16 v[40:43], a[212:215], v[152:155], v[40:43]\n"
      "v_exp_f32 v176, v176\n"
      "v_mfma_f32_16x16x32_bf16 v[44:47], a[208:211], v[140:143], v[44:47]\n"
      "s_add_u32 m0, s81, 4096\n"
      "ds_read_b128 a[144:147], v218 offset:33024\n"
      "v_add_f32 v208, v208, v176\n"
      "v_mfma_f32_16x16x32_bf16 v[44:47], a[212:215], v[156:159], v[44:47]\n"
      "buffer_load_dwordx4 v230, s[56:59], s96 offen offset:0 lds\n"
      "v_mfma_f32_16x16x32_bf16 v[48:51], a[216:219], v[128:131], v[48:51]\n"
      "v_exp_f32 v177, v177\n"
      "v_mfma_f32_16x16x32_bf16 v[48:51], a[220:223], v[144:147], v[48:51]\n"
      "ds_read_b128 a[148:151], v219 offset:33024\n"
      "v_add_f32 v208, v208, v177\n"
      "v_mfma_f32_16x16x32_bf16 v[52:55], a[216:219], v[132:135], v[52:55]\n"
      "ds_read_b128 a[152:155], v220 offset:33024\n"
      "v_cvt_pk_bf16_f32 v162, v176, v177\n"
      "v_mfma_f32_16x16x32_bf16 v[52:55], a[220:223], v[148:151], v[52:55]\n"
      "v_exp_f32 v178, v178\n"
      "v_mfma_f32_16x16x32_bf16 v[56:59], a[216:219], v[136:139], v[56:59]\n"
      "buffer_load_dwordx4 v231, s[56:59], s96 offen offset:1024 lds\n"
      "v_mfma_f32_16x16x32_bf16 v[56:59], a[220:223], v[152:155], v[56:59]\n"
      "ds_read_b128 a[156:159], v221 offset:33024\n"
      "v_add_f32 v208, v208, v178\n"
      "v_mfma_f32_16x16x32_bf16 v[60:63], a[216:219], v[140:143], v[60:63]\n"
      "v_exp_f32 v179, v179\n"
      "v_mfma_f32_16x16x32_bf16 v[60:63], a[220:223], v[156:159], v[60:63]\n"
      "ds_read_b128 a[160:163], v218 offset:40960\n"
      "v_add_f32 v208, v208, v179\n"
      "v_mfma_f32_16x16x32_bf16 v[64:67], a[224:227], v[128:131], v[64:67]\n"
      "ds_read_b128 a[164:167], v219 offset:40960\n"
      "v_cvt_pk_bf16_f32 v163, v178, v179\n"
      "v_mfma_f32_16x16x32_bf16 v[64:67], a[228:231], v[144:147], v[64:67]\n"
      "buffer_load_dwordx4 v232, s[56:59], s96 offen offset:2048 lds\n"
      "v_mfma_f32_16x16x32_bf16 v[68:71], a[224:227], v[132:135], v[68:71]\n"
      "v_exp_f32 v164, v164\n"
      "v_mfma_f32_16x16x32_bf16 v[68:71], a[228:231], v[148:151], v[68:71]\n"
      "ds_read_b128 a[168:171], v220 offset:40960\n"
      "v_add_f32 v209, v209, v164\n"
      "v_mfma_f32_16x16x32_bf16 v[72:75], a[224:227], v[136:139], v[72:75]\n"
      "v_exp_f32 v165, v165\n"
      "v_mfma_f32_16x16x32_bf16 v[72:75], a[228:231], v[152:155], v[72:75]\n"
      "ds_read_b128 a[172:175], v221 offset:40960\n"
      "v_add_f32 v209, v209, v165\n"
      "v_mfma_f32_16x16x32_bf16 v[76:79], a[224:227], v[140:143], v[76:79]\n"
      "buffer_load_dwordx4 v233, s[56:59], s96 offen offset:3072 lds\n"
      "v_mfma_f32_16x16x32_bf16 v[76:79], a[228:231], v[156:159], v[76:79]\n"
      "v_exp_f32 v166, v166\n"
      "v_mfma_f32_16x16x32_bf16 v[80:83], a[232:235], v[128:131], v[80:83]\n"
      "v_cvt_pk_bf16_f32 v164, v164, v165\n"
      "v_add_f32 v209, v209, v166\n"
      "v_mfma_f32_16x16x32_bf16 v[80:83], a[236:239], v[144:147], v[80:83]\n"
      "v_exp_f32 v167, v167\n"
      "v_mfma_f32_16x16x32_bf16 v[84:87], a[232:235], v[132:135], v[84:87]\n"
      "s_add_u32 m0, s81, 65536\n"
      "ds_read_b128 a[176:179], v218 offset:41216\n"
      "v_add_f32 v209, v209, v167\n"
      "v_mfma_f32_16x16x32_bf16 v[84:87], a[236:239], v[148:151], v[84:87]\n"
      "buffer_load_dwordx4 v234, s[60:63], s82 offen offset:0 lds\n"
      "v_mfma_f32_16x16x32_bf16 v[88:91], a[232:235], v[136:139], v[88:91]\n"
      "ds_read_b128 a[180:183], v219 offset:41216\n"
      "v_cvt_pk_bf16_f32 v165, v166, v167\n"
      "v_mfma_f32_16x16x32_bf16 v[88:91], a[236:239], v[152:155], v[88:91]\n"
      "v_exp_f32 v180, v180\n"
      "v_mfma_f32_16x16x32_bf16 v[92:95], a[232:235], v[140:143], v[92:95]\n"
      "ds_read_b128 a[184:187], v220 offset:41216\n"
      "v_add_f32 v209, v209, v180\n"
      "v_mfma_f32_16x16x32_bf16 v[92:95], a[236:239], v[156:159], v[92:95]\n"
      "v_exp_f32 v181, v181\n"
      "v_mfma_f32_16x16x32_bf16 v[96:99], a[240:243], v[128:131], v[96:99]\n"
      "buffer_load_dwordx4 v235, s[60:63], s82 offen offset:1024 lds\n"
      "v_mfma_f32_16x16x32_bf16 v[96:99], a[244:247], v[144:147], v[96:99]\n"
      "ds_read_b128 a[188:191], v221 offset:41216\n"
      "v_add_f32 v209, v209, v181\n"
      "v_mfma_f32_16x16x32_bf16 v[100:103], a[240:243], v[132:135], v[100:103]\n"
      "ds_read_b64_tr_b16 a[192:193], v222 offset:32768\n"
      "v_cvt_pk_bf16_f32 v166, v180, v181\n"
      "v_mfma_f32_16x16x32_bf16 v[100:103], a[244:247], v[148:151], v[100:103]\n"
      "v_exp_f32 v182, v182\n"
      "v_mfma_f32_16x16x32_bf16 v[104:107], a[240:243], v[136:139], v[104:107]\n"
      "ds_read_b64_tr_b16 a[194:195], v222 offset:40960\n"
      "v_add_f32 v209, v209, v182\n"
      "v_mfma_f32_16x16x32_bf16 v[104:107], a[244:247], v[152:155], v[104:107]\n"
      "buffer_load_dwordx4 v236, s[60:63], s82 offen offset:2048 lds\n"
      "v_mfma_f32_16x16x32_bf16 v[108:111], a[240:243], v[140:143], v[108:111]\n"
      "v_exp_f32 v183, v183\n"
      "v_mfma_f32_16x16x32_bf16 v[108:111], a[244:247], v[156:159], v[108:111]\n"
      "ds_read_b64_tr_b16 a[196:197], v222 offset:49152\n"
      "v_add_f32 v209, v209, v183\n"
      "v_mfma_f32_16x16x32_bf16 v[112:115], a[248:251], v[128:131], v[112:115]\n"
      "ds_read_b64_tr_b16 a[198:199], v222 offset:57344\n"
      "v_cvt_pk_bf16_f32 v167, v182, v183\n"
      "v_mfma_f32_16x16x32_bf16 v[112:115], a[252:255], v[144:147], v[112:115]\n"
      "s_add_u32 s96, s82, 4096\n"
      "v_exp_f32 v168, v168\n"
      "v_mfma_f32_16x16x32_bf16 v[116:119], a[248:251], v[132:135], v[116:119]\n"
      "buffer_load_dwordx4 v237, s[60:63], s82 offen offset:3072 lds\n"
      "v_mfma_f32_16x16x32_bf16 v[116:119], a[252:255], v[148:151], v[116:119]\n"
      "ds_read_b64_tr_b16 a[200:201], v223 offset:32768\n"
      "v_add_f32 v210, v210, v168\n"
      "v_mfma_f32_16x16x32_bf16 v[120:123], a[248:251], v[136:139], v[120:123]\n"
      "v_exp_f32 v169, v169\n"
      "v_mfma_f32_16x16x32_bf16 v[120:123], a[252:255], v[152:155], v[120:123]\n"
      "ds_read_b64_tr_b16 a[202:203], v223 offset:40960\n"
      "v_add_f32 v210, v210, v169\n"
      "v_mfma_f32_16x16x32_bf16 v[124:127], a[248:251], v[140:143], v[124:127]\n"
      "ds_read_b64_tr_b16 a[204:205], v223 offset:49152\n"
      "v_cvt_pk_bf16_f32 v168, v168, v169\n"
      "v_mfma_f32_16x16x32_bf16 v[124:127], a[252:255], v[156:159], v[124:127]\n"
      "s_waitcnt lgkmcnt(15)\n"
      "s_add_u32 m0, s81, 69632\n"
      "ds_read_b64_tr_b16 a[206:207], v223 offset:57344\n"
      "v_mfma_f32_16x16x32_bf16 v[128:131], a[128:131], a[0:3], v[192:195]\n"
      "buffer_load_dwordx4 v234, s[60:63], s96 offen offset:0 lds\n"
      "v_mfma_f32_16x16x32_bf16 v[128:131], a[132:135], a[4:7], v[128:131]\n"
      "v_exp_f32 v170, v170\n"
      "v_mfma_f32_16x16x32_bf16 v[128:131], a[136:139], a[8:11], v[128:131]\n"
      "ds_read_b64_tr_b16 a[208:209], v224 offset:32768\n"
      "v_add_f32 v210, v210, v170\n"
      "v_mfma_f32_16x16x32_bf16 v[128:131], a[140:143], a[12:15], v[128:131]\n"
      "v_exp_f32 v171, v171\n"
      "v_mfma_f32_16x16x32_bf16 v[128:131], a[144:147], a[16:19], v[128:131]\n"
      "v_add_f32 v210, v210, v171\n"
      "v_cvt_pk_bf16_f32 v169, v170, v171\n"
      "v_mfma_f32_16x16x32_bf16 v[128:131], a[148:151], a[20:23], v[128:131]\n"
      "buffer_load_dwordx4 v235, s[60:63], s96 offen offset:1024 lds\n"
      "v_mfma_f32_16x16x32_bf16 v[128:131], a[152:155], a[24:27], v[128:131]\n"
      "v_exp_f32 v184, v184\n"
      "v_mfma_f32_16x16x32_bf16 v[128:131], a[156:159], a[28:31], v[128:131]\n"
      "ds_read_b64_tr_b16 a[210:211], v224 offset:40960\n"
      "v_add_f32 v210, v210, v184\n"
      "v_mfma_f32_16x16x32_bf16 v[132:135], a[128:131], a[32:35], v[196:199]\n"
      "v_exp_f32 v185, v185\n"
      "v_mfma_f32_16x16x32_bf16 v[132:135], a[132:135], a[36:39], v[132:135]\n"
      "ds_read_b64_tr_b16 a[212:213], v224 offset:49152\n"
      "v_add_f32 v210, v210, v185\n"
      "v_mfma_f32_16x16x32_bf16 v[132:135], a[136:139], a[40:43], v[132:135]\n"
      "buffer_load_dwordx4 v236, s[60:63], s96 offen offset:2048 lds\n"
      "v_mfma_f32_16x16x32_bf16 v[132:135], a[140:143], a[44:47], v[132:135]\n"
      "ds_read_b64_tr_b16 a[214:215], v224 offset:57344\n"
      "v_cvt_pk_bf16_f32 v170, v184, v185\n"
      "v_mfma_f32_16x16x32_bf16 v[132:135], a[144:147], a[48:51], v[132:135]\n"
      "v_exp_f32 v186, v186\n"
      "v_mfma_f32_16x16x32_bf16 v[132:135], a[148:151], a[52:55], v[132:135]\n"
      "ds_read_b64_tr_b16 a[216:217], v225 offset:32768\n"
      "v_add_f32 v210, v210, v186\n"
      "v_mfma_f32_16x16x32_bf16 v[132:135], a[152:155], a[56:59], v[132:135]\n"
      "v_exp_f32 v187, v187\n"
      "v_mfma_f32_16x16x32_bf16 v[132:135], a[156:159], a[60:63], v[132:135]\n"
      "buffer_load_dwordx4 v237, s[60:63], s96 offen offset:3072 lds\n"
      "v_mfma_f32_16x16x32_bf16 v[136:139], a[128:131], a[64:67], v[200:203]\n"
      "buffer_load_dword v217, v255, s[72:75], s76 offen\n"
      "v_mfma_f32_16x16x32_bf16 v[136:139], a[132:135], a[68:71], v[136:139]\n"
      "s_add_u32 s76, s76, s77\n"
      "ds_read_b64_tr_b16 a[218:219], v225 offset:40960\n"
      "v_add_f32 v210, v210, v187\n"
      "v_mfma_f32_16x16x32_bf16 v[136:139], a[136:139], a[72:75], v[136:139]\n"
      "ds_read_b64_tr_b16 a[220:221], v225 offset:49152\n"
      "v_cvt_pk_bf16_f32 v171, v186, v187\n"
      "v_mfma_f32_16x16x32_bf16 v[136:139], a[140:143], a[76:79], v[136:139]\n"
      "ds_read_b64_tr_b16 a[222:223], v225 offset:57344\n"
      "v_max3_f32 v216, v128, v129, v129\n"
      "v_mfma_f32_16x16x32_bf16 v[136:139], a[144:147], a[80:83], v[136:139]\n"
      "ds_read_b64_tr_b16 a[224:225], v226 offset:32768\n"
      "v_max3_f32 v216, v130, v131, v216\n"
      "v_mfma_f32_16x16x32_bf16 v[136:139], a[148:151], a[84:87], v[136:139]\n"
      "v_exp_f32 v172, v172\n"
      "v_mfma_f32_16x16x32_bf16 v[136:139], a[152:155], a[88:91], v[136:139]\n"
      "ds_read_b64_tr_b16 a[226:227], v226 offset:40960\n"
      "v_add_f32 v211, v211, v172\n"
      "v_mfma_f32_16x16x32_bf16 v[136:139], a[156:159], a[92:95], v[136:139]\n"
      "ds_read_b64_tr_b16 a[228:229], v226 offset:49152\n"
      "v_max3_f32 v216, v132, v133, v216\n"
      "v_mfma_f32_16x16x32_bf16 v[140:143], a[128:131], a[96:99], v[204:207]\n"
      "ds_read_b64_tr_b16 a[230:231], v226 offset:57344\n"
      "v_max3_f32 v216, v134, v135, v216\n"
      "v_mfma_f32_16x16x32_bf16 v[140:143], a[132:135], a[100:103], v[140:143]\n"
      "v_exp_f32 v173, v173\n"
      "v_mfma_f32_16x16x32_bf16 v[140:143], a[136:139], a[104:107], v[140:143]\n"
      "ds_read_b64_tr_b16 a[232:233], v227 offset:32768\n"
      "v_add_f32 v211, v211, v173\n"
      "v_mfma_f32_16x16x32_bf16 v[140:143], a[140:143], a[108:111], v[140:143]\n"
      "ds_read_b64_tr_b16 a[234:235], v227 offset:40960\n"
      "v_cvt_pk_bf16_f32 v172, v172, v173\n"
      "v_mfma_f32_16x16x32_bf16 v[140:143], a[144:147], a[112:115], v[140:143]\n"
      "v_exp_f32 v174, v174\n"
      "v_mfma_f32_16x16x32_bf16 v[140:143], a[148:151], a[116:119], v[140:143]\n"
      "ds_read_b64_tr_b16 a[236:237], v227 offset:49152\n"
      "v_add_f32 v211, v211, v174\n"
      "v_mfma_f32_16x16x32_bf16 v[140:143], a[152:155], a[120:123], v[140:143]\n"
      "v_exp_f32 v175, v175\n"
      "v_mfma_f32_16x16x32_bf16 v[140:143], a[156:159], a[124:127], v[140:143]\n"
      "s_waitcnt lgkmcnt(15)\n"
      "ds_read_b64_tr_b16 a[238:239], v227 offset:57344\n"
      "v_add_f32 v211, v211, v175\n"
      "v_mfma_f32_16x16x32_bf16 v[144:147], a[160:163], a[0:3], v[192:195]\n"
      "ds_read_b64_tr_b16 a[240:241], v228 offset:32768\n"
      "v_cvt_pk_bf16_f32 v173, v174, v175\n"
      "v_mfma_f32_16x16x32_bf16 v[144:147], a[164:167], a[4:7], v[144:147]\n"
      "ds_write_b128 v238, v[160:163] offset:0\n"
      "v_mfma_f32_16x16x32_bf16 v[144:147], a[168:171], a[8:11], v[144:147]\n"
      "v_exp_f32 v188, v188\n"
      "v_mfma_f32_16x16x32_bf16 v[144:147], a[172:175], a[12:15], v[144:147]\n"
      "v_exp_f32 v189, v189\n"
      "v_mfma_f32_16x16x32_bf16 v[144:147], a[176:179], a[16:19], v[144:147]\n"
      "ds_write_b128 v238, v[164:167] offset:1024\n"
      "v_mfma_f32_16x16x32_bf16 v[144:147], a[180:183], a[20:23], v[144:147]\n"
      "v_add_f32 v211, v211, v188\n"
      "v_add_f32 v211, v211, v189\n"
      "v_mfma_f32_16x16x32_bf16 v[144:147], a[184:187], a[24:27], v[144:147]\n"
      "v_exp_f32 v190, v190\n"
      "v_mfma_f32_16x16x32_bf16 v[144:147], a[188:191], a[28:31], v[144:147]\n"
      "ds_write_b128 v238, v[168:171] offset:2048\n"
      "v_mfma_f32_16x16x32_bf16 v[148:151], a[160:163], a[32:35], v[196:199]\n"
      "v_cvt_pk_bf16_f32 v174, v188, v189\n"
      "v_add_f32 v211, v211, v190\n"
      "v_mfma_f32_16x16x32_bf16 v[148:151], a[164:167], a[36:39], v[148:151]\n"
      "v_exp_f32 v191, v191\n"
      "v_mfma_f32_16x16x32_bf16 v[148:151], a[168:171], a[40:43], v[148:151]\n"
      "v_add_f32 v211, v211, v191\n"
      "v_cvt_pk_bf16_f32 v175, v190, v191\n"
      "v_mfma_f32_16x16x32_bf16 v[148:151], a[172:175], a[44:47], v[148:151]\n"
      "ds_write_b128 v238, v[172:175] offset:3072\n"
      "v_mfma_f32_16x16x32_bf16 v[148:151], a[176:179], a[48:51], v[148:151]\n"
      "v_max3_f32 v216, v136, v137, v216\n"
      "v_max3_f32 v216, v138, v139, v216\n"
      "v_mfma_f32_16x16x32_bf16 v[148:151], a[180:183], a[52:55], v[148:151]\n"
      "v_max3_f32 v216, v140, v141, v216\n"
      "v_max3_f32 v216, v142, v143, v216\n"
      "v_mfma_f32_16x16x32_bf16 v[148:151], a[184:187], a[56:59], v[148:151]\n"
      "s_waitcnt lgkmcnt(0)\n"
      "s_barrier\n"
      "v_mfma_f32_16x16x32_bf16 v[148:151], a[188:191], a[60:63], v[148:151]\n"
      "ds_read_b32 v240, v241 offset:4\n"
      "v_max3_f32 v216, v144, v145, v216\n"
      "v_mfma_f32_16x16x32_bf16 v[152:155], a[160:163], a[64:67], v[200:203]\n"
      "ds_read_b128 v[176:179], v239 offset:0\n"
      "v_max3_f32 v216, v146, v147, v216\n"
      "v_mfma_f32_16x16x32_bf16 v[152:155], a[164:167], a[68:71], v[152:155]\n"
      "ds_read_b128 v[180:183], v239 offset:1024\n"
      "v_mfma_f32_16x16x32_bf16 v[152:155], a[168:171], a[72:75], v[152:155]\n"
      "ds_read_b128 v[184:187], v239 offset:2048\n"
      "v_mfma_f32_16x16x32_bf16 v[152:155], a[172:175], a[76:79], v[152:155]\n"
      "ds_read_b128 v[188:191], v239 offset:3072\n"
      "v_mfma_f32_16x16x32_bf16 v[152:155], a[176:179], a[80:83], v[152:155]\n"
      "ds_read_b64_tr_b16 a[242:243], v228 offset:40960\n"
      "v_mfma_f32_16x16x32_bf16 v[152:155], a[180:183], a[84:87], v[152:155]\n"
      "s_waitcnt lgkmcnt(5)\n"
      "ds_read_b64_tr_b16 a[244:245], v228 offset:49152\n"
      "v_cmp_eq_u32 vcc, s88, v240\n"
      "v_mfma_f32_16x16x32_bf16 v[152:155], a[184:187], a[88:91], v[152:155]\n"
      "ds_read_b64_tr_b16 a[246:247], v228 offset:57344\n"
      "v_mfma_f32_16x16x32_bf16 v[152:155], a[188:191], a[92:95], v[152:155]\n"
      "ds_read_b64_tr_b16 a[248:249], v229 offset:32768\n"
      "v_max3_f32 v216, v148, v149, v216\n"
      "v_mfma_f32_16x16x32_bf16 v[156:159], a[160:163], a[96:99], v[204:207]\n"
      "ds_read_b64_tr_b16 a[250:251], v229 offset:40960\n"
      "v_max3_f32 v216, v150, v151, v216\n"
      "v_mfma_f32_16x16x32_bf16 v[156:159], a[164:167], a[100:103], v[156:159]\n"
      "ds_read_b64_tr_b16 a[252:253], v229 offset:49152\n"
      "v_mfma_f32_16x16x32_bf16 v[156:159], a[168:171], a[104:107], v[156:159]\n"
      "ds_read_b64_tr_b16 a[254:255], v229 offset:57344\n"
      "v_mfma_f32_16x16x32_bf16 v[156:159], a[172:175], a[108:111], v[156:159]\n"
      "v_mfma_f32_16x16x32_bf16 v[156:159], a[176:179], a[112:115], v[156:159]\n"
      "v_mfma_f32_16x16x32_bf16 v[156:159], a[180:183], a[116:119], v[156:159]\n"
      "v_mfma_f32_16x16x32_bf16 v[156:159], a[184:187], a[120:123], v[156:159]\n"
      "s_sub_u32 s90, s90, 1\n"
      "v_mfma_f32_16x16x32_bf16 v[156:159], a[188:191], a[124:127], v[156:159]\n"
      "s_waitcnt lgkmcnt(0)\n"
      "s_cbranch_vccnz R2_5_%=\n"
      "B2_6_%=:\n"
      "s_cmp_eq_u32 s90, 0\n"
      "s_cbranch_scc1 LEXIT_%=\n"
      "v_mfma_f32_16x16x32_bf16 v[0:3], a[192:195], v[160:163], v[0:3]\n"
      "s_add_u32 s88, s88, 1\n"
      "v_max3_f32 v216, v152, v153, v216\n"
      "v_max3_f32 v216, v154, v155, v216\n"
      "v_mfma_f32_16x16x32_bf16 v[0:3], a[196:199], v[176:179], v[0:3]\n"
      "s_add_u32 s82, s82, 65536\n"
      "s_add_u32 m0, s81, 32768\n"
      "v_mfma_f32_16x16x32_bf16 v[4:7], a[192:195], v[164:167], v[4:7]\n"
      "buffer_load_dwordx4 v230, s[56:59], s82 offen offset:0 lds\n"
      "v_mfma_f32_16x16x32_bf16 v[4:7], a[196:199], v[180:183], v[4:7]\n"
      "v_mfma_f32_16x16x32_bf16 v[8:11], a[192:195], v[168:171], v[8:11]\n"
      "v_max3_f32 v216, v156, v157, v216\n"
      "v_max3_f32 v216, v158, v159, v216\n"
      "v_mfma_f32_16x16x32_bf16 v[8:11], a[196:199], v[184:187], v[8:11]\n"
      "v_cmp_lt_f32 vcc, s89, v216\n"
      "v_mfma_f32_16x16x32_bf16 v[12:15], a[192:195], v[172:175], v[12:15]\n"
      "v_mfma_f32_16x16x32_bf16 v[12:15], a[196:199], v[188:191], v[12:15]\n"
      "s_cbranch_vccnz R1_7_%=\n"
      "B1_8_%=:\n"
      "v_mfma_f32_16x16x32_bf16 v[16:19], a[200:203], v[160:163], v[16:19]\n"
      "buffer_load_dwordx4 v231, s[56:59], s82 offen offset:1024 lds\n"
      "v_mfma_f32_16x16x32_bf16 v[16:19], a[204:207], v[176:179], v[16:19]\n"
      "v_exp_f32 v128, v128\n"
      "v_mfma_f32_16x16x32_bf16 v[20:23], a[200:203], v[164:167], v[20:23]\n"
      "v_exp_f32 v129, v129\n"
      "v_mfma_f32_16x16x32_bf16 v[20:23], a[204:207], v[180:183], v[20:23]\n"
      "v_add_f32 v208, v208, v128\n"
      "v_add_f32 v208, v208, v129\n"
      "v_mfma_f32_16x16x32_bf16 v[24:27], a[200:203], v[168:171], v[24:27]\n"
      "v_exp_f32 v130, v130\n"
      "v_mfma_f32_16x16x32_bf16 v[24:27], a[204:207], v[184:187], v[24:27]\n"
      "buffer_load_dwordx4 v232, s[56:59], s82 offen offset:2048 lds\n"
      "v_mfma_f32_16x16x32_bf16 v[28:31], a[200:203], v[172:175], v[28:31]\n"
      "s_waitcnt vmcnt(4)\n"
      "s_barrier\n"
      "v_mfma_f32_16x16x32_bf16 v[28:31], a[204:207], v[188:191], v[28:31]\n"
      "ds_read_b128 a[128:131], v218 offset:0\n"
      "v_cvt_pk_bf16_f32 v128, v128, v129\n"
      "v_mfma_f32_16x16x32_bf16 v[32:35], a[208:211], v[160:163], v[32:35]\n"
      "ds_read_b128 a[132:135], v219 offset:0\n"
      "v_add_f32 v208, v208, v130\n"
      "v_mfma_f32_16x16x32_bf16 v[32:35], a[212:215], v[176:179], v[32:35]\n"
      "s_add_u32 s96, s82, 4096\n"
      "v_exp_f32 v131, v131\n"
      "v_mfma_f32_16x16x32_bf16 v[36:39], a[208:211], v[164:167], v[36:39]\n"
      "buffer_load_dwordx4 v233, s[56:59], s82 offen offset:3072 lds\n"
      "v_mfma_f32_16x16x32_bf16 v[36:39], a[212:215], v[180:183], v[36:39]\n"
      "ds_read_b128 a[136:139], v220 offset:0\n"
      "v_add_f32 v208, v208, v131\n"
      "v_mfma_f32_16x16x32_bf16 v[40:43], a[208:211], v[168:171], v[40:43]\n"
      "ds_read_b128 a[140:143], v221 offset:0\n"
      "v_cvt_pk_bf16_f32 v129, v130, v131\n"
      "v_mfma_f32_16x16x32_bf16 v[40:43], a[212:215], v[184:187], v[40:43]\n"
      "v_exp_f32 v144, v144\n"
      "v_mfma_f32_16x16x32_bf16 v[44:47], a[208:211], v[172:175], v[44:47]\n"
      "s_add_u32 m0, s81, 36864\n"
      "ds_read_b128 a[144:147], v218 offset:256\n"
      "v_add_f32 v208, v208, v144\n"
      "v_mfma_f32_16x16x32_bf16 v[44:47], a[212:215], v[188:191], v[44:47]\n"
      "buffer_load_dwordx4 v230, s[56:59], s96 offen offset:0 lds\n"
      "v_mfma_f32_16x16x32_bf16 v[48:51], a[216:219], v[160:163], v[48:51]\n"
      "v_exp_f32 v145, v145\n"
      "v_mfma_f32_16x16x32_bf16 v[48:51], a[220:223], v[176:179], v[48:51]\n"
      "ds_read_b128 a[148:151], v219 offset:256\n"
      "v_add_f32 v208, v208, v145\n"
      "v_mfma_f32_16x16x32_bf16 v[52:55], a[216:219], v[164:167], v[52:55]\n"
      "ds_read_b128 a[152:155], v220 offset:256\n"
      "v_cvt_pk_bf16_f32 v130, v144, v145\n"
      "v_mfma_f32_16x16x32_bf16 v[52:55], a[220:223], v[180:183], v[52:55]\n"
      "v_exp_f32 v146, v146\n"
      "v_mfma_f32_16x16x32_bf16 v[56:59], a[216:219], v[168:171], v[56:59]\n"
      "buffer_load_dwordx4 v231, s[56:59], s96 offen offset:1024 lds\n"
      "v_mfma_f32_16x16x32_bf16 v[56:59], a[220:223], v[184:187], v[56:59]\n"
      "ds_read_b128 a[156:159], v221 offset:256\n"
      "v_add_f32 v208, v208, v146\n"
      "v_mfma_f32_16x16x32_bf16 v[60:63], a[216:219], v[172:175], v[60:63]\n"
      "v_exp_f32 v147, v147\n"
      "v_mfma_f32_16x16x32_bf16 v[60:63], a[220:223], v[188:191], v[60:63]\n"
      "ds_read_b128 a[160:163], v218 offset:8192\n"
      "v_add_f32 v208, v208, v147\n"
      "v_mfma_f32_16x16x32_bf16 v[64:67], a[224:227], v[160:163], v[64:67]\n"
      "ds_read_b128 a[164:167], v219 offset:8192\n"
      "v_cvt_pk_bf16_f32 v131, v146, v147\n"
      "v_mfma_f32_16x16x32_bf16 v[64:67], a[228:231], v[176:179], v[64:67]\n"
      "buffer_load_dwordx4 v232, s[56:59], s96 offen offset:2048 lds\n"
      "v_mfma_f32_16x16x32_bf16 v[68:71], a[224:227], v[164:167], v[68:71]\n"
      "v_exp_f32 v132, v132\n"
      "v_mfma_f32_16x16x32_bf16 v[68:71], a[228:231], v[180:183], v[68:71]\n"
      "ds_read_b128 a[168:171], v220 offset:8192\n"
      "v_add_f32 v209, v209, v132\n"
      "v_mfma_f32_16x16x32_bf16 v[72:75], a[224:227], v[168:171], v[72:75]\n"
      "v_exp_f32 v133, v133\n"
      "v_mfma_f32_16x16x32_bf16 v[72:75], a[228:231], v[184:187], v[72:75]\n"
      "ds_read_b128 a[172:175], v221 offset:8192\n"
      "v_add_f32 v209, v209, v133\n"
      "v_mfma_f32_16x16x32_bf16 v[76:79], a[224:227], v[172:175], v[76:79]\n"
      "buffer_load_dwordx4 v233, s[56:59], s96 offen offset:3072 lds\n"
      "v_mfma_f32_16x16x32_bf16 v[76:79], a[228:231], v[188:191], v[76:79]\n"
      "v_exp_f32 v134, v134\n"
      "v_mfma_f32_16x16x32_bf16 v[80:83], a[232:235], v[160:163], v[80:83]\n"
      "v_cvt_pk_bf16_f32 v132, v132, v133\n"
      "v_add_f32 v209, v209, v134\n"
      "v_mfma_f32_16x16x32_bf16 v[80:83], a[236:239], v[176:179], v[80:83]\n"
      "v_exp_f32 v135, v135\n"
      "v_mfma_f32_16x16x32_bf16 v[84:87], a[232:235], v[164:167], v[84:87]\n"
      "s_add_u32 m0, s81, 98304\n"
      "ds_read_b128 a[176:179], v218 offset:8448\n"
      "v_add_f32 v209, v209, v135\n"
      "v_mfma_f32_16x16x32_bf16 v[84:87], a[236:239], v[180:183], v[84:87]\n"
      "buffer_load_dwordx4 v234, s[60:63], s83 offen offset:0 lds\n"
      "v_mfma_f32_16x16x32_bf16 v[88:91], a[232:235], v[168:171], v[88:91]\n"
      "ds_read_b128 a[180:183], v219 offset:8448\n"
      "v_cvt_pk_bf16_f32 v133, v134, v135\n"
      "v_mfma_f32_16x16x32_bf16 v[88:91], a[236:239], v[184:187], v[88:91]\n"
      "v_exp_f32 v148, v148\n"
      "v_mfma_f32_16x16x32_bf16 v[92:95], a[232:235], v[172:175], v[92:95]\n"
      "ds_read_b128 a[184:187], v220 offset:8448\n"
      "v_add_f32 v209, v209, v148\n"
      "v_mfma_f32_16x16x32_bf16 v[92:95], a[236:239], v[188:191], v[92:95]\n"
      "v_exp_f32 v149, v149\n"
      "v_mfma_f32_16x16x32_bf16 v[96:99], a[240:243], v[160:163], v[96:99]\n"
      "buffer_load_dwordx4 v235, s[60:63], s83 offen offset:1024 lds\n"
      "v_mfma_f32_16x16x32_bf16 v[96:99], a[244:247], v[176:179], v[96:99]\n"
      "ds_read_b128 a[188:191], v221 offset:8448\n"
      "v_add_f32 v209, v209, v149\n"
      "v_mfma_f32_16x16x32_bf16 v[100:103], a[240:243], v[164:167], v[100:103]\n"
      "ds_read_b64_tr_b16 a[192:193], v222 offset:0\n"
      "v_cvt_pk_bf16_f32 v134, v148, v149\n"
      "v_mfma_f32_16x16x32_bf16 v[100:103], a[244:247], v[180:183], v[100:103]\n"
      "v_exp_f32 v150, v150\n"
      "v_mfma_f32_16x16x32_bf16 v[104:107], a[240:243], v[168:171], v[104:107]\n"
      "ds_read_b64_tr_b16 a[194:195], v222 offset:8192\n"
      "v_add_f32 v209, v209, v150\n"
      "v_mfma_f32_16x16x32_bf16 v[104:107], a[244:247], v[184:187], v[104:107]\n"
      "buffer_load_dwordx4 v236, s[60:63], s83 offen offset:2048 lds\n"
      "v_mfma_f32_16x16x32_bf16 v[108:111], a[240:243], v[172:175], v[108:111]\n"
      "v_exp_f32 v151, v151\n"
      "v_mfma_f32_16x16x32_bf16 v[108:111], a[244:247], v[188:191], v[108:111]\n"
      "ds_read_b64_tr_b16 a[196:197], v222 offset:16384\n"
      "v_add_f32 v209, v209, v151\n"
      "v_mfma_f32_16x16x32_bf16 v[112:115], a[248:251], v[160:163], v[112:115]\n"
      "ds_read_b64_tr_b16 a[198:199], v222 offset:24576\n"
      "v_cvt_pk_bf16_f32 v135, v150, v151\n"
      "v_mfma_f32_16x16x32_bf16 v[112:115], a[252:255], v[176:179], v[112:115]\n"
      "s_add_u32 s96, s83, 4096\n"
      "v_exp_f32 v136, v136\n"
      "v_mfma_f32_16x16x32_bf16 v[116:119], a[248:251], v[164:167], v[116:119]\n"
      "buffer_load_dwordx4 v237, s[60:63], s83 offen offset:3072 lds\n"
      "v_mfma_f32_16x16x32_bf16 v[116:119], a[252:255], v[180:183], v[116:119]\n"
      "ds_read_b64_tr_b16 a[200:201], v223 offset:0\n"
      "v_add_f32 v210, v210, v136\n"
      "v_mfma_f32_16x16x32_bf16 v[120:123], a[248:251], v[168:171], v[120:123]\n"
      "v_exp_f32 v137, v137\n"
      "v_mfma_f32_16x16x32_bf16 v[120:123], a[252:255], v[184:187], v[120:123]\n"
      "ds_read_b64_tr_b16 a[202:203], v223 offset:8192\n"
      "v_add_f32 v210, v210, v137\n"
      "v_mfma_f32_16x16x32_bf16 v[124:127], a[248:251], v[172:175], v[124:127]\n"
      "ds_read_b64_tr_b16 a[204:205], v223 offset:16384\n"
      "v_cvt_pk_bf16_f32 v136, v136, v137\n"
      "v_mfma_f32_16x16x32_bf16 v[124:127], a[252:255], v[188:191], v[124:127]\n"
      "s_waitcnt lgkmcnt(15)\n"
      "s_add_u32 m0, s81, 102400\n"
      "ds_read_b64_tr_b16 a[206:207], v223 offset:24576\n"
      "v_mfma_f32_16x16x32_bf16 v[160:163], a[128:131], a[0:3], v[192:195]\n"
      "buffer_load_dwordx4 v234, s[60:63], s96 offen offset:0 lds\n"
      "v_mfma_f32_16x16x32_bf16 v[160:163], a[132:135], a[4:7], v[160:163]\n"
      "v_exp_f32 v138, v138\n"
      "v_mfma_f32_16x16x32_bf16 v[160:163], a[136:139], a[8:11], v[160:163]\n"
      "ds_read_b64_tr_b16 a[208:209], v224 offset:0\n"
      "v_add_f32 v210, v210, v138\n"
      "v_mfma_f32_16x16x32_bf16 v[160:163], a[140:143], a[12:15], v[160:163]\n"
      "v_exp_f32 v139, v139\n"
      "v_mfma_f32_16x16x32_bf16 v[160:163], a[144:147], a[16:19], v[160:163]\n"
      "v_add_f32 v210, v210, v139\n"
      "v_cvt_pk_bf16_f32 v137, v138, v139\n"
      "v_mfma_f32_16x16x32_bf16 v[160:163], a[148:151], a[20:23], v[160:163]\n"
      "buffer_load_dwordx4 v235, s[60:63], s96 offen offset:1024 lds\n"
      "v_mfma_f32_16x16x32_bf16 v[160:163], a[152:155], a[24:27], v[160:163]\n"
      "v_exp_f32 v152, v152\n"
      "v_mfma_f32_16x16x32_bf16 v[160:163], a[156:159], a[28:31], v[160:163]\n"
      "ds_read_b64_tr_b16 a[210:211], v224 offset:8192\n"
      "v_add_f32 v210, v210, v152\n"
      "v_mfma_f32_16x16x32_bf16 v[164:167], a[128:131], a[32:35], v[196:199]\n"
      "v_exp_f32 v153, v153\n"
      "v_mfma_f32_16x16x32_bf16 v[164:167], a[132:135], a[36:39], v[164:167]\n"
      "ds_read_b64_tr_b16 a[212:213], v224 offset:16384\n"
      "v_add_f32 v210, v210, v153\n"
      "v_mfma_f32_16x16x32_bf16 v[164:167], a[136:139], a[40:43], v[164:167]\n"
      "buffer_load_dwordx4 v236, s[60:63], s96 offen offset:2048 lds\n"
      "v_mfma_f32_16x16x32_bf16 v[164:167], a[140:143], a[44:47], v[164:167]\n"
      "ds_read_b64_tr_b16 a[214:215], v224 offset:24576\n"
      "v_cvt_pk_bf16_f32 v138, v152, v153\n"
      "v_mfma_f32_16x16x32_bf16 v[164:167], a[144:147], a[48:51], v[164:167]\n"
      "v_exp_f32 v154, v154\n"
      "v_mfma_f32_16x16x32_bf16 v[164:167], a[148:151], a[52:55], v[164:167]\n"
      "ds_read_b64_tr_b16 a[216:217], v225 offset:0\n"
      "v_add_f32 v210, v210, v154\n"
      "v_mfma_f32_16x16x32_bf16 v[164:167], a[152:155], a[56:59], v[164:167]\n"
      "v_exp_f32 v155, v155\n"
      "v_mfma_f32_16x16x32_bf16 v[164:167], a[156:159], a[60:63], v[164:167]\n"
      "buffer_load_dwordx4 v237, s[60:63], s96 offen offset:3072 lds\n"
      "v_mfma_f32_16x16x32_bf16 v[168:171], a[128:131], a[64:67], v[200:203]\n"
      "ds_read_b64_tr_b16 a[218:219], v225 offset:8192\n"
      "v_add_f32 v210, v210, v155\n"
      "v_mfma_f32_16x16x32_bf16 v[168:171], a[132:135], a[68:71], v[168:171]\n"
      "ds_read_b64_tr_b16 a[220:221], v225 offset:16384\n"
      "v_cvt_pk_bf16_f32 v139, v154, v155\n"
      "v_mfma_f32_16x16x32_bf16 v[168:171], a[136:139], a[72:75], v[168:171]\n"
      "ds_read_b64_tr_b16 a[222:223], v225 offset:24576\n"
      "v_max3_f32 v216, v160, v161, v161\n"
      "v_mfma_f32_16x16x32_bf16 v[168:171], a[140:143], a[76:79], v[168:171]\n"
      "ds_read_b64_tr_b16 a[224:225], v226 offset:0\n"
      "v_max3_f32 v216, v162, v163, v216\n"
      "v_mfma_f32_16x16x32_bf16 v[168:171], a[144:147], a[80:83], v[168:171]\n"
      "v_exp_f32 v140, v140\n"
      "v_mfma_f32_16x16x32_bf16 v[168:171], a[148:151], a[84:87], v[168:171]\n"
      "ds_read_b64_tr_b16 a[226:227], v226 offset:8192\n"
      "v_add_f32 v211, v211, v140\n"
      "v_mfma_f32_16x16x32_bf16 v[168:171], a[152:155], a[88:91], v[168:171]\n"
      "v_exp_f32 v141, v141\n"
      "v_mfma_f32_16x16x32_bf16 v[168:171], a[156:159], a[92:95], v[168:171]\n"
      "ds_read_b64_tr_b16 a[228:229], v226 offset:16384\n"
      "v_add_f32 v211, v211, v141\n"
      "v_mfma_f32_16x16x32_bf16 v[172:175], a[128:131], a[96:99], v[204:207]\n"
      "ds_read_b64_tr_b16 a[230:231], v226 offset:24576\n"
      "v_cvt_pk_bf16_f32 v140, v140, v141\n"
      "v_mfma_f32_16x16x32_bf16 v[172:175], a[132:135], a[100:103], v[172:175]\n"
      "ds_read_b64_tr_b16 a[232:233], v227 offset:0\n"
      "v_max3_f32 v216, v164, v165, v216\n"
      "v_mfma_f32_16x16x32_bf16 v[172:175], a[136:139], a[104:107], v[172:175]\n"
      "ds_read_b64_tr_b16 a[234:235], v227 offset:8192\n"
      "v_max3_f32 v216, v166, v167, v216\n"
      "v_mfma_f32_16x16x32_bf16 v[172:175], a[140:143], a[108:111], v[172:175]\n"
      "v_exp_f32 v142, v142\n"
      "v_mfma_f32_16x16x32_bf16 v[172:175], a[144:147], a[112:115], v[172:175]\n"
      "ds_read_b64_tr_b16 a[236:237], v227 offset:16384\n"
      "v_add_f32 v211, v211, v142\n"
      "v_mfma_f32_16x16x32_bf16 v[172:175], a[148:151], a[116:119], v[172:175]\n"
      "v_exp_f32 v143, v143\n"
      "v_mfma_f32_16x16x32_bf16 v[172:175], a[152:155], a[120:123], v[172:175]\n"
      "ds_read_b64_tr_b16 a[238:239], v227 offset:24576\n"
      "v_add_f32 v211, v211, v143\n"
      "v_mfma_f32_16x16x32_bf16 v[172:175], a[156:159], a[124:127], v[172:175]\n"
      "s_waitcnt lgkmcnt(15)\n"
      "ds_read_b64_tr_b16 a[240:241], v228 offset:0\n"
      "v_cvt_pk_bf16_f32 v141, v142, v143\n"
      "v_mfma_f32_16x16x32_bf16 v[176:179], a[160:163], a[0:3], v[192:195]\n"
      "v_exp_f32 v156, v156\n"
      "v_mfma_f32_16x16x32_bf16 v[176:179], a[164:167], a[4:7], v[176:179]\n"
      "ds_write_b128 v238, v[128:131] offset:0\n"
      "v_mfma_f32_16x16x32_bf16 v[176:179], a[168:171], a[8:11], v[176:179]\n"
      "v_exp_f32 v157, v157\n"
      "v_mfma_f32_16x16x32_bf16 v[176:179], a[172:175], a[12:15], v[176:179]\n"
      "v_add_f32 v211, v211, v156\n"
      "v_add_f32 v211, v211, v157\n"
      "v_mfma_f32_16x16x32_bf16 v[176:179], a[176:179], a[16:19], v[176:179]\n"
      "ds_write_b128 v238, v[132:135] offset:1024\n"
      "v_mfma_f32_16x16x32_bf16 v[176:179], a[180:183], a[20:23], v[176:179]\n"
      "v_exp_f32 v158, v158\n"
      "v_mfma_f32_16x16x32_bf16 v[176:179], a[184:187], a[24:27], v[176:179]\n"
      "v_cvt_pk_bf16_f32 v142, v156, v157\n"
      "v_add_f32 v211, v211, v158\n"
      "v_mfma_f32_16x16x32_bf16 v[176:179], a[188:191], a[28:31], v[176:179]\n"
      "ds_write_b128 v238, v[136:139] offset:2048\n"
      "v_mfma_f32_16x16x32_bf16 v[180:183], a[160:163], a[32:35], v[196:199]\n"
      "v_exp_f32 v159, v159\n"
      "v_mfma_f32_16x16x32_bf16 v[180:183], a[164:167], a[36:39], v[180:183]\n"
      "v_add_f32 v211, v211, v159\n"
      "v_cvt_pk_bf16_f32 v143, v158, v159\n"
      "v_mfma_f32_16x16x32_bf16 v[180:183], a[168:171], a[40:43], v[180:183]\n"
      "ds_write_b128 v238, v[140:143] offset:3072\n"
      "v_mfma_f32_16x16x32_bf16 v[180:183], a[172:175], a[44:47], v[180:183]\n"
      "v_max3_f32 v216, v168, v169, v216\n"
      "v_max3_f32 v216, v170, v171, v216\n"
      "v_mfma_f32_16x16x32_bf16 v[180:183], a[176:179], a[48:51], v[180:183]\n"
      "v_max3_f32 v216, v172, v173, v216\n"
      "v_max3_f32 v216, v174, v175, v216\n"
      "v_mfma_f32_16x16x32_bf16 v[180:183], a[180:183], a[52:55], v[180:183]\n"
      "s_waitcnt lgkmcnt(0)\n"
      "s_barrier\n"
      "v_mfma_f32_16x16x32_bf16 v[180:183], a[184:187], a[56:59], v[180:183]\n"
      "ds_read_b32 v240, v241 offset:0\n"
      "v_mfma_f32_16x16x32_bf16 v[180:183], a[188:191], a[60:63], v[180:183]\n"
      "ds_read_b128 v[144:147], v239 offset:0\n"
      "v_max3_f32 v216, v176, v177, v216\n"
      "v_mfma_f32_16x16x32_bf16 v[184:187], a[160:163], a[64:67], v[200:203]\n"
      "ds_read_b128 v[148:151], v239 offset:1024\n"
      "v_max3_f32 v216, v178, v179, v216\n"
      "v_mfma_f32_16x16x32_bf16 v[184:187], a[164:167], a[68:71], v[184:187]\n"
      "ds_read_b128 v[152:155], v239 offset:2048\n"
      "v_mfma_f32_16x16x32_bf16 v[184:187], a[168:171], a[72:75], v[184:187]\n"
      "ds_read_b128 v[156:159], v239 offset:3072\n"
      "v_mfma_f32_16x16x32_bf16 v[184:187], a[172:175], a[76:79], v[184:187]\n"
      "ds_read_b64_tr_b16 a[242:243], v228 offset:8192\n"
      "v_mfma_f32_16x16x32_bf16 v[184:187], a[176:179], a[80:83], v[184:187]\n"
      "s_waitcnt lgkmcnt(5)\n"
      "ds_read_b64_tr_b16 a[244:245], v228 offset:16384\n"
      "v_cmp_eq_u32 vcc, s88, v240\n"
      "v_mfma_f32_16x16x32_bf16 v[184:187], a[180:183], a[84:87], v[184:187]\n"
      "ds_read_b64_tr_b16 a[246:247], v228 offset:24576\n"
      "v_mfma_f32_16x16x32_bf16 v[184:187], a[184:187], a[88:91], v[184:187]\n"
      "ds_read_b64_tr_b16 a[248:249], v229 offset:0\n"
      "v_mfma_f32_16x16x32_bf16 v[184:187], a[188:191], a[92:95], v[184:187]\n"
      "ds_read_b64_tr_b16 a[250:251], v229 offset:8192\n"
      "v_max3_f32 v216, v180, v181, v216\n"
      "v_mfma_f32_16x16x32_bf16 v[188:191], a[160:163], a[96:99], v[204:207]\n"
      "ds_read_b64_tr_b16 a[252:253], v229 offset:16384\n"
      "v_max3_f32 v216, v182, v183, v216\n"
      "v_mfma_f32_16x16x32_bf16 v[188:191], a[164:167], a[100:103], v[188:191]\n"
      "ds_read_b64_tr_b16 a[254:255], v229 offset:24576\n"
      "v_mfma_f32_16x16x32_bf16 v[188:191], a[168:171], a[104:107], v[188:191]\n"
      "v_mfma_f32_16x16x32_bf16 v[188:191], a[172:175], a[108:111], v[188:191]\n"
      "v_mfma_f32_16x16x32_bf16 v[188:191], a[176:179], a[112:115], v[188:191]\n"
      "v_mfma_f32_16x16x32_bf16 v[188:191], a[180:183], a[116:119], v[188:191]\n"
      "v_mfma_f32_16x16x32_bf16 v[188:191], a[184:187], a[120:123], v[188:191]\n"
      "v_mfma_f32_16x16x32_bf16 v[188:191], a[188:191], a[124:127], v[188:191]\n"
      "s_waitcnt lgkmcnt(0)\n"
      "s_cbranch_vccnz R2_9_%=\n"
      "B2_10_%=:\n"
      "s_branch LOOP_%=\n"
      "LEXIT_%=:\n"
      "s_mul_i32 s87, s80, 57344\n"
      "v_lshrrev_b32 v252, 7, v242\n"
      "v_mul_u32_u24 v252, 896, v252\n"
      "v_lshrrev_b32 v253, 3, v242\n"
      "v_xor_b32 v253, v253, v242\n"
      "v_and_b32 v253, 0x60, v253\n"
      "v_and_or_b32 v253, v242, 16, v253\n"
      "v_add3_u32 v230, v252, v253, s87\n"
      "v_add_u32 v231, 6144, v230\n"
      "v_add_u32 v232, 12288, v230\n"
      "v_add_u32 v233, 18432, v230\n"
      "s_lshl_b32 s91, s98, 7\n"
      "v_mfma_f32_16x16x32_bf16 v[0:3], a[192:195], v[160:163], v[0:3]\n"
      "s_add_u32 s88, s88, 1\n"
      "v_max3_f32 v216, v152, v153, v216\n"
      "v_max3_f32 v216, v154, v155, v216\n"
      "v_mfma_f32_16x16x32_bf16 v[0:3], a[196:199], v[176:179], v[0:3]\n"
      "s_add_u32 m0, s81, 32768\n"
      "s_add_u32 s95, s91, 0\n"
      "v_mfma_f32_16x16x32_bf16 v[4:7], a[192:195], v[164:167], v[4:7]\n"
      "buffer_load_dwordx4 v230, s[68:71], s95 offen offset:0 lds\n"
      "v_mfma_f32_16x16x32_bf16 v[4:7], a[196:199], v[180:183], v[4:7]\n"
      "v_mfma_f32_16x16x32_bf16 v[8:11], a[192:195], v[168:171], v[8:11]\n"
      "v_max3_f32 v216, v156, v157, v216\n"
      "v_max3_f32 v216, v158, v159, v216\n"
      "v_mfma_f32_16x16x32_bf16 v[8:11], a[196:199], v[184:187], v[8:11]\n"
      "v_cmp_lt_f32 vcc, s89, v216\n"
      "v_mfma_f32_16x16x32_bf16 v[12:15], a[192:195], v[172:175], v[12:15]\n"
      "v_mfma_f32_16x16x32_bf16 v[12:15], a[196:199], v[188:191], v[12:15]\n"
      "s_cbranch_vccnz R1_11_%=\n"
      "B1_12_%=:\n"
      "v_mfma_f32_16x16x32_bf16 v[16:19], a[200:203], v[160:163], v[16:19]\n"
      "buffer_load_dwordx4 v231, s[68:71], s95 offen offset:1024 lds\n"
      "v_mfma_f32_16x16x32_bf16 v[16:19], a[204:207], v[176:179], v[16:19]\n"
      "v_exp_f32 v128, v128\n"
      "v_mfma_f32_16x16x32_bf16 v[20:23], a[200:203], v[164:167], v[20:23]\n"
      "v_exp_f32 v129, v129\n"
      "v_mfma_f32_16x16x32_bf16 v[20:23], a[204:207], v[180:183], v[20:23]\n"
      "v_add_f32 v208, v208, v128\n"
      "v_add_f32 v208, v208, v129\n"
      "v_mfma_f32_16x16x32_bf16 v[24:27], a[200:203], v[168:171], v[24:27]\n"
      "v_exp_f32 v130, v130\n"
      "v_mfma_f32_16x16x32_bf16 v[24:27], a[204:207], v[184:187], v[24:27]\n"
      "buffer_load_dwordx4 v232, s[68:71], s95 offen offset:2048 lds\n"
      "v_mfma_f32_16x16x32_bf16 v[28:31], a[200:203], v[172:175], v[28:31]\n"
      "s_waitcnt vmcnt(4)\n"
      "s_barrier\n"
      "v_mfma_f32_16x16x32_bf16 v[28:31], a[204:207], v[188:191], v[28:31]\n"
      "ds_read_b128 a[128:131], v218 offset:0\n"
      "v_cvt_pk_bf16_f32 v128, v128, v129\n"
      "v_mfma_f32_16x16x32_bf16 v[32:35], a[208:211], v[160:163], v[32:35]\n"
      "ds_read_b128 a[132:135], v219 offset:0\n"
      "v_add_f32 v208, v208, v130\n"
      "v_mfma_f32_16x16x32_bf16 v[32:35], a[212:215], v[176:179], v[32:35]\n"
      "s_add_u32 s96, s95, 28672\n"
      "v_exp_f32 v131, v131\n"
      "v_mfma_f32_16x16x32_bf16 v[36:39], a[208:211], v[164:167], v[36:39]\n"
      "buffer_load_dwordx4 v233, s[68:71], s95 offen offset:3072 lds\n"
      "v_mfma_f32_16x16x32_bf16 v[36:39], a[212:215], v[180:183], v[36:39]\n"
      "ds_read_b128 a[136:139], v220 offset:0\n"
      "v_add_f32 v208, v208, v131\n"
      "v_mfma_f32_16x16x32_bf16 v[40:43], a[208:211], v[168:171], v[40:43]\n"
      "ds_read_b128 a[140:143], v221 offset:0\n"
      "v_cvt_pk_bf16_f32 v129, v130, v131\n"
      "v_mfma_f32_16x16x32_bf16 v[40:43], a[212:215], v[184:187], v[40:43]\n"
      "v_exp_f32 v144, v144\n"
      "v_mfma_f32_16x16x32_bf16 v[44:47], a[208:211], v[172:175], v[44:47]\n"
      "s_add_u32 m0, s81, 36864\n"
      "ds_read_b128 a[144:147], v218 offset:256\n"
      "v_add_f32 v208, v208, v144\n"
      "v_mfma_f32_16x16x32_bf16 v[44:47], a[212:215], v[188:191], v[44:47]\n"
      "buffer_load_dwordx4 v230, s[68:71], s96 offen offset:0 lds\n"
      "v_mfma_f32_16x16x32_bf16 v[48:51], a[216:219], v[160:163], v[48:51]\n"
      "v_exp_f32 v145, v145\n"
      "v_mfma_f32_16x16x32_bf16 v[48:51], a[220:223], v[176:179], v[48:51]\n"
      "ds_read_b128 a[148:151], v219 offset:256\n"
      "v_add_f32 v208, v208, v145\n"
      "v_mfma_f32_16x16x32_bf16 v[52:55], a[216:219], v[164:167], v[52:55]\n"
      "ds_read_b128 a[152:155], v220 offset:256\n"
      "v_cvt_pk_bf16_f32 v130, v144, v145\n"
      "v_mfma_f32_16x16x32_bf16 v[52:55], a[220:223], v[180:183], v[52:55]\n"
      "v_exp_f32 v146, v146\n"
      "v_mfma_f32_16x16x32_bf16 v[56:59], a[216:219], v[168:171], v[56:59]\n"
      "buffer_load_dwordx4 v231, s[68:71], s96 offen offset:1024 lds\n"
      "v_mfma_f32_16x16x32_bf16 v[56:59], a[220:223], v[184:187], v[56:59]\n"
      "ds_read_b128 a[156:159], v221 offset:256\n"
      "v_add_f32 v208, v208, v146\n"
      "v_mfma_f32_16x16x32_bf16 v[60:63], a[216:219], v[172:175], v[60:63]\n"
      "v_exp_f32 v147, v147\n"
      "v_mfma_f32_16x16x32_bf16 v[60:63], a[220:223], v[188:191], v[60:63]\n"
      "ds_read_b128 a[160:163], v218 offset:8192\n"
      "v_add_f32 v208, v208, v147\n"
      "v_mfma_f32_16x16x32_bf16 v[64:67], a[224:227], v[160:163], v[64:67]\n"
      "ds_read_b128 a[164:167], v219 offset:8192\n"
      "v_cvt_pk_bf16_f32 v131, v146, v147\n"
      "v_mfma_f32_16x16x32_bf16 v[64:67], a[228:231], v[176:179], v[64:67]\n"
      "buffer_load_dwordx4 v232, s[68:71], s96 offen offset:2048 lds\n"
      "v_mfma_f32_16x16x32_bf16 v[68:71], a[224:227], v[164:167], v[68:71]\n"
      "v_exp_f32 v132, v132\n"
      "v_mfma_f32_16x16x32_bf16 v[68:71], a[228:231], v[180:183], v[68:71]\n"
      "ds_read_b128 a[168:171], v220 offset:8192\n"
      "v_add_f32 v209, v209, v132\n"
      "v_mfma_f32_16x16x32_bf16 v[72:75], a[224:227], v[168:171], v[72:75]\n"
      "v_exp_f32 v133, v133\n"
      "v_mfma_f32_16x16x32_bf16 v[72:75], a[228:231], v[184:187], v[72:75]\n"
      "ds_read_b128 a[172:175], v221 offset:8192\n"
      "v_add_f32 v209, v209, v133\n"
      "v_mfma_f32_16x16x32_bf16 v[76:79], a[224:227], v[172:175], v[76:79]\n"
      "buffer_load_dwordx4 v233, s[68:71], s96 offen offset:3072 lds\n"
      "v_mfma_f32_16x16x32_bf16 v[76:79], a[228:231], v[188:191], v[76:79]\n"
      "v_exp_f32 v134, v134\n"
      "v_mfma_f32_16x16x32_bf16 v[80:83], a[232:235], v[160:163], v[80:83]\n"
      "v_cvt_pk_bf16_f32 v132, v132, v133\n"
      "v_add_f32 v209, v209, v134\n"
      "v_mfma_f32_16x16x32_bf16 v[80:83], a[236:239], v[176:179], v[80:83]\n"
      "v_exp_f32 v135, v135\n"
      "v_mfma_f32_16x16x32_bf16 v[84:87], a[232:235], v[164:167], v[84:87]\n"
      "s_add_u32 m0, s81, 98304\n"
      "ds_read_b128 a[176:179], v218 offset:8448\n"
      "v_add_f32 v209, v209, v135\n"
      "v_mfma_f32_16x16x32_bf16 v[84:87], a[236:239], v[180:183], v[84:87]\n"
      "buffer_load_dwordx4 v234, s[60:63], s83 offen offset:0 lds\n"
      "v_mfma_f32_16x16x32_bf16 v[88:91], a[232:235], v[168:171], v[88:91]\n"
      "ds_read_b128 a[180:183], v219 offset:8448\n"
      "v_cvt_pk_bf16_f32 v133, v134, v135\n"
      "v_mfma_f32_16x16x32_bf16 v[88:91], a[236:239], v[184:187], v[88:91]\n"
      "v_exp_f32 v148, v148\n"
      "v_mfma_f32_16x16x32_bf16 v[92:95], a[232:235], v[172:175], v[92:95]\n"
      "ds_read_b128 a[184:187], v220 offset:8448\n"
      "v_add_f32 v209, v209, v148\n"
      "v_mfma_f32_16x16x32_bf16 v[92:95], a[236:239], v[188:191], v[92:95]\n"
      "v_exp_f32 v149, v149\n"
      "v_mfma_f32_16x16x32_bf16 v[96:99], a[240:243], v[160:163], v[96:99]\n"
      "buffer_load_dwordx4 v235, s[60:63], s83 offen offset:1024 lds\n"
      "v_mfma_f32_16x16x32_bf16 v[96:99], a[244:247], v[176:179], v[96:99]\n"
      "ds_read_b128 a[188:191], v221 offset:8448\n"
      "v_add_f32 v209, v209, v149\n"
      "v_mfma_f32_16x16x32_bf16 v[100:103], a[240:243], v[164:167], v[100:103]\n"
      "ds_read_b64_tr_b16 a[192:193], v222 offset:0\n"
      "v_cvt_pk_bf16_f32 v134, v148, v149\n"
      "v_mfma_f32_16x16x32_bf16 v[100:103], a[244:247], v[180:183], v[100:103]\n"
      "v_exp_f32 v150, v150\n"
      "v_mfma_f32_16x16x32_bf16 v[104:107], a[240:243], v[168:171], v[104:107]\n"
      "ds_read_b64_tr_b16 a[194:195], v222 offset:8192\n"
      "v_add_f32 v209, v209, v150\n"
      "v_mfma_f32_16x16x32_bf16 v[104:107], a[244:247], v[184:187], v[104:107]\n"
      "buffer_load_dwordx4 v236, s[60:63], s83 offen offset:2048 lds\n"
      "v_mfma_f32_16x16x32_bf16 v[108:111], a[240:243], v[172:175], v[108:111]\n"
      "v_exp_f32 v151, v151\n"
      "v_mfma_f32_16x16x32_bf16 v[108:111], a[244:247], v[188:191], v[108:111]\n"
      "ds_read_b64_tr_b16 a[196:197], v222 offset:16384\n"
      "v_add_f32 v209, v209, v151\n"
      "v_mfma_f32_16x16x32_bf16 v[112:115], a[248:251], v[160:163], v[112:115]\n"
      "ds_read_b64_tr_b16 a[198:199], v222 offset:24576\n"
      "v_cvt_pk_bf16_f32 v135, v150, v151\n"
      "v_mfma_f32_16x16x32_bf16 v[112:115], a[252:255], v[176:179], v[112:115]\n"
      "s_add_u32 s96, s83, 4096\n"
      "v_exp_f32 v136, v136\n"
      "v_mfma_f32_16x16x32_bf16 v[116:119], a[248:251], v[164:167], v[116:119]\n"
      "buffer_load_dwordx4 v237, s[60:63], s83 offen offset:3072 lds\n"
      "v_mfma_f32_16x16x32_bf16 v[116:119], a[252:255], v[180:183], v[116:119]\n"
      "ds_read_b64_tr_b16 a[200:201], v223 offset:0\n"
      "v_add_f32 v210, v210, v136\n"
      "v_mfma_f32_16x16x32_bf16 v[120:123], a[248:251], v[168:171], v[120:123]\n"
      "v_exp_f32 v137, v137\n"
      "v_mfma_f32_16x16x32_bf16 v[120:123], a[252:255], v[184:187], v[120:123]\n"
      "ds_read_b64_tr_b16 a[202:203], v223 offset:8192\n"
      "v_add_f32 v210, v210, v137\n"
      "v_mfma_f32_16x16x32_bf16 v[124:127], a[248:251], v[172:175], v[124:127]\n"
      "ds_read_b64_tr_b16 a[204:205], v223 offset:16384\n"
      "v_cvt_pk_bf16_f32 v136, v136, v137\n"
      "v_mfma_f32_16x16x32_bf16 v[124:127], a[252:255], v[188:191], v[124:127]\n"
      "s_waitcnt lgkmcnt(15)\n"
      "s_add_u32 m0, s81, 102400\n"
      "ds_read_b64_tr_b16 a[206:207], v223 offset:24576\n"
      "v_mfma_f32_16x16x32_bf16 v[160:163], a[128:131], a[0:3], v[192:195]\n"
      "buffer_load_dwordx4 v234, s[60:63], s96 offen offset:0 lds\n"
      "v_mfma_f32_16x16x32_bf16 v[160:163], a[132:135], a[4:7], v[160:163]\n"
      "v_exp_f32 v138, v138\n"
      "v_mfma_f32_16x16x32_bf16 v[160:163], a[136:139], a[8:11], v[160:163]\n"
      "ds_read_b64_tr_b16 a[208:209], v224 offset:0\n"
      "v_add_f32 v210, v210, v138\n"
      "v_mfma_f32_16x16x32_bf16 v[160:163], a[140:143], a[12:15], v[160:163]\n"
      "v_exp_f32 v139, v139\n"
      "v_mfma_f32_16x16x32_bf16 v[160:163], a[144:147], a[16:19], v[160:163]\n"
      "v_add_f32 v210, v210, v139\n"
      "v_cvt_pk_bf16_f32 v137, v138, v139\n"
      "v_mfma_f32_16x16x32_bf16 v[160:163], a[148:151], a[20:23], v[160:163]\n"
      "buffer_load_dwordx4 v235, s[60:63], s96 offen offset:1024 lds\n"
      "v_mfma_f32_16x16x32_bf16 v[160:163], a[152:155], a[24:27], v[160:163]\n"
      "v_exp_f32 v152, v152\n"
      "v_mfma_f32_16x16x32_bf16 v[160:163], a[156:159], a[28:31], v[160:163]\n"
      "ds_read_b64_tr_b16 a[210:211], v224 offset:8192\n"
      "v_add_f32 v210, v210, v152\n"
      "v_mfma_f32_16x16x32_bf16 v[164:167], a[128:131], a[32:35], v[196:199]\n"
      "v_exp_f32 v153, v153\n"
      "v_mfma_f32_16x16x32_bf16 v[164:167], a[132:135], a[36:39], v[164:167]\n"
      "ds_read_b64_tr_b16 a[212:213], v224 offset:16384\n"
      "v_add_f32 v210, v210, v153\n"
      "v_mfma_f32_16x16x32_bf16 v[164:167], a[136:139], a[40:43], v[164:167]\n"
      "buffer_load_dwordx4 v236, s[60:63], s96 offen offset:2048 lds\n"
      "v_mfma_f32_16x16x32_bf16 v[164:167], a[140:143], a[44:47], v[164:167]\n"
      "ds_read_b64_tr_b16 a[214:215], v224 offset:24576\n"
      "v_cvt_pk_bf16_f32 v138, v152, v153\n"
      "v_mfma_f32_16x16x32_bf16 v[164:167], a[144:147], a[48:51], v[164:167]\n"
      "v_exp_f32 v154, v154\n"
      "v_mfma_f32_16x16x32_bf16 v[164:167], a[148:151], a[52:55], v[164:167]\n"
      "ds_read_b64_tr_b16 a[216:217], v225 offset:0\n"
      "v_add_f32 v210, v210, v154\n"
      "v_mfma_f32_16x16x32_bf16 v[164:167], a[152:155], a[56:59], v[164:167]\n"
      "v_exp_f32 v155, v155\n"
      "v_mfma_f32_16x16x32_bf16 v[164:167], a[156:159], a[60:63], v[164:167]\n"
      "buffer_load_dwordx4 v237, s[60:63], s96 offen offset:3072 lds\n"
      "v_mfma_f32_16x16x32_bf16 v[168:171], a[128:131], a[64:67], v[200:203]\n"
      "ds_read_b64_tr_b16 a[218:219], v225 offset:8192\n"
      "v_add_f32 v210, v210, v155\n"
      "v_mfma_f32_16x16x32_bf16 v[168:171], a[132:135], a[68:71], v[168:171]\n"
      "ds_read_b64_tr_b16 a[220:221], v225 offset:16384\n"
      "v_cvt_pk_bf16_f32 v139, v154, v155\n"
      "v_mfma_f32_16x16x32_bf16 v[168:171], a[136:139], a[72:75], v[168:171]\n"
      "ds_read_b64_tr_b16 a[222:223], v225 offset:24576\n"
      "v_max3_f32 v216, v160, v161, v161\n"
      "v_mfma_f32_16x16x32_bf16 v[168:171], a[140:143], a[76:79], v[168:171]\n"
      "ds_read_b64_tr_b16 a[224:225], v226 offset:0\n"
      "v_max3_f32 v216, v162, v163, v216\n"
      "v_mfma_f32_16x16x32_bf16 v[168:171], a[144:147], a[80:83], v[168:171]\n"
      "v_exp_f32 v140, v140\n"
      "v_mfma_f32_16x16x32_bf16 v[168:171], a[148:151], a[84:87], v[168:171]\n"
      "ds_read_b64_tr_b16 a[226:227], v226 offset:8192\n"
      "v_add_f32 v211, v211, v140\n"
      "v_mfma_f32_16x16x32_bf16 v[168:171], a[152:155], a[88:91], v[168:171]\n"
      "v_exp_f32 v141, v141\n"
      "v_mfma_f32_16x16x32_bf16 v[168:171], a[156:159], a[92:95], v[168:171]\n"
      "ds_read_b64_tr_b16 a[228:229], v226 offset:16384\n"
      "v_add_f32 v211, v211, v141\n"
      "v_mfma_f32_16x16x32_bf16 v[172:175], a[128:131], a[96:99], v[204:207]\n"
      "ds_read_b64_tr_b16 a[230:231], v226 offset:24576\n"
      "v_cvt_pk_bf16_f32 v140, v140, v141\n"
      "v_mfma_f32_16x16x32_bf16 v[172:175], a[132:135], a[100:103], v[172:175]\n"
      "ds_read_b64_tr_b16 a[232:233], v227 offset:0\n"
      "v_max3_f32 v216, v164, v165, v216\n"
      "v_mfma_f32_16x16x32_bf16 v[172:175], a[136:139], a[104:107], v[172:175]\n"
      "ds_read_b64_tr_b16 a[234:235], v227 offset:8192\n"
      "v_max3_f32 v216, v166, v167, v216\n"
      "v_mfma_f32_16x16x32_bf16 v[172:175], a[140:143], a[108:111], v[172:175]\n"
      "v_exp_f32 v142, v142\n"
      "v_mfma_f32_16x16x32_bf16 v[172:175], a[144:147], a[112:115], v[172:175]\n"
      "ds_read_b64_tr_b16 a[236:237], v227 offset:16384\n"
      "v_add_f32 v211, v211, v142\n"
      "v_mfma_f32_16x16x32_bf16 v[172:175], a[148:151], a[116:119], v[172:175]\n"
      "v_exp_f32 v143, v143\n"
      "v_mfma_f32_16x16x32_bf16 v[172:175], a[152:155], a[120:123], v[172:175]\n"
      "ds_read_b64_tr_b16 a[238:239], v227 offset:24576\n"
      "v_add_f32 v211, v211, v143\n"
      "v_mfma_f32_16x16x32_bf16 v[172:175], a[156:159], a[124:127], v[172:175]\n"
      "s_waitcnt lgkmcnt(15)\n"
      "ds_read_b64_tr_b16 a[240:241], v228 offset:0\n"
      "v_cvt_pk_bf16_f32 v141, v142, v143\n"
      "v_mfma_f32_16x16x32_bf16 v[176:179], a[160:163], a[0:3], v[192:195]\n"
      "v_exp_f32 v156, v156\n"
      "v_mfma_f32_16x16x32_bf16 v[176:179], a[164:167], a[4:7], v[176:179]\n"
      "ds_write_b128 v238, v[128:131] offset:0\n"
      "v_mfma_f32_16x16x32_bf16 v[176:179], a[168:171], a[8:11], v[176:179]\n"
      "v_exp_f32 v157, v157\n"
      "v_mfma_f32_16x16x32_bf16 v[176:179], a[172:175], a[12:15], v[176:179]\n"
      "v_add_f32 v211, v211, v156\n"
      "v_add_f32 v211, v211, v157\n"
      "v_mfma_f32_16x16x32_bf16 v[176:179], a[176:179], a[16:19], v[176:179]\n"
      "ds_write_b128 v238, v[132:135] offset:1024\n"
      "v_mfma_f32_16x16x32_bf16 v[176:179], a[180:183], a[20:23], v[176:179]\n"
      "v_exp_f32 v158, v158\n"
      "v_mfma_f32_16x16x32_bf16 v[176:179], a[184:187], a[24:27], v[176:179]\n"
      "v_cvt_pk_bf16_f32 v142, v156, v157\n"
      "v_add_f32 v211, v211, v158\n"
      "v_mfma_f32_16x16x32_bf16 v[176:179], a[188:191], a[28:31], v[176:179]\n"
      "ds_write_b128 v238, v[136:139] offset:2048\n"
      "v_mfma_f32_16x16x32_bf16 v[180:183], a[160:163], a[32:35], v[196:199]\n"
      "v_exp_f32 v159, v159\n"
      "v_mfma_f32_16x16x32_bf16 v[180:183], a[164:167], a[36:39], v[180:183]\n"
      "v_add_f32 v211, v211, v159\n"
      "v_cvt_pk_bf16_f32 v143, v158, v159\n"
      "v_mfma_f32_16x16x32_bf16 v[180:183], a[168:171], a[40:43], v[180:183]\n"
      "ds_write_b128 v238, v[140:143] offset:3072\n"
      "v_mfma_f32_16x16x32_bf16 v[180:183], a[172:175], a[44:47], v[180:183]\n"
      "v_max3_f32 v216, v168, v169, v216\n"
      "v_max3_f32 v216, v170, v171, v216\n"
      "v_mfma_f32_16x16x32_bf16 v[180:183], a[176:179], a[48:51], v[180:183]\n"
      "v_max3_f32 v216, v172, v173, v216\n"
      "v_max3_f32 v216, v174, v175, v216\n"
      "v_mfma_f32_16x16x32_bf16 v[180:183], a[180:183], a[52:55], v[180:183]\n"
      "s_waitcnt lgkmcnt(0)\n"
      "s_barrier\n"
      "v_mfma_f32_16x16x32_bf16 v[180:183], a[184:187], a[56:59], v[180:183]\n"
      "ds_read_b32 v240, v241 offset:0\n"
      "v_mfma_f32_16x16x32_bf16 v[180:183], a[188:191], a[60:63], v[180:183]\n"
      "ds_read_b128 v[144:147], v239 offset:0\n"
      "v_max3_f32 v216, v176, v177, v216\n"
      "v_mfma_f32_16x16x32_bf16 v[184:187], a[160:163], a[64:67], v[200:203]\n"
      "ds_read_b128 v[148:151], v239 offset:1024\n"
      "v_max3_f32 v216, v178, v179, v216\n"
      "v_mfma_f32_16x16x32_bf16 v[184:187], a[164:167], a[68:71], v[184:187]\n"
      "ds_read_b128 v[152:155], v239 offset:2048\n"
      "v_mfma_f32_16x16x32_bf16 v[184:187], a[168:171], a[72:75], v[184:187]\n"
      "ds_read_b128 v[156:159], v239 offset:3072\n"
      "v_mfma_f32_16x16x32_bf16 v[184:187], a[172:175], a[76:79], v[184:187]\n"
      "ds_read_b64_tr_b16 a[242:243], v228 offset:8192\n"
      "v_mfma_f32_16x16x32_bf16 v[184:187], a[176:179], a[80:83], v[184:187]\n"
      "s_waitcnt lgkmcnt(5)\n"
      "ds_read_b64_tr_b16 a[244:245], v228 offset:16384\n"
      "v_cmp_eq_u32 vcc, s88, v240\n"
      "v_mfma_f32_16x16x32_bf16 v[184:187], a[180:183], a[84:87], v[184:187]\n"
      "ds_read_b64_tr_b16 a[246:247], v228 offset:24576\n"
      "v_mfma_f32_16x16x32_bf16 v[184:187], a[184:187], a[88:91], v[184:187]\n"
      "ds_read_b64_tr_b16 a[248:249], v229 offset:0\n"
      "v_mfma_f32_16x16x32_bf16 v[184:187], a[188:191], a[92:95], v[184:187]\n"
      "ds_read_b64_tr_b16 a[250:251], v229 offset:8192\n"
      "v_max3_f32 v216, v180, v181, v216\n"
      "v_mfma_f32_16x16x32_bf16 v[188:191], a[160:163], a[96:99], v[204:207]\n"
      "ds_read_b64_tr_b16 a[252:253], v229 offset:16384\n"
      "v_max3_f32 v216, v182, v183, v216\n"
      "v_mfma_f32_16x16x32_bf16 v[188:191], a[164:167], a[100:103], v[188:191]\n"
      "ds_read_b64_tr_b16 a[254:255], v229 offset:24576\n"
      "v_mfma_f32_16x16x32_bf16 v[188:191], a[168:171], a[104:107], v[188:191]\n"
      "v_mfma_f32_16x16x32_bf16 v[188:191], a[172:175], a[108:111], v[188:191]\n"
      "v_mfma_f32_16x16x32_bf16 v[188:191], a[176:179], a[112:115], v[188:191]\n"
      "v_mfma_f32_16x16x32_bf16 v[188:191], a[180:183], a[116:119], v[188:191]\n"
      "v_mfma_f32_16x16x32_bf16 v[188:191], a[184:187], a[120:123], v[188:191]\n"
      "v_mfma_f32_16x16x32_bf16 v[188:191], a[188:191], a[124:127], v[188:191]\n"
      "s_waitcnt lgkmcnt(0)\n"
      "s_cbranch_vccnz R2_13_%=\n"
      "B2_14_%=:\n"
      "v_mfma_f32_16x16x32_bf16 v[0:3], a[192:195], v[128:131], v[0:3]\n"
      "s_add_u32 s88, s88, 1\n"
      "v_max3_f32 v216, v184, v185, v216\n"
      "v_max3_f32 v216, v186, v187, v216\n"
      "v_mfma_f32_16x16x32_bf16 v[0:3], a[196:199], v[144:147], v[0:3]\n"
      "s_add_u32 m0, s81, 0\n"
      "s_add_u32 s95, s91, 229376\n"
      "v_mfma_f32_16x16x32_bf16 v[4:7], a[192:195], v[132:135], v[4:7]\n"
      "buffer_load_dwordx4 v230, s[68:71], s95 offen offset:0 lds\n"
      "v_mfma_f32_16x16x32_bf16 v[4:7], a[196:199], v[148:151], v[4:7]\n"
      "v_mfma_f32_16x16x32_bf16 v[8:11], a[192:195], v[136:139], v[8:11]\n"
      "v_max3_f32 v216, v188, v189, v216\n"
      "v_max3_f32 v216, v190, v191, v216\n"
      "v_mfma_f32_16x16x32_bf16 v[8:11], a[196:199], v[152:155], v[8:11]\n"
      "v_cmp_lt_f32 vcc, s89, v216\n"
      "v_mfma_f32_16x16x32_bf16 v[12:15], a[192:195], v[140:143], v[12:15]\n"
      "v_mfma_f32_16x16x32_bf16 v[12:15], a[196:199], v[156:159], v[12:15]\n"
      "s_cbranch_vccnz R1_15_%=\n"
      "B1_16_%=:\n"
      "v_mfma_f32_16x16x32_bf16 v[16:19], a[200:203], v[128:131], v[16:19]\n"
      "buffer_load_dwordx4 v231, s[68:71], s95 offen offset:1024 lds\n"
      "v_mfma_f32_16x16x32_bf16 v[16:19], a[204:207], v[144:147], v[16:19]\n"
      "v_exp_f32 v160, v160\n"
      "v_mfma_f32_16x16x32_bf16 v[20:23], a[200:203], v[132:135], v[20:23]\n"
      "v_exp_f32 v161, v161\n"
      "v_mfma_f32_16x16x32_bf16 v[20:23], a[204:207], v[148:151], v[20:23]\n"
      "v_add_f32 v208, v208, v160\n"
      "v_add_f32 v208, v208, v161\n"
      "v_mfma_f32_16x16x32_bf16 v[24:27], a[200:203], v[136:139], v[24:27]\n"
      "v_exp_f32 v162, v162\n"
      "v_mfma_f32_16x16x32_bf16 v[24:27], a[204:207], v[152:155], v[24:27]\n"
      "buffer_load_dwordx4 v232, s[68:71], s95 offen offset:2048 lds\n"
      "v_mfma_f32_16x16x32_bf16 v[28:31], a[200:203], v[140:143], v[28:31]\n"
      "s_waitcnt vmcnt(3)\n"
      "s_barrier\n"
      "v_mfma_f32_16x16x32_bf16 v[28:31], a[204:207], v[156:159], v[28:31]\n"
      "ds_read_b64_tr_b16 a[192:193], v222 offset:32768\n"
      "v_cvt_pk_bf16_f32 v160, v160, v161\n"
      "v_mfma_f32_16x16x32_bf16 v[32:35], a[208:211], v[128:131], v[32:35]\n"
      "ds_read_b64_tr_b16 a[194:195], v222 offset:40960\n"
      "v_add_f32 v208, v208, v162\n"
      "v_mfma_f32_16x16x32_bf16 v[32:35], a[212:215], v[144:147], v[32:35]\n"
      "s_add_u32 s96, s95, 28672\n"
      "v_exp_f32 v163, v163\n"
      "v_mfma_f32_16x16x32_bf16 v[36:39], a[208:211], v[132:135], v[36:39]\n"
      "buffer_load_dwordx4 v233, s[68:71], s95 offen offset:3072 lds\n"
      "v_mfma_f32_16x16x32_bf16 v[36:39], a[212:215], v[148:151], v[36:39]\n"
      "ds_read_b64_tr_b16 a[196:197], v222 offset:49152\n"
      "v_add_f32 v208, v208, v163\n"
      "v_mfma_f32_16x16x32_bf16 v[40:43], a[208:211], v[136:139], v[40:43]\n"
      "ds_read_b64_tr_b16 a[198:199], v222 offset:57344\n"
      "v_cvt_pk_bf16_f32 v161, v162, v163\n"
      "v_mfma_f32_16x16x32_bf16 v[40:43], a[212:215], v[152:155], v[40:43]\n"
      "v_exp_f32 v176, v176\n"
      "v_mfma_f32_16x16x32_bf16 v[44:47], a[208:211], v[140:143], v[44:47]\n"
      "s_add_u32 m0, s81, 4096\n"
      "ds_read_b64_tr_b16 a[200:201], v223 offset:32768\n"
      "v_add_f32 v208, v208, v176\n"
      "v_mfma_f32_16x16x32_bf16 v[44:47], a[212:215], v[156:159], v[44:47]\n"
      "buffer_load_dwordx4 v230, s[68:71], s96 offen offset:0 lds\n"
      "v_mfma_f32_16x16x32_bf16 v[48:51], a[216:219], v[128:131], v[48:51]\n"
      "v_exp_f32 v177, v177\n"
      "v_mfma_f32_16x16x32_bf16 v[48:51], a[220:223], v[144:147], v[48:51]\n"
      "ds_read_b64_tr_b16 a[202:203], v223 offset:40960\n"
      "v_add_f32 v208, v208, v177\n"
      "v_mfma_f32_16x16x32_bf16 v[52:55], a[216:219], v[132:135], v[52:55]\n"
      "ds_read_b64_tr_b16 a[204:205], v223 offset:49152\n"
      "v_cvt_pk_bf16_f32 v162, v176, v177\n"
      "v_mfma_f32_16x16x32_bf16 v[52:55], a[220:223], v[148:151], v[52:55]\n"
      "s_add_u32 s91, s91, 128\n"
      "v_exp_f32 v178, v178\n"
      "v_mfma_f32_16x16x32_bf16 v[56:59], a[216:219], v[136:139], v[56:59]\n"
      "buffer_load_dwordx4 v231, s[68:71], s96 offen offset:1024 lds\n"
      "v_mfma_f32_16x16x32_bf16 v[56:59], a[220:223], v[152:155], v[56:59]\n"
      "ds_read_b64_tr_b16 a[206:207], v223 offset:57344\n"
      "v_add_f32 v208, v208, v178\n"
      "v_mfma_f32_16x16x32_bf16 v[60:63], a[216:219], v[140:143], v[60:63]\n"
      "v_exp_f32 v179, v179\n"
      "v_mfma_f32_16x16x32_bf16 v[60:63], a[220:223], v[156:159], v[60:63]\n"
      "ds_read_b64_tr_b16 a[208:209], v224 offset:32768\n"
      "v_add_f32 v208, v208, v179\n"
      "v_mfma_f32_16x16x32_bf16 v[64:67], a[224:227], v[128:131], v[64:67]\n"
      "s_cmp_ge_u32 s91, 896\n"
      "s_cselect_b32 s91, 0, s91\n"
      "ds_read_b64_tr_b16 a[210:211], v224 offset:40960\n"
      "v_mfma_f32_16x16x32_bf16 v[64:67], a[228:231], v[144:147], v[64:67]\n"
      "buffer_load_dwordx4 v232, s[68:71], s96 offen offset:2048 lds\n"
      "v_mfma_f32_16x16x32_bf16 v[68:71], a[224:227], v[132:135], v[68:71]\n"
      "ds_read_b64_tr_b16 a[212:213], v224 offset:49152\n"
      "v_cvt_pk_bf16_f32 v163, v178, v179\n"
      "v_mfma_f32_16x16x32_bf16 v[68:71], a[228:231], v[148:151], v[68:71]\n"
      "v_exp_f32 v164, v164\n"
      "v_mfma_f32_16x16x32_bf16 v[72:75], a[224:227], v[136:139], v[72:75]\n"
      "ds_read_b64_tr_b16 a[214:215], v224 offset:57344\n"
      "v_add_f32 v209, v209, v164\n"
      "v_mfma_f32_16x16x32_bf16 v[72:75], a[228:231], v[152:155], v[72:75]\n"
      "v_exp_f32 v165, v165\n"
      "v_mfma_f32_16x16x32_bf16 v[76:79], a[224:227], v[140:143], v[76:79]\n"
      "buffer_load_dwordx4 v233, s[68:71], s96 offen offset:3072 lds\n"
      "v_mfma_f32_16x16x32_bf16 v[76:79], a[228:231], v[156:159], v[76:79]\n"
      "v_add_f32 v209, v209, v165\n"
      "v_cvt_pk_bf16_f32 v164, v164, v165\n"
      "v_mfma_f32_16x16x32_bf16 v[80:83], a[232:235], v[128:131], v[80:83]\n"
      "v_exp_f32 v166, v166\n"
      "v_mfma_f32_16x16x32_bf16 v[80:83], a[236:239], v[144:147], v[80:83]\n"
      "ds_read_b64_tr_b16 a[216:217], v225 offset:32768\n"
      "v_add_f32 v209, v209, v166\n"
      "v_mfma_f32_16x16x32_bf16 v[84:87], a[232:235], v[132:135], v[84:87]\n"
      "s_add_u32 m0, s81, 65536\n"
      "s_add_u32 s95, s91, 0\n"
      "ds_read_b64_tr_b16 a[218:219], v225 offset:40960\n"
      "v_mfma_f32_16x16x32_bf16 v[84:87], a[236:239], v[148:151], v[84:87]\n"
      "buffer_load_dwordx4 v230, s[68:71], s95 offen offset:0 lds\n"
      "v_mfma_f32_16x16x32_bf16 v[88:91], a[232:235], v[136:139], v[88:91]\n"
      "v_exp_f32 v167, v167\n"
      "v_mfma_f32_16x16x32_bf16 v[88:91], a[236:239], v[152:155], v[88:91]\n"
      "ds_read_b64_tr_b16 a[220:221], v225 offset:49152\n"
      "v_add_f32 v209, v209, v167\n"
      "v_mfma_f32_16x16x32_bf16 v[92:95], a[232:235], v[140:143], v[92:95]\n"
      "ds_read_b64_tr_b16 a[222:223], v225 offset:57344\n"
      "v_cvt_pk_bf16_f32 v165, v166, v167\n"
      "v_mfma_f32_16x16x32_bf16 v[92:95], a[236:239], v[156:159], v[92:95]\n"
      "v_exp_f32 v180, v180\n"
      "v_mfma_f32_16x16x32_bf16 v[96:99], a[240:243], v[128:131], v[96:99]\n"
      "buffer_load_dwordx4 v231, s[68:71], s95 offen offset:1024 lds\n"
      "v_mfma_f32_16x16x32_bf16 v[96:99], a[244:247], v[144:147], v[96:99]\n"
      "ds_read_b64_tr_b16 a[224:225], v226 offset:32768\n"
      "v_add_f32 v209, v209, v180\n"
      "v_mfma_f32_16x16x32_bf16 v[100:103], a[240:243], v[132:135], v[100:103]\n"
      "v_exp_f32 v181, v181\n"
      "v_mfma_f32_16x16x32_bf16 v[100:103], a[244:247], v[148:151], v[100:103]\n"
      "ds_read_b64_tr_b16 a[226:227], v226 offset:40960\n"
      "v_add_f32 v209, v209, v181\n"
      "v_mfma_f32_16x16x32_bf16 v[104:107], a[240:243], v[136:139], v[104:107]\n"
      "ds_read_b64_tr_b16 a[228:229], v226 offset:49152\n"
      "v_cvt_pk_bf16_f32 v166, v180, v181\n"
      "v_mfma_f32_16x16x32_bf16 v[104:107], a[244:247], v[152:155], v[104:107]\n"
      "buffer_load_dwordx4 v232, s[68:71], s95 offen offset:2048 lds\n"
      "v_mfma_f32_16x16x32_bf16 v[108:111], a[240:243], v[140:143], v[108:111]\n"
      "v_exp_f32 v182, v182\n"
      "v_mfma_f32_16x16x32_bf16 v[108:111], a[244:247], v[156:159], v[108:111]\n"
      "ds_read_b64_tr_b16 a[230:231], v226 offset:57344\n"
      "v_add_f32 v209, v209, v182\n"
      "v_mfma_f32_16x16x32_bf16 v[112:115], a[248:251], v[128:131], v[112:115]\n"
      "v_exp_f32 v183, v183\n"
      "v_mfma_f32_16x16x32_bf16 v[112:115], a[252:255], v[144:147], v[112:115]\n"
      "s_add_u32 s96, s95, 28672\n"
      "ds_read_b64_tr_b16 a[232:233], v227 offset:32768\n"
      "v_add_f32 v209, v209, v183\n"
      "v_mfma_f32_16x16x32_bf16 v[116:119], a[248:251], v[132:135], v[116:119]\n"
      "buffer_load_dwordx4 v233, s[68:71], s95 offen offset:3072 lds\n"
      "v_mfma_f32_16x16x32_bf16 v[116:119], a[252:255], v[148:151], v[116:119]\n"
      "ds_read_b64_tr_b16 a[234:235], v227 offset:40960\n"
      "v_cvt_pk_bf16_f32 v167, v182, v183\n"
      "v_mfma_f32_16x16x32_bf16 v[120:123], a[248:251], v[136:139], v[120:123]\n"
      "v_exp_f32 v168, v168\n"
      "v_mfma_f32_16x16x32_bf16 v[120:123], a[252:255], v[152:155], v[120:123]\n"
      "ds_read_b64_tr_b16 a[236:237], v227 offset:49152\n"
      "v_add_f32 v210, v210, v168\n"
      "v_mfma_f32_16x16x32_bf16 v[124:127], a[248:251], v[140:143], v[124:127]\n"
      "s_add_u32 m0, s81, 69632\n"
      "v_exp_f32 v169, v169\n"
      "v_mfma_f32_16x16x32_bf16 v[124:127], a[252:255], v[156:159], v[124:127]\n"
      "buffer_load_dwordx4 v230, s[68:71], s96 offen offset:0 lds\n"
      "ds_read_b64_tr_b16 a[238:239], v227 offset:57344\n"
      "v_add_f32 v210, v210, v169\n"
      "ds_read_b64_tr_b16 a[240:241], v228 offset:32768\n"
      "v_cvt_pk_bf16_f32 v168, v168, v169\n"
      "v_exp_f32 v170, v170\n"
      "v_exp_f32 v171, v171\n"
      "buffer_load_dwordx4 v231, s[68:71], s96 offen offset:1024 lds\n"
      "v_add_f32 v210, v210, v170\n"
      "v_add_f32 v210, v210, v171\n"
      "ds_read_b64_tr_b16 a[242:243], v228 offset:40960\n"
      "v_cvt_pk_bf16_f32 v169, v170, v171\n"
      "v_exp_f32 v184, v184\n"
      "ds_read_b64_tr_b16 a[244:245], v228 offset:49152\n"
      "v_add_f32 v210, v210, v184\n"
      "buffer_load_dwordx4 v232, s[68:71], s96 offen offset:2048 lds\n"
      "v_exp_f32 v185, v185\n"
      "ds_read_b64_tr_b16 a[246:247], v228 offset:57344\n"
      "v_add_f32 v210, v210, v185\n"
      "ds_read_b64_tr_b16 a[248:249], v229 offset:32768\n"
      "v_cvt_pk_bf16_f32 v170, v184, v185\n"
      "v_exp_f32 v186, v186\n"
      "buffer_load_dwordx4 v233, s[68:71], s96 offen offset:3072 lds\n"
      "ds_read_b64_tr_b16 a[250:251], v229 offset:40960\n"
      "v_add_f32 v210, v210, v186\n"
      "v_exp_f32 v187, v187\n"
      "ds_read_b64_tr_b16 a[252:253], v229 offset:49152\n"
      "v_add_f32 v210, v210, v187\n"
      "ds_write_b128 v238, v[160:163] offset:0\n"
      "v_exp_f32 v172, v172\n"
      "s_nop 0\n"
      "v_cvt_pk_bf16_f32 v171, v186, v187\n"
      "v_add_f32 v211, v211, v172\n"
      "ds_write_b128 v238, v[164:167] offset:1024\n"
      "v_exp_f32 v173, v173\n"
      "s_nop 0\n"
      "v_add_f32 v211, v211, v173\n"
      "v_cvt_pk_bf16_f32 v172, v172, v173\n"
      "ds_write_b128 v238, v[168:171] offset:2048\n"
      "v_exp_f32 v174, v174\n"
      "v_exp_f32 v175, v175\n"
      "ds_read_b64_tr_b16 a[254:255], v229 offset:57344\n"
      "v_add_f32 v211, v211, v174\n"
      "v_add_f32 v211, v211, v175\n"
      "v_cvt_pk_bf16_f32 v173, v174, v175\n"
      "v_exp_f32 v188, v188\n"
      "v_exp_f32 v189, v189\n"
      "s_nop 0\n"
      "v_add_f32 v211, v211, v188\n"
      "v_add_f32 v211, v211, v189\n"
      "v_exp_f32 v190, v190\n"
      "s_nop 0\n"
      "v_cvt_pk_bf16_f32 v174, v188, v189\n"
      "v_add_f32 v211, v211, v190\n"
      "v_exp_f32 v191, v191\n"
      "s_nop 0\n"
      "v_add_f32 v211, v211, v191\n"
      "v_cvt_pk_bf16_f32 v175, v190, v191\n"
      "ds_write_b128 v238, v[172:175] offset:3072\n"
      "s_waitcnt lgkmcnt(0)\n"
      "s_barrier\n"
      "ds_read_b32 v240, v241 offset:4\n"
      "ds_read_b128 v[176:179], v239 offset:0\n"
      "ds_read_b128 v[180:183], v239 offset:1024\n"
      "ds_read_b128 v[184:187], v239 offset:2048\n"
      "ds_read_b128 v[188:191], v239 offset:3072\n"
      "s_waitcnt lgkmcnt(4)\n"
      "v_cmp_eq_u32 vcc, s88, v240\n"
      "s_waitcnt lgkmcnt(0)\n"
      "s_cbranch_vccnz R2_17_%=\n"
      "B2_18_%=:\n"
      "v_mov_b32 v248, v208\n"
      "v_mov_b32 v249, v209\n"
      "v_mov_b32 v250, v210\n"
      "v_mov_b32 v251, v211\n"
      "s_nop 1\n"
      "v_permlane32_swap_b32 v208, v248\n"
      "v_permlane32_swap_b32 v209, v249\n"
      "v_permlane32_swap_b32 v210, v250\n"
      "v_permlane32_swap_b32 v211, v251\n"
      "v_add_f32 v208, v208, v248\n"
      "v_add_f32 v209, v209, v249\n"
      "v_add_f32 v210, v210, v250\n"
      "v_add_f32 v211, v211, v251\n"
      "v_mov_b32 v248, v208\n"
      "v_mov_b32 v249, v209\n"
      "v_mov_b32 v250, v210\n"
      "v_mov_b32 v251, v211\n"
      "s_nop 1\n"
      "v_permlane16_swap_b32 v208, v248\n"
      "v_permlane16_swap_b32 v209, v249\n"
      "v_permlane16_swap_b32 v210, v250\n"
      "v_permlane16_swap_b32 v211, v251\n"
      "v_add_f32 v208, v208, v248\n"
      "v_add_f32 v209, v209, v249\n"
      "v_add_f32 v210, v210, v250\n"
      "v_add_f32 v211, v211, v251\n"
      "v_mfma_f32_16x16x32_bf16 v[0:3], a[192:195], v[160:163], v[0:3]\n"
      "v_add_u32 v243, s99, v242\n"
      "v_add_u32 v252, s97, v242\n"
      "v_mfma_f32_16x16x32_bf16 v[0:3], a[196:199], v[176:179], v[0:3]\n"
      "v_bfe_u32 v244, v242, 7, 2\n"
      "v_xor_b32 v244, s80, v244\n"
      "v_mfma_f32_16x16x32_bf16 v[4:7], a[192:195], v[164:167], v[4:7]\n"
      "v_lshlrev_b32 v244, 5, v244\n"
      "v_lshrrev_b32 v245, 6, v242\n"
      "v_mfma_f32_16x16x32_bf16 v[4:7], a[196:199], v[180:183], v[4:7]\n"
      "v_lshl_add_u32 v244, v245, 7, v244\n"
      "v_bfe_u32 v245, v242, 4, 2\n"
      "v_mfma_f32_16x16x32_bf16 v[8:11], a[192:195], v[168:171], v[8:11]\n"
      "v_lshl_add_u32 v218, v245, 3, v244\n"
      "v_add_u32 v219, 0x10000, v218\n"
      "v_mfma_f32_16x16x32_bf16 v[8:11], a[196:199], v[184:187], v[8:11]\n"
      "v_lshl_add_u32 v244, s80, 14, v242\n"
      "v_add_u32 v244, 98304, v244\n"
      "v_mfma_f32_16x16x32_bf16 v[12:15], a[192:195], v[172:175], v[12:15]\n"
      "v_mfma_f32_16x16x32_bf16 v[12:15], a[196:199], v[188:191], v[12:15]\n"
      "v_mfma_f32_16x16x32_bf16 v[16:19], a[200:203], v[160:163], v[16:19]\n"
      "v_mfma_f32_16x16x32_bf16 v[16:19], a[204:207], v[176:179], v[16:19]\n"
      "ds_write_b128 v243, v[208:211]\n"
      "v_mfma_f32_16x16x32_bf16 v[20:23], a[200:203], v[164:167], v[20:23]\n"
      "v_mfma_f32_16x16x32_bf16 v[20:23], a[204:207], v[180:183], v[20:23]\n"
      "v_mfma_f32_16x16x32_bf16 v[24:27], a[200:203], v[168:171], v[24:27]\n"
      "v_mfma_f32_16x16x32_bf16 v[24:27], a[204:207], v[184:187], v[24:27]\n"
      "s_waitcnt lgkmcnt(0)\n"
      "s_barrier\n"
      "v_mfma_f32_16x16x32_bf16 v[28:31], a[200:203], v[172:175], v[28:31]\n"
      "ds_read_b128 v[248:251], v252\n"
      "v_mfma_f32_16x16x32_bf16 v[28:31], a[204:207], v[188:191], v[28:31]\n"
      "ds_read_b64_tr_b16 v[128:129], v218 offset:32768\n"
      "v_mfma_f32_16x16x32_bf16 v[32:35], a[208:211], v[160:163], v[32:35]\n"
      "ds_read_b64_tr_b16 v[130:131], v218 offset:34816\n"
      "v_mfma_f32_16x16x32_bf16 v[32:35], a[212:215], v[176:179], v[32:35]\n"
      "v_mfma_f32_16x16x32_bf16 v[36:39], a[208:211], v[164:167], v[36:39]\n"
      "ds_read_b64_tr_b16 v[132:133], v218 offset:36864\n"
      "v_mfma_f32_16x16x32_bf16 v[36:39], a[212:215], v[180:183], v[36:39]\n"
      "ds_read_b64_tr_b16 v[134:135], v218 offset:38912\n"
      "v_mfma_f32_16x16x32_bf16 v[40:43], a[208:211], v[168:171], v[40:43]\n"
      "s_waitcnt lgkmcnt(4)\n"
      "v_add_f32 v208, v208, v248\n"
      "v_add_f32 v209, v209, v249\n"
      "v_mfma_f32_16x16x32_bf16 v[40:43], a[212:215], v[184:187], v[40:43]\n"
      "ds_read_b64_tr_b16 v[136:137], v218 offset:40960\n"
      "v_add_f32 v210, v210, v250\n"
      "v_mfma_f32_16x16x32_bf16 v[44:47], a[208:211], v[172:175], v[44:47]\n"
      "ds_read_b64_tr_b16 v[138:139], v218 offset:43008\n"
      "v_add_f32 v211, v211, v251\n"
      "v_mfma_f32_16x16x32_bf16 v[44:47], a[212:215], v[188:191], v[44:47]\n"
      "v_rcp_f32 v208, v208\n"
      "v_mfma_f32_16x16x32_bf16 v[48:51], a[216:219], v[160:163], v[48:51]\n"
      "ds_read_b64_tr_b16 v[140:141], v218 offset:45056\n"
      "v_mfma_f32_16x16x32_bf16 v[48:51], a[220:223], v[176:179], v[48:51]\n"
      "ds_read_b64_tr_b16 v[142:143], v218 offset:47104\n"
      "v_mfma_f32_16x16x32_bf16 v[52:55], a[216:219], v[164:167], v[52:55]\n"
      "v_rcp_f32 v209, v209\n"
      "v_mfma_f32_16x16x32_bf16 v[52:55], a[220:223], v[180:183], v[52:55]\n"
      "ds_read_b64_tr_b16 v[144:145], v218 offset:49152\n"
      "v_mfma_f32_16x16x32_bf16 v[56:59], a[216:219], v[168:171], v[56:59]\n"
      "v_rcp_f32 v210, v210\n"
      "v_mfma_f32_16x16x32_bf16 v[56:59], a[220:223], v[184:187], v[56:59]\n"
      "ds_read_b64_tr_b16 v[146:147], v218 offset:51200\n"
      "v_mfma_f32_16x16x32_bf16 v[60:63], a[216:219], v[172:175], v[60:63]\n"
      "ds_read_b64_tr_b16 v[148:149], v218 offset:53248\n"
      "v_mfma_f32_16x16x32_bf16 v[60:63], a[220:223], v[188:191], v[60:63]\n"
      "v_rcp_f32 v211, v211\n"
      "v_mfma_f32_16x16x32_bf16 v[64:67], a[224:227], v[160:163], v[64:67]\n"
      "ds_read_b64_tr_b16 v[150:151], v218 offset:55296\n"
      "v_mfma_f32_16x16x32_bf16 v[64:67], a[228:231], v[176:179], v[64:67]\n"
      "ds_read_b64_tr_b16 v[152:153], v218 offset:57344\n"
      "v_mul_f32 v0, v0, v208\n"
      "v_mfma_f32_16x16x32_bf16 v[68:71], a[224:227], v[164:167], v[68:71]\n"
      "v_mul_f32 v1, v1, v208\n"
      "v_mul_f32 v2, v2, v208\n"
      "v_mfma_f32_16x16x32_bf16 v[68:71], a[228:231], v[180:183], v[68:71]\n"
      "ds_read_b64_tr_b16 v[154:155], v218 offset:59392\n"
      "v_mul_f32 v3, v3, v208\n"
      "v_mfma_f32_16x16x32_bf16 v[72:75], a[224:227], v[168:171], v[72:75]\n"
      "ds_read_b64_tr_b16 v[156:157], v218 offset:61440\n"
      "v_mul_f32 v16, v16, v208\n"
      "v_mfma_f32_16x16x32_bf16 v[72:75], a[228:231], v[184:187], v[72:75]\n"
      "v_mul_f32 v17, v17, v208\n"
      "v_mul_f32 v18, v18, v208\n"
      "v_mfma_f32_16x16x32_bf16 v[76:79], a[224:227], v[172:175], v[76:79]\n"
      "ds_read_b64_tr_b16 v[158:159], v218 offset:63488\n"
      "v_mul_f32 v19, v19, v208\n"
      "v_mfma_f32_16x16x32_bf16 v[76:79], a[228:231], v[188:191], v[76:79]\n"
      "v_cvt_pk_bf16_f32 v0, v0, v1\n"
      "v_cvt_pk_bf16_f32 v1, v2, v3\n"
      "v_mfma_f32_16x16x32_bf16 v[80:83], a[232:235], v[160:163], v[80:83]\n"
      "v_cvt_pk_bf16_f32 v2, v16, v17\n"
      "v_cvt_pk_bf16_f32 v3, v18, v19\n"
      "v_mfma_f32_16x16x32_bf16 v[80:83], a[236:239], v[176:179], v[80:83]\n"
      "v_mul_f32 v4, v4, v209\n"
      "v_mul_f32 v5, v5, v209\n"
      "v_mfma_f32_16x16x32_bf16 v[84:87], a[232:235], v[164:167], v[84:87]\n"
      "v_mul_f32 v6, v6, v209\n"
      "v_mul_f32 v7, v7, v209\n"
      "v_mfma_f32_16x16x32_bf16 v[84:87], a[236:239], v[180:183], v[84:87]\n"
      "v_mul_f32 v20, v20, v209\n"
      "v_mul_f32 v21, v21, v209\n"
      "v_mfma_f32_16x16x32_bf16 v[88:91], a[232:235], v[168:171], v[88:91]\n"
      "v_mul_f32 v22, v22, v209\n"
      "v_mul_f32 v23, v23, v209\n"
      "v_mfma_f32_16x16x32_bf16 v[88:91], a[236:239], v[184:187], v[88:91]\n"
      "v_cvt_pk_bf16_f32 v4, v4, v5\n"
      "v_cvt_pk_bf16_f32 v5, v6, v7\n"
      "v_mfma_f32_16x16x32_bf16 v[92:95], a[232:235], v[172:175], v[92:95]\n"
      "s_waitcnt lgkmcnt(0)\n"
      "s_barrier\n"
      "v_mfma_f32_16x16x32_bf16 v[92:95], a[236:239], v[188:191], v[92:95]\n"
      "s_add_u32 m0, s81, 32768\n"
      "s_add_u32 s95, s91, 229376\n"
      "v_cvt_pk_bf16_f32 v6, v20, v21\n"
      "v_mfma_f32_16x16x32_bf16 v[96:99], a[240:243], v[160:163], v[96:99]\n"
      "buffer_load_dwordx4 v230, s[68:71], s95 offen offset:0 lds\n"
      "v_mfma_f32_16x16x32_bf16 v[96:99], a[244:247], v[176:179], v[96:99]\n"
      "v_cvt_pk_bf16_f32 v7, v22, v23\n"
      "v_mul_f32 v8, v8, v210\n"
      "v_mfma_f32_16x16x32_bf16 v[100:103], a[240:243], v[164:167], v[100:103]\n"
      "v_mul_f32 v9, v9, v210\n"
      "v_mul_f32 v10, v10, v210\n"
      "v_mfma_f32_16x16x32_bf16 v[100:103], a[244:247], v[180:183], v[100:103]\n"
      "v_mul_f32 v11, v11, v210\n"
      "v_mul_f32 v24, v24, v210\n"
      "v_mfma_f32_16x16x32_bf16 v[104:107], a[240:243], v[168:171], v[104:107]\n"
      "buffer_load_dwordx4 v231, s[68:71], s95 offen offset:1024 lds\n"
      "v_mfma_f32_16x16x32_bf16 v[104:107], a[244:247], v[184:187], v[104:107]\n"
      "v_mul_f32 v25, v25, v210\n"
      "v_mul_f32 v26, v26, v210\n"
      "v_mfma_f32_16x16x32_bf16 v[108:111], a[240:243], v[172:175], v[108:111]\n"
      "v_mul_f32 v27, v27, v210\n"
      "v_cvt_pk_bf16_f32 v8, v8, v9\n"
      "v_mfma_f32_16x16x32_bf16 v[108:111], a[244:247], v[188:191], v[108:111]\n"
      "v_cvt_pk_bf16_f32 v9, v10, v11\n"
      "v_cvt_pk_bf16_f32 v10, v24, v25\n"
      "v_mfma_f32_16x16x32_bf16 v[112:115], a[248:251], v[160:163], v[112:115]\n"
      "buffer_load_dwordx4 v232, s[68:71], s95 offen offset:2048 lds\n"
      "v_mfma_f32_16x16x32_bf16 v[112:115], a[252:255], v[176:179], v[112:115]\n"
      "v_cvt_pk_bf16_f32 v11, v26, v27\n"
      "v_mfma_f32_16x16x32_bf16 v[116:119], a[248:251], v[164:167], v[116:119]\n"
      "v_mfma_f32_16x16x32_bf16 v[116:119], a[252:255], v[180:183], v[116:119]\n"
      "s_add_u32 s96, s95, 28672\n"
      "v_mfma_f32_16x16x32_bf16 v[120:123], a[248:251], v[168:171], v[120:123]\n"
      "buffer_load_dwordx4 v233, s[68:71], s95 offen offset:3072 lds\n"
      "v_mfma_f32_16x16x32_bf16 v[120:123], a[252:255], v[184:187], v[120:123]\n"
      "v_mfma_f32_16x16x32_bf16 v[124:127], a[248:251], v[172:175], v[124:127]\n"
      "v_mfma_f32_16x16x32_bf16 v[124:127], a[252:255], v[188:191], v[124:127]\n"
      "s_load_dwordx8 s[56:63], s[34:35], 0x40\n"
      "s_load_dwordx2 s[64:65], s[34:35], 0x60\n"
      "ds_write_b128 v244, v[0:3] offset:0\n"
      "v_pk_mul_f32 v[12:13], v[12:13], v[210:211] op_sel:[0,1] op_sel_hi:[1,1]\n"
      "v_pk_mul_f32 v[14:15], v[14:15], v[210:211] op_sel:[0,1] op_sel_hi:[1,1]\n"
      "v_pk_mul_f32 v[28:29], v[28:29], v[210:211] op_sel:[0,1] op_sel_hi:[1,1]\n"
      "v_pk_mul_f32 v[30:31], v[30:31], v[210:211] op_sel:[0,1] op_sel_hi:[1,1]\n"
      "v_cvt_pk_bf16_f32 v12, v12, v13\n"
      "v_cvt_pk_bf16_f32 v13, v14, v15\n"
      "v_cvt_pk_bf16_f32 v14, v28, v29\n"
      "v_cvt_pk_bf16_f32 v15, v30, v31\n"
      "ds_write_b128 v244, v[12:15] offset:3072\n"
      "s_add_u32 m0, s81, 36864\n"
      "s_nop 0\n"
      "buffer_load_dwordx4 v230, s[68:71], s96 offen offset:0 lds\n"
      "ds_write_b128 v244, v[4:7] offset:1024\n"
      "v_pk_mul_f32 v[32:33], v[32:33], v[208:209] op_sel:[0,0] op_sel_hi:[1,0]\n"
      "v_pk_mul_f32 v[34:35], v[34:35], v[208:209] op_sel:[0,0] op_sel_hi:[1,0]\n"
      "v_pk_mul_f32 v[48:49], v[48:49], v[208:209] op_sel:[0,0] op_sel_hi:[1,0]\n"
      "v_pk_mul_f32 v[50:51], v[50:51], v[208:209] op_sel:[0,0] op_sel_hi:[1,0]\n"
      "v_cvt_pk_bf16_f32 v32, v32, v33\n"
      "v_cvt_pk_bf16_f32 v33, v34, v35\n"
      "v_cvt_pk_bf16_f32 v34, v48, v49\n"
      "v_cvt_pk_bf16_f32 v35, v50, v51\n"
      "ds_write_b128 v244, v[32:35] offset:4096\n"
      "ds_write_b128 v244, v[8:11] offset:2048\n"
      "v_pk_mul_f32 v[36:37], v[36:37], v[208:209] op_sel:[0,1] op_sel_hi:[1,1]\n"
      "v_pk_mul_f32 v[38:39], v[38:39], v[208:209] op_sel:[0,1] op_sel_hi:[1,1]\n"
      "v_pk_mul_f32 v[52:53], v[52:53], v[208:209] op_sel:[0,1] op_sel_hi:[1,1]\n"
      "v_pk_mul_f32 v[54:55], v[54:55], v[208:209] op_sel:[0,1] op_sel_hi:[1,1]\n"
      "v_cvt_pk_bf16_f32 v36, v36, v37\n"
      "v_cvt_pk_bf16_f32 v37, v38, v39\n"
      "v_cvt_pk_bf16_f32 v38, v52, v53\n"
      "v_cvt_pk_bf16_f32 v39, v54, v55\n"
      "ds_write_b128 v244, v[36:39] offset:5120\n"
      "s_add_u32 s91, s91, 128\n"
      "s_nop 0\n"
      "buffer_load_dwordx4 v231, s[68:71], s96 offen offset:1024 lds\n"
      "v_pk_mul_f32 v[40:41], v[40:41], v[210:211] op_sel:[0,0] op_sel_hi:[1,0]\n"
      "v_pk_mul_f32 v[42:43], v[42:43], v[210:211] op_sel:[0,0] op_sel_hi:[1,0]\n"
      "v_pk_mul_f32 v[56:57], v[56:57], v[210:211] op_sel:[0,0] op_sel_hi:[1,0]\n"
      "v_pk_mul_f32 v[58:59], v[58:59], v[210:211] op_sel:[0,0] op_sel_hi:[1,0]\n"
      "v_cvt_pk_bf16_f32 v40, v40, v41\n"
      "v_cvt_pk_bf16_f32 v41, v42, v43\n"
      "v_cvt_pk_bf16_f32 v42, v56, v57\n"
      "v_cvt_pk_bf16_f32 v43, v58, v59\n"
      "ds_write_b128 v244, v[40:43] offset:6144\n"
      "v_pk_mul_f32 v[44:45], v[44:45], v[210:211] op_sel:[0,1] op_sel_hi:[1,1]\n"
      "v_pk_mul_f32 v[46:47], v[46:47], v[210:211] op_sel:[0,1] op_sel_hi:[1,1]\n"
      "v_pk_mul_f32 v[60:61], v[60:61], v[210:211] op_sel:[0,1] op_sel_hi:[1,1]\n"
      "v_pk_mul_f32 v[62:63], v[62:63], v[210:211] op_sel:[0,1] op_sel_hi:[1,1]\n"
      "v_cvt_pk_bf16_f32 v44, v44, v45\n"
      "v_cvt_pk_bf16_f32 v45, v46, v47\n"
      "v_cvt_pk_bf16_f32 v46, v60, v61\n"
      "v_cvt_pk_bf16_f32 v47, v62, v63\n"
      "ds_write_b128 v244, v[44:47] offset:7168\n"
      "s_cmp_ge_u32 s91, 896\n"
      "s_cselect_b32 s91, 0, s91\n"
      "s_nop 0\n"
      "buffer_load_dwordx4 v232, s[68:71], s96 offen offset:2048 lds\n"
      "v_pk_mul_f32 v[64:65], v[64:65], v[208:209] op_sel:[0,0] op_sel_hi:[1,0]\n"
      "v_pk_mul_f32 v[66:67], v[66:67], v[208:209] op_sel:[0,0] op_sel_hi:[1,0]\n"
      "v_pk_mul_f32 v[80:81], v[80:81], v[208:209] op_sel:[0,0] op_sel_hi:[1,0]\n"
      "v_pk_mul_f32 v[82:83], v[82:83], v[208:209] op_sel:[0,0] op_sel_hi:[1,0]\n"
      "v_cvt_pk_bf16_f32 v64, v64, v65\n"
      "v_cvt_pk_bf16_f32 v65, v66, v67\n"
      "v_cvt_pk_bf16_f32 v66, v80, v81\n"
      "v_cvt_pk_bf16_f32 v67, v82, v83\n"
      "ds_write_b128 v244, v[64:67] offset:8192\n"
      "v_pk_mul_f32 v[68:69], v[68:69], v[208:209] op_sel:[0,1] op_sel_hi:[1,1]\n"
      "v_pk_mul_f32 v[70:71], v[70:71], v[208:209] op_sel:[0,1] op_sel_hi:[1,1]\n"
      "v_pk_mul_f32 v[84:85], v[84:85], v[208:209] op_sel:[0,1] op_sel_hi:[1,1]\n"
      "v_pk_mul_f32 v[86:87], v[86:87], v[208:209] op_sel:[0,1] op_sel_hi:[1,1]\n"
      "v_cvt_pk_bf16_f32 v68, v68, v69\n"
      "v_cvt_pk_bf16_f32 v69, v70, v71\n"
      "v_cvt_pk_bf16_f32 v70, v84, v85\n"
      "v_cvt_pk_bf16_f32 v71, v86, v87\n"
      "ds_write_b128 v244, v[68:71] offset:9216\n"
      "buffer_load_dwordx4 v233, s[68:71], s96 offen offset:3072 lds\n"
      "v_pk_mul_f32 v[72:73], v[72:73], v[210:211] op_sel:[0,0] op_sel_hi:[1,0]\n"
      "v_pk_mul_f32 v[74:75], v[74:75], v[210:211] op_sel:[0,0] op_sel_hi:[1,0]\n"
      "v_pk_mul_f32 v[88:89], v[88:89], v[210:211] op_sel:[0,0] op_sel_hi:[1,0]\n"
      "v_pk_mul_f32 v[90:91], v[90:91], v[210:211] op_sel:[0,0] op_sel_hi:[1,0]\n"
      "v_cvt_pk_bf16_f32 v72, v72, v73\n"
      "v_cvt_pk_bf16_f32 v73, v74, v75\n"
      "v_cvt_pk_bf16_f32 v74, v88, v89\n"
      "v_cvt_pk_bf16_f32 v75, v90, v91\n"
      "ds_write_b128 v244, v[72:75] offset:10240\n"
      "v_pk_mul_f32 v[76:77], v[76:77], v[210:211] op_sel:[0,1] op_sel_hi:[1,1]\n"
      "v_pk_mul_f32 v[78:79], v[78:79], v[210:211] op_sel:[0,1] op_sel_hi:[1,1]\n"
      "v_pk_mul_f32 v[92:93], v[92:93], v[210:211] op_sel:[0,1] op_sel_hi:[1,1]\n"
      "v_pk_mul_f32 v[94:95], v[94:95], v[210:211] op_sel:[0,1] op_sel_hi:[1,1]\n"
      "v_cvt_pk_bf16_f32 v76, v76, v77\n"
      "v_cvt_pk_bf16_f32 v77, v78, v79\n"
      "v_cvt_pk_bf16_f32 v78, v92, v93\n"
      "v_cvt_pk_bf16_f32 v79, v94, v95\n"
      "ds_write_b128 v244, v[76:79] offset:11264\n"
      "v_pk_mul_f32 v[96:97], v[96:97], v[208:209] op_sel:[0,0] op_sel_hi:[1,0]\n"
      "v_pk_mul_f32 v[98:99], v[98:99], v[208:209] op_sel:[0,0] op_sel_hi:[1,0]\n"
      "v_pk_mul_f32 v[112:113], v[112:113], v[208:209] op_sel:[0,0] op_sel_hi:[1,0]\n"
      "v_pk_mul_f32 v[114:115], v[114:115], v[208:209] op_sel:[0,0] op_sel_hi:[1,0]\n"
      "v_cvt_pk_bf16_f32 v96, v96, v97\n"
      "v_cvt_pk_bf16_f32 v97, v98, v99\n"
      "v_cvt_pk_bf16_f32 v98, v112, v113\n"
      "v_cvt_pk_bf16_f32 v99, v114, v115\n"
      "ds_write_b128 v244, v[96:99] offset:12288\n"
      "v_pk_mul_f32 v[100:101], v[100:101], v[208:209] op_sel:[0,1] op_sel_hi:[1,1]\n"
      "v_pk_mul_f32 v[102:103], v[102:103], v[208:209] op_sel:[0,1] op_sel_hi:[1,1]\n"
      "v_pk_mul_f32 v[116:117], v[116:117], v[208:209] op_sel:[0,1] op_sel_hi:[1,1]\n"
      "v_pk_mul_f32 v[118:119], v[118:119], v[208:209] op_sel:[0,1] op_sel_hi:[1,1]\n"
      "v_cvt_pk_bf16_f32 v100, v100, v101\n"
      "v_cvt_pk_bf16_f32 v101, v102, v103\n"
      "v_cvt_pk_bf16_f32 v102, v116, v117\n"
      "v_cvt_pk_bf16_f32 v103, v118, v119\n"
      "ds_write_b128 v244, v[100:103] offset:13312\n"
      "v_pk_mul_f32 v[104:105], v[104:105], v[210:211] op_sel:[0,0] op_sel_hi:[1,0]\n"
      "v_pk_mul_f32 v[106:107], v[106:107], v[210:211] op_sel:[0,0] op_sel_hi:[1,0]\n"
      "v_pk_mul_f32 v[120:121], v[120:121], v[210:211] op_sel:[0,0] op_sel_hi:[1,0]\n"
      "v_pk_mul_f32 v[122:123], v[122:123], v[210:211] op_sel:[0,0] op_sel_hi:[1,0]\n"
      "v_cvt_pk_bf16_f32 v104, v104, v105\n"
      "v_cvt_pk_bf16_f32 v105, v106, v107\n"
      "v_cvt_pk_bf16_f32 v106, v120, v121\n"
      "v_cvt_pk_bf16_f32 v107, v122, v123\n"
      "ds_write_b128 v244, v[104:107] offset:14336\n"
      "v_pk_mul_f32 v[108:109], v[108:109], v[210:211] op_sel:[0,1] op_sel_hi:[1,1]\n"
      "v_pk_mul_f32 v[110:111], v[110:111], v[210:211] op_sel:[0,1] op_sel_hi:[1,1]\n"
      "v_pk_mul_f32 v[124:125], v[124:125], v[210:211] op_sel:[0,1] op_sel_hi:[1,1]\n"
      "v_pk_mul_f32 v[126:127], v[126:127], v[210:211] op_sel:[0,1] op_sel_hi:[1,1]\n"
      "v_cvt_pk_bf16_f32 v108, v108, v109\n"
      "v_cvt_pk_bf16_f32 v109, v110, v111\n"
      "v_cvt_pk_bf16_f32 v110, v124, v125\n"
      "v_cvt_pk_bf16_f32 v111, v126, v127\n"
      "ds_write_b128 v244, v[108:111] offset:15360\n"
      "v_add_u32 v243, 98304, v242\n"
      "s_waitcnt lgkmcnt(0)\n"
      "s_barrier\n"
      "ds_read_b128 a[0:3], v243 offset:0\n"
      "ds_read_b128 a[16:19], v243 offset:4096\n"
      "ds_read_b128 a[32:35], v243 offset:8192\n"
      "ds_read_b128 a[48:51], v243 offset:12288\n"
      "ds_read_b128 a[64:67], v243 offset:16384\n"
      "ds_read_b128 a[80:83], v243 offset:20480\n"
      "ds_read_b128 a[96:99], v243 offset:24576\n"
      "ds_read_b128 a[112:115], v243 offset:28672\n"
      "ds_read_b128 a[4:7], v243 offset:1024\n"
      "ds_read_b128 a[20:23], v243 offset:5120\n"
      "ds_read_b128 a[36:39], v243 offset:9216\n"
      "ds_read_b128 a[52:55], v243 offset:13312\n"
      "ds_read_b128 a[68:71], v243 offset:17408\n"
      "ds_read_b128 a[84:87], v243 offset:21504\n"
      "ds_read_b128 a[100:103], v243 offset:25600\n"
      "ds_read_b128 a[116:119], v243 offset:29696\n"
      "v_bfe_u32 v252, v242, 6, 2\n"
      "v_mul_u32_u24 v252, 57344, v252\n"
      "v_lshrrev_b32 v253, 8, v242\n"
      "v_lshl_add_u32 v252, v253, 6, v252\n"
      "v_and_b32 v253, 48, v242\n"
      "v_add_u32 v238, v253, v252\n"
      "s_mul_i32 s84, s92, 458752\n"
      "s_mul_i32 s85, s93, 7168\n"
      "s_add_u32 s84, s84, s85\n"
      "s_lshl_b32 s85, s80, 8\n"
      "s_add_u32 s84, s84, s85\n"
      "s_add_u32 s82, s84, 4194304\n"
      "s_lshl_b32 s83, s98, 10\n"
      "s_mov_b32 s89, 28672\n"
      "s_mov_b32 s72, s50\n"
      "s_and_b32 s73, s51, 0xffff\n"
      "s_mov_b32 s74, 18874368\n"
      "v_lshrrev_b32 v254, 4, v242\n"
      "s_mul_i32 s86, s93, 28\n"
      "s_lshl_b32 s87, s93, 2\n"
      "s_sub_u32 s87, s87, 28\n"
      "s_cmp_lt_u32 s92, 28\n"
      "s_cselect_b32 s94, s86, s87\n"
      "s_cselect_b64 s[40:41], s[58:59], s[62:63]\n"
      "s_cselect_b64 s[44:45], s[60:61], s[62:63]\n"
      "s_cmp_lt_u32 s92, 28\n"
      "s_cselect_b32 s99, 229376, s89\n"
      "s_cselect_b32 s66, 4294737920, s89\n"
      "s_cselect_b32 s67, 1073741824, s89\n"
      "s_cmp_lt_u32 s92, 28\n"
      "s_cselect_b32 s86, 4, 5\n"
      "s_cselect_b64 vcc, -1, 0\n"
      "s_add_u32 s94, s94, s92\n"
      "s_lshl_b32 s97, s94, s86\n"
      "s_and_b32 s41, s41, 0xffff\n"
      "s_mov_b32 s42, 1605632\n"
      "s_mov_b32 s43, 0x20000\n"
      "s_and_b32 s45, s45, 0xffff\n"
      "s_mov_b32 s46, 1605632\n"
      "s_mov_b32 s47, 0x20000\n"
      "v_lshl_add_u32 v245, s80, 7, v254\n"
      "v_mul_u32_u24 v245, 3584, v245\n"
      "s_mul_i32 s87, s80, 448\n"
      "v_lshrrev_b32 v246, 1, v254\n"
      "v_add_u32 v246, s87, v246\n"
      "v_mul_u32_u24 v246, 896, v246\n"
      "v_and_or_b32 v246, v242, 16, v246\n"
      "v_cndmask_b32 v234, v246, v245, vcc\n"
      "s_lshl_b32 s98, s80, 14\n"
      "v_add_u32 v239, s98, v242\n"
      "v_add_u32 v239, 98304, v239\n"
      "s_waitcnt vmcnt(16) lgkmcnt(8)\n"
      "s_barrier\n"
      "v_mfma_f32_16x16x32_bf16 v[0:3], v[128:131], a[0:3], 0\n"
      "ds_read_b128 a[8:11], v243 offset:2048\n"
      "ds_read_b128 a[24:27], v243 offset:6144\n"
      "v_mfma_f32_16x16x32_bf16 v[0:3], v[132:135], a[16:19], v[0:3]\n"
      "buffer_load_dwordx4 v[64:67], v234, s[40:43], s97 offen\n"
      "v_mfma_f32_16x16x32_bf16 v[0:3], v[136:139], a[32:35], v[0:3]\n"
      "ds_read_b128 a[40:43], v243 offset:10240\n"
      "ds_read_b128 a[56:59], v243 offset:14336\n"
      "v_mfma_f32_16x16x32_bf16 v[0:3], v[140:143], a[48:51], v[0:3]\n"
      "ds_read_b128 a[72:75], v243 offset:18432\n"
      "ds_read_b128 a[88:91], v243 offset:22528\n"
      "v_mfma_f32_16x16x32_bf16 v[0:3], v[144:147], a[64:67], v[0:3]\n"
      "ds_read_b128 a[104:107], v243 offset:26624\n"
      "ds_read_b128 a[120:123], v243 offset:30720\n"
      "v_mfma_f32_16x16x32_bf16 v[0:3], v[148:151], a[80:83], v[0:3]\n"
      "ds_read_b128 a[12:15], v243 offset:3072\n"
      "ds_read_b128 a[28:31], v243 offset:7168\n"
      "v_mfma_f32_16x16x32_bf16 v[0:3], v[152:155], a[96:99], v[0:3]\n"
      "ds_read_b128 a[44:47], v243 offset:11264\n"
      "ds_read_b128 a[60:63], v243 offset:15360\n"
      "v_mfma_f32_16x16x32_bf16 v[0:3], v[156:159], a[112:115], v[0:3]\n"
      "s_waitcnt lgkmcnt(12)\n"
      "s_add_u32 s97, s97, s99\n"
      "ds_read_b128 a[76:79], v243 offset:19456\n"
      "ds_read_b128 a[92:95], v243 offset:23552\n"
      "v_mfma_f32_16x16x32_bf16 v[4:7], v[128:131], a[4:7], 0\n"
      "buffer_load_dwordx4 v[68:71], v234, s[40:43], s97 offen\n"
      "v_mfma_f32_16x16x32_bf16 v[4:7], v[132:135], a[20:23], v[4:7]\n"
      "ds_read_b128 a[108:111], v243 offset:27648\n"
      "ds_read_b128 a[124:127], v243 offset:31744\n"
      "v_mfma_f32_16x16x32_bf16 v[4:7], v[136:139], a[36:39], v[4:7]\n"
      "ds_read_b64_tr_b16 v[160:161], v218 offset:0\n"
      "ds_read_b64_tr_b16 v[162:163], v218 offset:2048\n"
      "v_mfma_f32_16x16x32_bf16 v[4:7], v[140:143], a[52:55], v[4:7]\n"
      "ds_read_b64_tr_b16 v[164:165], v218 offset:4096\n"
      "ds_read_b64_tr_b16 v[166:167], v218 offset:6144\n"
      "v_mfma_f32_16x16x32_bf16 v[4:7], v[144:147], a[68:71], v[4:7]\n"
      "ds_read_b64_tr_b16 v[168:169], v218 offset:8192\n"
      "ds_read_b64_tr_b16 v[170:171], v218 offset:10240\n"
      "v_mfma_f32_16x16x32_bf16 v[4:7], v[148:151], a[84:87], v[4:7]\n"
      "ds_read_b64_tr_b16 v[172:173], v218 offset:12288\n"
      "ds_read_b64_tr_b16 v[174:175], v218 offset:14336\n"
      "v_mfma_f32_16x16x32_bf16 v[4:7], v[152:155], a[100:103], v[4:7]\n"
      "s_add_u32 s97, s97, s66\n"
      "ds_read_b64_tr_b16 v[176:177], v218 offset:16384\n"
      "ds_read_b64_tr_b16 v[178:179], v218 offset:18432\n"
      "v_mfma_f32_16x16x32_bf16 v[4:7], v[156:159], a[116:119], v[4:7]\n"
      "s_waitcnt lgkmcnt(15)\n"
      "buffer_load_dwordx4 v[72:75], v234, s[44:47], s97 offen\n"
      "v_mfma_f32_16x16x32_bf16 v[8:11], v[128:131], a[8:11], 0\n"
      "ds_read_b64_tr_b16 v[180:181], v218 offset:20480\n"
      "ds_read_b64_tr_b16 v[182:183], v218 offset:22528\n"
      "v_mfma_f32_16x16x32_bf16 v[8:11], v[132:135], a[24:27], v[8:11]\n"
      "ds_read_b64_tr_b16 v[184:185], v218 offset:24576\n"
      "ds_read_b64_tr_b16 v[186:187], v218 offset:26624\n"
      "v_mfma_f32_16x16x32_bf16 v[8:11], v[136:139], a[40:43], v[8:11]\n"
      "ds_read_b64_tr_b16 v[188:189], v218 offset:28672\n"
      "v_mfma_f32_16x16x32_bf16 v[8:11], v[140:143], a[56:59], v[8:11]\n"
      "ds_read_b64_tr_b16 v[190:191], v218 offset:30720\n"
      "v_mfma_f32_16x16x32_bf16 v[8:11], v[144:147], a[72:75], v[8:11]\n"
      "ds_read_b128 a[128:131], v243 offset:32768\n"
      "v_mfma_f32_16x16x32_bf16 v[8:11], v[148:151], a[88:91], v[8:11]\n"
      "s_add_u32 s97, s97, s99\n"
      "ds_read_b128 a[144:147], v243 offset:36864\n"
      "v_mfma_f32_16x16x32_bf16 v[8:11], v[152:155], a[104:107], v[8:11]\n"
      "buffer_load_dwordx4 v[76:79], v234, s[44:47], s97 offen\n"
      "v_mfma_f32_16x16x32_bf16 v[8:11], v[156:159], a[120:123], v[8:11]\n"
      "s_waitcnt lgkmcnt(15)\n"
      "ds_read_b128 a[160:163], v243 offset:40960\n"
      "v_mfma_f32_16x16x32_bf16 v[12:15], v[128:131], a[12:15], 0\n"
      "ds_read_b128 a[176:179], v243 offset:45056\n"
      "v_mfma_f32_16x16x32_bf16 v[12:15], v[132:135], a[28:31], v[12:15]\n"
      "ds_read_b128 a[192:195], v243 offset:49152\n"
      "v_mfma_f32_16x16x32_bf16 v[12:15], v[136:139], a[44:47], v[12:15]\n"
      "ds_read_b128 a[208:211], v243 offset:53248\n"
      "v_mfma_f32_16x16x32_bf16 v[12:15], v[140:143], a[60:63], v[12:15]\n"
      "ds_read_b128 a[224:227], v243 offset:57344\n"
      "v_mfma_f32_16x16x32_bf16 v[12:15], v[144:147], a[76:79], v[12:15]\n"
      "ds_read_b128 a[240:243], v243 offset:61440\n"
      "v_mfma_f32_16x16x32_bf16 v[12:15], v[148:151], a[92:95], v[12:15]\n"
      "v_mfma_f32_16x16x32_bf16 v[12:15], v[152:155], a[108:111], v[12:15]\n"
      "v_mfma_f32_16x16x32_bf16 v[12:15], v[156:159], a[124:127], v[12:15]\n"
      "s_waitcnt vmcnt(12) lgkmcnt(0)\n"
      "s_barrier\n"
      "v_mfma_f32_16x16x32_bf16 v[0:3], v[160:163], a[128:131], v[0:3]\n"
      "s_add_u32 m0, s81, 0\n"
      "s_add_u32 s95, s91, 0\n"
      "ds_read_b128 a[132:135], v243 offset:33792\n"
      "ds_read_b128 a[148:151], v243 offset:37888\n"
      "v_mfma_f32_16x16x32_bf16 v[0:3], v[164:167], a[144:147], v[0:3]\n"
      "buffer_load_dwordx4 v230, s[68:71], s95 offen offset:0 lds\n"
      "v_mfma_f32_16x16x32_bf16 v[0:3], v[168:171], a[160:163], v[0:3]\n"
      "ds_read_b128 a[164:167], v243 offset:41984\n"
      "ds_read_b128 a[180:183], v243 offset:46080\n"
      "v_mfma_f32_16x16x32_bf16 v[0:3], v[172:175], a[176:179], v[0:3]\n"
      "ds_read_b128 a[196:199], v243 offset:50176\n"
      "ds_read_b128 a[212:215], v243 offset:54272\n"
      "v_mfma_f32_16x16x32_bf16 v[0:3], v[176:179], a[192:195], v[0:3]\n"
      "buffer_load_dwordx4 v231, s[68:71], s95 offen offset:1024 lds\n"
      "v_mfma_f32_16x16x32_bf16 v[0:3], v[180:183], a[208:211], v[0:3]\n"
      "ds_read_b128 a[228:231], v243 offset:58368\n"
      "ds_read_b128 a[244:247], v243 offset:62464\n"
      "v_mfma_f32_16x16x32_bf16 v[0:3], v[184:187], a[224:227], v[0:3]\n"
      "ds_read_b128 a[136:139], v243 offset:34816\n"
      "ds_read_b128 a[152:155], v243 offset:38912\n"
      "v_mfma_f32_16x16x32_bf16 v[0:3], v[188:191], a[240:243], v[0:3]\n"
      "s_waitcnt lgkmcnt(2)\n"
      "ds_read_b128 a[168:171], v243 offset:43008\n"
      "ds_read_b128 a[184:187], v243 offset:47104\n"
      "v_mfma_f32_16x16x32_bf16 v[4:7], v[160:163], a[132:135], v[4:7]\n"
      "buffer_load_dwordx4 v232, s[68:71], s95 offen offset:2048 lds\n"
      "v_mfma_f32_16x16x32_bf16 v[4:7], v[164:167], a[148:151], v[4:7]\n"
      "ds_read_b128 a[200:203], v243 offset:51200\n"
      "ds_read_b128 a[216:219], v243 offset:55296\n"
      "v_mfma_f32_16x16x32_bf16 v[4:7], v[168:171], a[164:167], v[4:7]\n"
      "s_add_u32 s96, s95, 28672\n"
      "ds_read_b128 a[232:235], v243 offset:59392\n"
      "ds_read_b128 a[248:251], v243 offset:63488\n"
      "v_mfma_f32_16x16x32_bf16 v[4:7], v[172:175], a[180:183], v[4:7]\n"
      "buffer_load_dwordx4 v233, s[68:71], s95 offen offset:3072 lds\n"
      "v_mfma_f32_16x16x32_bf16 v[4:7], v[176:179], a[196:199], v[4:7]\n"
      "ds_read_b128 a[140:143], v243 offset:35840\n"
      "ds_read_b128 a[156:159], v243 offset:39936\n"
      "v_mfma_f32_16x16x32_bf16 v[4:7], v[180:183], a[212:215], v[4:7]\n"
      "s_add_u32 m0, s81, 4096\n"
      "ds_read_b128 a[172:175], v243 offset:44032\n"
      "ds_read_b128 a[188:191], v243 offset:48128\n"
      "v_mfma_f32_16x16x32_bf16 v[4:7], v[184:187], a[228:231], v[4:7]\n"
      "buffer_load_dwordx4 v230, s[68:71], s96 offen offset:0 lds\n"
      "v_mfma_f32_16x16x32_bf16 v[4:7], v[188:191], a[244:247], v[4:7]\n"
      "s_waitcnt lgkmcnt(4)\n"
      "ds_read_b128 a[204:207], v243 offset:52224\n"
      "ds_read_b128 a[220:223], v243 offset:56320\n"
      "v_mfma_f32_16x16x32_bf16 v[8:11], v[160:163], a[136:139], v[8:11]\n"
      "ds_read_b128 a[236:239], v243 offset:60416\n"
      "ds_read_b128 a[252:255], v243 offset:64512\n"
      "v_mfma_f32_16x16x32_bf16 v[8:11], v[164:167], a[152:155], v[8:11]\n"
      "buffer_load_dwordx4 v231, s[68:71], s96 offen offset:1024 lds\n"
      "v_mfma_f32_16x16x32_bf16 v[8:11], v[168:171], a[168:171], v[8:11]\n"
      "ds_read_b64_tr_b16 v[128:129], v219 offset:0\n"
      "ds_read_b64_tr_b16 v[130:131], v219 offset:2048\n"
      "v_mfma_f32_16x16x32_bf16 v[8:11], v[172:175], a[184:187], v[8:11]\n"
      "ds_read_b64_tr_b16 v[132:133], v219 offset:4096\n"
      "ds_read_b64_tr_b16 v[134:135], v219 offset:6144\n"
      "v_mfma_f32_16x16x32_bf16 v[8:11], v[176:179], a[200:203], v[8:11]\n"
      "buffer_load_dwordx4 v232, s[68:71], s96 offen offset:2048 lds\n"
      "v_mfma_f32_16x16x32_bf16 v[8:11], v[180:183], a[216:219], v[8:11]\n"
      "ds_read_b64_tr_b16 v[136:137], v219 offset:8192\n"
      "ds_read_b64_tr_b16 v[138:139], v219 offset:10240\n"
      "v_mfma_f32_16x16x32_bf16 v[8:11], v[184:187], a[232:235], v[8:11]\n"
      "ds_read_b64_tr_b16 v[140:141], v219 offset:12288\n"
      "ds_read_b64_tr_b16 v[142:143], v219 offset:14336\n"
      "v_mfma_f32_16x16x32_bf16 v[8:11], v[188:191], a[248:251], v[8:11]\n"
      "s_waitcnt lgkmcnt(8)\n"
      "ds_read_b64_tr_b16 v[144:145], v219 offset:16384\n"
      "ds_read_b64_tr_b16 v[146:147], v219 offset:18432\n"
      "v_mfma_f32_16x16x32_bf16 v[12:15], v[160:163], a[140:143], v[12:15]\n"
      "buffer_load_dwordx4 v233, s[68:71], s96 offen offset:3072 lds\n"
      "v_mfma_f32_16x16x32_bf16 v[12:15], v[164:167], a[156:159], v[12:15]\n"
      "ds_read_b64_tr_b16 v[148:149], v219 offset:20480\n"
      "ds_read_b64_tr_b16 v[150:151], v219 offset:22528\n"
      "v_mfma_f32_16x16x32_bf16 v[12:15], v[168:171], a[172:175], v[12:15]\n"
      "ds_read_b64_tr_b16 v[152:153], v219 offset:24576\n"
      "ds_read_b64_tr_b16 v[154:155], v219 offset:26624\n"
      "v_mfma_f32_16x16x32_bf16 v[12:15], v[172:175], a[188:191], v[12:15]\n"
      "ds_read_b64_tr_b16 v[156:157], v219 offset:28672\n"
      "ds_read_b64_tr_b16 v[158:159], v219 offset:30720\n"
      "v_mfma_f32_16x16x32_bf16 v[12:15], v[176:179], a[204:207], v[12:15]\n"
      "s_cmp_lt_u32 s80, 3\n"
      "s_cselect_b32 s88, 1.0, 0\n"
      "s_cselect_b32 s86, 0, 1073741824\n"
      "s_cselect_b32 s87, 1024, 0\n"
      "v_mfma_f32_16x16x32_bf16 v[12:15], v[180:183], a[220:223], v[12:15]\n"
      "s_lshl_b32 s89, s93, 14\n"
      "v_mfma_f32_16x16x32_bf16 v[12:15], v[184:187], a[236:239], v[12:15]\n"
      "v_mfma_f32_16x16x32_bf16 v[12:15], v[188:191], a[252:255], v[12:15]\n"
      "s_waitcnt vmcnt(12) lgkmcnt(0)\n"
      "s_barrier\n"
      "v_mfma_f32_16x16x32_bf16 v[16:19], v[128:131], a[0:3], 0\n"
      "s_add_u32 m0, s81, 65536\n"
      "s_add_u32 s95, s91, 229376\n"
      "ds_read_b64_tr_b16 v[160:161], v218 offset:32768\n"
      "v_mfma_f32_16x16x32_bf16 v[16:19], v[132:135], a[16:19], v[16:19]\n"
      "buffer_load_dwordx4 v230, s[68:71], s95 offen offset:0 lds\n"
      "v_mfma_f32_16x16x32_bf16 v[16:19], v[136:139], a[32:35], v[16:19]\n"
      "s_add_u32 s84, s82, s83\n"
      "ds_read_b64_tr_b16 v[162:163], v218 offset:34816\n"
      "v_mfma_f32_16x16x32_bf16 v[16:19], v[140:143], a[48:51], v[16:19]\n"
      "s_add_u32 s85, s84, 229376\n"
      "ds_read_b64_tr_b16 v[164:165], v218 offset:36864\n"
      "v_mfma_f32_16x16x32_bf16 v[16:19], v[144:147], a[64:67], v[16:19]\n"
      "buffer_load_dwordx4 v231, s[68:71], s95 offen offset:1024 lds\n"
      "v_mfma_f32_16x16x32_bf16 v[16:19], v[148:151], a[80:83], v[16:19]\n"
      "s_add_u32 s89, s89, 3584\n"
      "ds_read_b64_tr_b16 v[166:167], v218 offset:38912\n"
      "v_cvt_pk_bf16_f32 v0, v0, v1\n"
      "v_mfma_f32_16x16x32_bf16 v[16:19], v[152:155], a[96:99], v[16:19]\n"
      "ds_read_b64_tr_b16 v[168:169], v218 offset:40960\n"
      "v_cvt_pk_bf16_f32 v1, v2, v3\n"
      "v_mfma_f32_16x16x32_bf16 v[16:19], v[156:159], a[112:115], v[16:19]\n"
      "buffer_load_dwordx4 v232, s[68:71], s95 offen offset:2048 lds\n"
      "v_mfma_f32_16x16x32_bf16 v[20:23], v[128:131], a[4:7], 0\n"
      "ds_read_b64_tr_b16 v[170:171], v218 offset:43008\n"
      "v_cvt_pk_bf16_f32 v2, v4, v5\n"
      "v_mfma_f32_16x16x32_bf16 v[20:23], v[132:135], a[20:23], v[20:23]\n"
      "s_add_u32 s96, s95, 28672\n"
      "ds_read_b64_tr_b16 v[172:173], v218 offset:45056\n"
      "v_cvt_pk_bf16_f32 v3, v6, v7\n"
      "v_mfma_f32_16x16x32_bf16 v[20:23], v[136:139], a[36:39], v[20:23]\n"
      "buffer_load_dwordx4 v233, s[68:71], s95 offen offset:3072 lds\n"
      "v_mfma_f32_16x16x32_bf16 v[20:23], v[140:143], a[52:55], v[20:23]\n"
      "ds_read_b64_tr_b16 v[174:175], v218 offset:47104\n"
      "v_cvt_pk_bf16_f32 v8, v8, v9\n"
      "v_mfma_f32_16x16x32_bf16 v[20:23], v[144:147], a[68:71], v[20:23]\n"
      "s_add_u32 m0, s81, 69632\n"
      "ds_read_b64_tr_b16 v[176:177], v218 offset:49152\n"
      "v_cvt_pk_bf16_f32 v9, v10, v11\n"
      "v_mfma_f32_16x16x32_bf16 v[20:23], v[148:151], a[84:87], v[20:23]\n"
      "buffer_load_dwordx4 v230, s[68:71], s96 offen offset:0 lds\n"
      "v_mfma_f32_16x16x32_bf16 v[20:23], v[152:155], a[100:103], v[20:23]\n"
      "ds_read_b64_tr_b16 v[178:179], v218 offset:51200\n"
      "v_cvt_pk_bf16_f32 v10, v12, v13\n"
      "v_mfma_f32_16x16x32_bf16 v[20:23], v[156:159], a[116:119], v[20:23]\n"
      "s_add_u32 s91, s91, 128\n"
      "ds_read_b64_tr_b16 v[180:181], v218 offset:53248\n"
      "v_cvt_pk_bf16_f32 v11, v14, v15\n"
      "v_mfma_f32_16x16x32_bf16 v[24:27], v[128:131], a[8:11], 0\n"
      "buffer_load_dwordx4 v231, s[68:71], s96 offen offset:1024 lds\n"
      "v_mfma_f32_16x16x32_bf16 v[24:27], v[132:135], a[24:27], v[24:27]\n"
      "ds_read_b64_tr_b16 v[182:183], v218 offset:55296\n"
      "v_and_b32 v244, 12, v254\n"
      "v_mfma_f32_16x16x32_bf16 v[24:27], v[136:139], a[40:43], v[24:27]\n"
      "s_cmp_ge_u32 s91, 896\n"
      "s_cselect_b32 s91, 0, s91\n"
      "ds_read_b64_tr_b16 v[184:185], v218 offset:57344\n"
      "v_mfma_f32_16x16x32_bf16 v[24:27], v[140:143], a[56:59], v[24:27]\n"
      "buffer_load_dwordx4 v232, s[68:71], s96 offen offset:2048 lds\n"
      "v_mfma_f32_16x16x32_bf16 v[24:27], v[144:147], a[72:75], v[24:27]\n"
      "ds_read_b64_tr_b16 v[186:187], v218 offset:59392\n"
      "v_lshrrev_b32 v245, 5, v254\n"
      "v_mfma_f32_16x16x32_bf16 v[24:27], v[148:151], a[88:91], v[24:27]\n"
      "ds_read_b64_tr_b16 v[188:189], v218 offset:61440\n"
      "v_lshl_add_u32 v244, v245, 4, v244\n"
      "v_mfma_f32_16x16x32_bf16 v[24:27], v[152:155], a[104:107], v[24:27]\n"
      "buffer_load_dwordx4 v233, s[68:71], s96 offen offset:3072 lds\n"
      "v_mfma_f32_16x16x32_bf16 v[24:27], v[156:159], a[120:123], v[24:27]\n"
      "ds_read_b64_tr_b16 v[190:191], v218 offset:63488\n"
      "v_lshl_add_u32 v244, s80, 6, v244\n"
      "v_mfma_f32_16x16x32_bf16 v[28:31], v[128:131], a[12:15], 0\n"
      "buffer_store_dwordx4 v[0:3], v238, s[72:75], s84 offen sc1\n"
      "v_mfma_f32_16x16x32_bf16 v[28:31], v[132:135], a[28:31], v[28:31]\n"
      "v_lshlrev_b32 v225, 1, v244\n"
      "v_mfma_f32_16x16x32_bf16 v[28:31], v[136:139], a[44:47], v[28:31]\n"
      "v_lshlrev_b32 v223, 2, v244\n"
      "v_mfma_f32_16x16x32_bf16 v[28:31], v[140:143], a[60:63], v[28:31]\n"
      "v_add_u32 v224, s87, v223\n"
      "v_mfma_f32_16x16x32_bf16 v[28:31], v[144:147], a[76:79], v[28:31]\n"
      "v_add_u32 v227, s86, v224\n"
      "v_mfma_f32_16x16x32_bf16 v[28:31], v[148:151], a[92:95], v[28:31]\n"
      "v_add_u32 v226, s86, v225\n"
      "v_mfma_f32_16x16x32_bf16 v[28:31], v[152:155], a[108:111], v[28:31]\n"
      "v_mfma_f32_16x16x32_bf16 v[28:31], v[156:159], a[124:127], v[28:31]\n"
      "s_waitcnt vmcnt(9) lgkmcnt(0)\n"
      "s_barrier\n"
      "v_mfma_f32_16x16x32_bf16 v[16:19], v[160:163], a[128:131], v[16:19]\n"
      "s_add_u32 m0, s81, 32768\n"
      "s_add_u32 s95, s91, 0\n"
      "ds_read_b64_tr_b16 v[128:129], v218 offset:0\n"
      "v_mfma_f32_16x16x32_bf16 v[16:19], v[164:167], a[144:147], v[16:19]\n"
      "buffer_load_dwordx4 v230, s[68:71], s95 offen offset:0 lds\n"
      "v_mfma_f32_16x16x32_bf16 v[16:19], v[168:171], a[160:163], v[16:19]\n"
      "s_add_u32 s83, s83, 1024\n"
      "ds_read_b64_tr_b16 v[130:131], v218 offset:2048\n"
      "v_mfma_f32_16x16x32_bf16 v[16:19], v[172:175], a[176:179], v[16:19]\n"
      "s_cmp_ge_u32 s83, 7168\n"
      "s_cselect_b32 s83, 0, s83\n"
      "ds_read_b64_tr_b16 v[132:133], v218 offset:4096\n"
      "v_mfma_f32_16x16x32_bf16 v[16:19], v[176:179], a[192:195], v[16:19]\n"
      "buffer_load_dwordx4 v231, s[68:71], s95 offen offset:1024 lds\n"
      "v_mfma_f32_16x16x32_bf16 v[16:19], v[180:183], a[208:211], v[16:19]\n"
      "s_lshl_b32 s90, s92, 6\n"
      "ds_read_b64_tr_b16 v[134:135], v218 offset:6144\n"
      "v_mfma_f32_16x16x32_bf16 v[16:19], v[184:187], a[224:227], v[16:19]\n"
      "s_lshl_b32 s87, s93, 2\n"
      "ds_read_b64_tr_b16 v[136:137], v218 offset:8192\n"
      "v_mfma_f32_16x16x32_bf16 v[16:19], v[188:191], a[240:243], v[16:19]\n"
      "buffer_load_dwordx4 v232, s[68:71], s95 offen offset:2048 lds\n"
      "v_mfma_f32_16x16x32_bf16 v[20:23], v[160:163], a[132:135], v[20:23]\n"
      "s_add_u32 s90, s90, s87\n"
      "ds_read_b64_tr_b16 v[138:139], v218 offset:10240\n"
      "v_mfma_f32_16x16x32_bf16 v[20:23], v[164:167], a[148:151], v[20:23]\n"
      "s_add_u32 s96, s95, 28672\n"
      "ds_read_b64_tr_b16 v[140:141], v218 offset:12288\n"
      "v_mfma_f32_16x16x32_bf16 v[20:23], v[168:171], a[164:167], v[20:23]\n"
      "buffer_load_dwordx4 v233, s[68:71], s95 offen offset:3072 lds\n"
      "v_mfma_f32_16x16x32_bf16 v[20:23], v[172:175], a[180:183], v[20:23]\n"
      "s_and_b32 s87, s87, 16\n"
      "ds_read_b64_tr_b16 v[142:143], v218 offset:14336\n"
      "v_mfma_f32_16x16x32_bf16 v[20:23], v[176:179], a[196:199], v[20:23]\n"
      "s_add_u32 m0, s81, 36864\n"
      "ds_read_b64_tr_b16 v[144:145], v218 offset:16384\n"
      "v_mfma_f32_16x16x32_bf16 v[20:23], v[180:183], a[212:215], v[20:23]\n"
      "buffer_load_dwordx4 v230, s[68:71], s96 offen offset:0 lds\n"
      "v_mfma_f32_16x16x32_bf16 v[20:23], v[184:187], a[228:231], v[20:23]\n"
      "s_add_u32 s90, s90, s87\n"
      "ds_read_b64_tr_b16 v[146:147], v218 offset:18432\n"
      "v_mfma_f32_16x16x32_bf16 v[20:23], v[188:191], a[244:247], v[20:23]\n"
      "ds_read_b64_tr_b16 v[148:149], v218 offset:20480\n"
      "v_and_b32 v245, 19, v254\n"
      "v_mfma_f32_16x16x32_bf16 v[24:27], v[160:163], a[136:139], v[24:27]\n"
      "buffer_load_dwordx4 v231, s[68:71], s96 offen offset:1024 lds\n"
      "v_mfma_f32_16x16x32_bf16 v[24:27], v[164:167], a[152:155], v[24:27]\n"
      "ds_read_b64_tr_b16 v[150:151], v218 offset:22528\n"
      "v_add_u32 v220, s90, v245\n"
      "v_mfma_f32_16x16x32_bf16 v[24:27], v[168:171], a[168:171], v[24:27]\n"
      "ds_read_b64_tr_b16 v[152:153], v218 offset:24576\n"
      "v_mul_u32_u24 v246, 896, v220\n"
      "v_mfma_f32_16x16x32_bf16 v[24:27], v[172:175], a[184:187], v[24:27]\n"
      "buffer_load_dwordx4 v232, s[68:71], s96 offen offset:2048 lds\n"
      "v_mfma_f32_16x16x32_bf16 v[24:27], v[176:179], a[200:203], v[24:27]\n"
      "ds_read_b64_tr_b16 v[154:155], v218 offset:26624\n"
      "v_add_u32 v221, v246, v225\n"
      "v_mfma_f32_16x16x32_bf16 v[24:27], v[180:183], a[216:219], v[24:27]\n"
      "ds_read_b64_tr_b16 v[156:157], v218 offset:28672\n"
      "v_add_u32 v222, s86, v221\n"
      "v_mfma_f32_16x16x32_bf16 v[24:27], v[184:187], a[232:235], v[24:27]\n"
      "buffer_load_dwordx4 v233, s[68:71], s96 offen offset:3072 lds\n"
      "v_mfma_f32_16x16x32_bf16 v[24:27], v[188:191], a[248:251], v[24:27]\n"
      "ds_read_b64_tr_b16 v[158:159], v218 offset:30720\n"
      "v_sub_u32 v220, 0x7ff, v220\n"
      "v_mfma_f32_16x16x32_bf16 v[28:31], v[160:163], a[140:143], v[28:31]\n"
      "buffer_store_dwordx4 v[8:11], v238, s[72:75], s85 offen sc1\n"
      "v_mfma_f32_16x16x32_bf16 v[28:31], v[164:167], a[156:159], v[28:31]\n"
      "s_add_u32 s58, s90, s80\n"
      "v_mfma_f32_16x16x32_bf16 v[28:31], v[168:171], a[172:175], v[28:31]\n"
      "s_mul_i32 s58, s58, 896\n"
      "v_mfma_f32_16x16x32_bf16 v[28:31], v[172:175], a[188:191], v[28:31]\n"
      "s_add_u32 s90, s89, 3584\n"
      "v_mfma_f32_16x16x32_bf16 v[28:31], v[176:179], a[204:207], v[28:31]\n"
      "s_mul_i32 s87, s52, 57344\n"
      "v_mfma_f32_16x16x32_bf16 v[28:31], v[180:183], a[220:223], v[28:31]\n"
      "s_add_u32 s87, s87, 4194304\n"
      "v_mfma_f32_16x16x32_bf16 v[28:31], v[184:187], a[236:239], v[28:31]\n"
      "v_mfma_f32_16x16x32_bf16 v[28:31], v[188:191], a[252:255], v[28:31]\n"
      "s_waitcnt vmcnt(10) lgkmcnt(0)\n"
      "s_barrier\n"
      "v_mfma_f32_16x16x32_bf16 v[0:3], v[128:131], a[0:3], 0\n"
      "s_add_u32 m0, s81, 0\n"
      "s_add_u32 s95, s91, 229376\n"
      "ds_read_b64_tr_b16 v[160:161], v219 offset:0\n"
      "v_mfma_f32_16x16x32_bf16 v[0:3], v[132:135], a[16:19], v[0:3]\n"
      "buffer_load_dwordx4 v230, s[68:71], s95 offen offset:0 lds\n"
      "v_mfma_f32_16x16x32_bf16 v[0:3], v[136:139], a[32:35], v[0:3]\n"
      "s_add_u32 s84, s82, s83\n"
      "ds_read_b64_tr_b16 v[162:163], v219 offset:2048\n"
      "v_lshl_add_u32 v228, s80, 10, v242\n"
      "v_mfma_f32_16x16x32_bf16 v[0:3], v[140:143], a[48:51], v[0:3]\n"
      "s_add_u32 s85, s84, 229376\n"
      "ds_read_b64_tr_b16 v[164:165], v219 offset:4096\n"
      "v_add_u32 v228, s87, v228\n"
      "v_mfma_f32_16x16x32_bf16 v[0:3], v[144:147], a[64:67], v[0:3]\n"
      "buffer_load_dwordx4 v231, s[68:71], s95 offen offset:1024 lds\n"
      "v_mfma_f32_16x16x32_bf16 v[0:3], v[148:151], a[80:83], v[0:3]\n"
      "ds_read_b64_tr_b16 v[166:167], v219 offset:6144\n"
      "v_cvt_pk_bf16_f32 v16, v16, v17\n"
      "v_mfma_f32_16x16x32_bf16 v[0:3], v[152:155], a[96:99], v[0:3]\n"
      "ds_read_b64_tr_b16 v[168:169], v219 offset:8192\n"
      "v_cvt_pk_bf16_f32 v17, v18, v19\n"
      "v_mfma_f32_16x16x32_bf16 v[0:3], v[156:159], a[112:115], v[0:3]\n"
      "buffer_load_dwordx4 v232, s[68:71], s95 offen offset:2048 lds\n"
      "v_mfma_f32_16x16x32_bf16 v[4:7], v[128:131], a[4:7], 0\n"
      "ds_read_b64_tr_b16 v[170:171], v219 offset:10240\n"
      "v_cvt_pk_bf16_f32 v18, v20, v21\n"
      "v_mfma_f32_16x16x32_bf16 v[4:7], v[132:135], a[20:23], v[4:7]\n"
      "s_add_u32 s96, s95, 28672\n"
      "ds_read_b64_tr_b16 v[172:173], v219 offset:12288\n"
      "v_cvt_pk_bf16_f32 v19, v22, v23\n"
      "v_mfma_f32_16x16x32_bf16 v[4:7], v[136:139], a[36:39], v[4:7]\n"
      "buffer_load_dwordx4 v233, s[68:71], s95 offen offset:3072 lds\n"
      "v_mfma_f32_16x16x32_bf16 v[4:7], v[140:143], a[52:55], v[4:7]\n"
      "ds_read_b64_tr_b16 v[174:175], v219 offset:14336\n"
      "v_cvt_pk_bf16_f32 v24, v24, v25\n"
      "v_mfma_f32_16x16x32_bf16 v[4:7], v[144:147], a[68:71], v[4:7]\n"
      "s_add_u32 m0, s81, 4096\n"
      "ds_read_b64_tr_b16 v[176:177], v219 offset:16384\n"
      "v_cvt_pk_bf16_f32 v25, v26, v27\n"
      "v_mfma_f32_16x16x32_bf16 v[4:7], v[148:151], a[84:87], v[4:7]\n"
      "buffer_load_dwordx4 v230, s[68:71], s96 offen offset:0 lds\n"
      "v_mfma_f32_16x16x32_bf16 v[4:7], v[152:155], a[100:103], v[4:7]\n"
      "ds_read_b64_tr_b16 v[178:179], v219 offset:18432\n"
      "v_cvt_pk_bf16_f32 v26, v28, v29\n"
      "v_mfma_f32_16x16x32_bf16 v[4:7], v[156:159], a[116:119], v[4:7]\n"
      "s_add_u32 s91, s91, 128\n"
      "ds_read_b64_tr_b16 v[180:181], v219 offset:20480\n"
      "v_cvt_pk_bf16_f32 v27, v30, v31\n"
      "v_mfma_f32_16x16x32_bf16 v[8:11], v[128:131], a[8:11], 0\n"
      "buffer_load_dwordx4 v231, s[68:71], s96 offen offset:1024 lds\n"
      "v_mfma_f32_16x16x32_bf16 v[8:11], v[132:135], a[24:27], v[8:11]\n"
      "ds_read_b64_tr_b16 v[182:183], v219 offset:22528\n"
      "v_add_u32 v229, s86, v228\n"
      "v_mfma_f32_16x16x32_bf16 v[8:11], v[136:139], a[40:43], v[8:11]\n"
      "s_cmp_ge_u32 s91, 896\n"
      "s_cselect_b32 s91, 0, s91\n"
      "ds_read_b64_tr_b16 v[184:185], v219 offset:24576\n"
      "v_mfma_f32_16x16x32_bf16 v[8:11], v[140:143], a[56:59], v[8:11]\n"
      "buffer_load_dwordx4 v232, s[68:71], s96 offen offset:2048 lds\n"
      "v_mfma_f32_16x16x32_bf16 v[8:11], v[144:147], a[72:75], v[8:11]\n"
      "ds_read_b64_tr_b16 v[186:187], v219 offset:26624\n"
      "v_add_u32 v229, 4096, v229\n"
      "v_mfma_f32_16x16x32_bf16 v[8:11], v[148:151], a[88:91], v[8:11]\n"
      "ds_read_b64_tr_b16 v[188:189], v219 offset:28672\n"
      "v_and_b32 v245, 3, v254\n"
      "v_mfma_f32_16x16x32_bf16 v[8:11], v[152:155], a[104:107], v[8:11]\n"
      "buffer_load_dwordx4 v233, s[68:71], s96 offen offset:3072 lds\n"
      "v_mfma_f32_16x16x32_bf16 v[8:11], v[156:159], a[120:123], v[8:11]\n"
      "ds_read_b64_tr_b16 v[190:191], v219 offset:30720\n"
      "v_lshlrev_b32 v245, 5, v245\n"
      "v_mfma_f32_16x16x32_bf16 v[12:15], v[128:131], a[12:15], 0\n"
      "buffer_store_dwordx4 v[16:19], v238, s[72:75], s84 offen sc1\n"
      "v_mfma_f32_16x16x32_bf16 v[12:15], v[132:135], a[28:31], v[12:15]\n"
      "v_and_or_b32 v245, v254, 16, v245\n"
      "v_mfma_f32_16x16x32_bf16 v[12:15], v[136:139], a[44:47], v[12:15]\n"
      "v_lshl_add_u32 v243, v245, 6, v225\n"
      "v_mfma_f32_16x16x32_bf16 v[12:15], v[140:143], a[60:63], v[12:15]\n"
      "v_add_u32 v243, 73728, v243\n"
      "v_mfma_f32_16x16x32_bf16 v[12:15], v[144:147], a[76:79], v[12:15]\n"
      "v_add_u32 v241, 0, v245\n"
      "v_mfma_f32_16x16x32_bf16 v[12:15], v[148:151], a[92:95], v[12:15]\n"
      "v_lshl_add_u32 v237, s80, 2, v241\n"
      "v_mfma_f32_16x16x32_bf16 v[12:15], v[152:155], a[108:111], v[12:15]\n"
      "v_mfma_f32_16x16x32_bf16 v[12:15], v[156:159], a[124:127], v[12:15]\n"
      "s_waitcnt vmcnt(10) lgkmcnt(0)\n"
      "s_barrier\n"
      "v_mfma_f32_16x16x32_bf16 v[0:3], v[160:163], a[128:131], v[0:3]\n"
      "s_add_u32 m0, s81, 65536\n"
      "s_add_u32 s95, s91, 0\n"
      "ds_read_b64_tr_b16 v[128:129], v218 offset:32768\n"
      "v_mfma_f32_16x16x32_bf16 v[0:3], v[164:167], a[144:147], v[0:3]\n"
      "buffer_load_dwordx4 v230, s[68:71], s95 offen offset:0 lds\n"
      "v_mfma_f32_16x16x32_bf16 v[0:3], v[168:171], a[160:163], v[0:3]\n"
      "s_add_u32 s83, s83, 1024\n"
      "ds_read_b64_tr_b16 v[130:131], v218 offset:34816\n"
      "v_lshl_add_u32 v245, s80, 6, v254\n"
      "v_mfma_f32_16x16x32_bf16 v[0:3], v[172:175], a[176:179], v[0:3]\n"
      "s_cmp_ge_u32 s83, 7168\n"
      "s_cselect_b32 s83, 0, s83\n"
      "ds_read_b64_tr_b16 v[132:133], v218 offset:36864\n"
      "v_mfma_f32_16x16x32_bf16 v[0:3], v[176:179], a[192:195], v[0:3]\n"
      "buffer_load_dwordx4 v231, s[68:71], s95 offen offset:1024 lds\n"
      "v_mfma_f32_16x16x32_bf16 v[0:3], v[180:183], a[208:211], v[0:3]\n"
      "ds_read_b64_tr_b16 v[134:135], v218 offset:38912\n"
      "v_lshrrev_b32 v246, 3, v245\n"
      "v_mfma_f32_16x16x32_bf16 v[0:3], v[184:187], a[224:227], v[0:3]\n"
      "ds_read_b64_tr_b16 v[136:137], v218 offset:40960\n"
      "v_min_u32 v246, 1, v246\n"
      "v_mfma_f32_16x16x32_bf16 v[0:3], v[188:191], a[240:243], v[0:3]\n"
      "buffer_load_dwordx4 v232, s[68:71], s95 offen offset:2048 lds\n"
      "v_mfma_f32_16x16x32_bf16 v[4:7], v[160:163], a[132:135], v[4:7]\n"
      "ds_read_b64_tr_b16 v[138:139], v218 offset:43008\n"
      "v_lshlrev_b32 v246, 30, v246\n"
      "v_mfma_f32_16x16x32_bf16 v[4:7], v[164:167], a[148:151], v[4:7]\n"
      "s_add_u32 s96, s95, 28672\n"
      "ds_read_b64_tr_b16 v[140:141], v218 offset:45056\n"
      "v_lshl_add_u32 v240, v245, 11, v246\n"
      "v_mfma_f32_16x16x32_bf16 v[4:7], v[168:171], a[164:167], v[4:7]\n"
      "buffer_load_dwordx4 v233, s[68:71], s95 offen offset:3072 lds\n"
      "v_mfma_f32_16x16x32_bf16 v[4:7], v[172:175], a[180:183], v[4:7]\n"
      "ds_read_b64_tr_b16 v[142:143], v218 offset:47104\n"
      "v_min_u32 v245, 1, v245\n"
      "v_mfma_f32_16x16x32_bf16 v[4:7], v[176:179], a[196:199], v[4:7]\n"
      "s_add_u32 m0, s81, 69632\n"
      "ds_read_b64_tr_b16 v[144:145], v218 offset:49152\n"
      "v_lshlrev_b32 v216, 30, v245\n"
      "v_mfma_f32_16x16x32_bf16 v[4:7], v[180:183], a[212:215], v[4:7]\n"
      "buffer_load_dwordx4 v230, s[68:71], s96 offen offset:0 lds\n"
      "v_mfma_f32_16x16x32_bf16 v[4:7], v[184:187], a[228:231], v[4:7]\n"
      "ds_read_b64_tr_b16 v[146:147], v218 offset:51200\n"
      "v_and_b32 v255, 7, v254\n"
      "v_mfma_f32_16x16x32_bf16 v[4:7], v[188:191], a[244:247], v[4:7]\n"
      "ds_read_b64_tr_b16 v[148:149], v218 offset:53248\n"
      "v_lshlrev_b32 v255, 2, v255\n"
      "v_mfma_f32_16x16x32_bf16 v[8:11], v[160:163], a[136:139], v[8:11]\n"
      "buffer_load_dwordx4 v231, s[68:71], s96 offen offset:1024 lds\n"
      "v_mfma_f32_16x16x32_bf16 v[8:11], v[164:167], a[152:155], v[8:11]\n"
      "ds_read_b64_tr_b16 v[150:151], v218 offset:55296\n"
      "v_and_b32 v245, 1, v254\n"
      "v_mfma_f32_16x16x32_bf16 v[8:11], v[168:171], a[168:171], v[8:11]\n"
      "ds_read_b64_tr_b16 v[152:153], v218 offset:57344\n"
      "v_lshlrev_b32 v245, 4, v245\n"
      "v_mfma_f32_16x16x32_bf16 v[8:11], v[172:175], a[184:187], v[8:11]\n"
      "buffer_load_dwordx4 v232, s[68:71], s96 offen offset:2048 lds\n"
      "v_mfma_f32_16x16x32_bf16 v[8:11], v[176:179], a[200:203], v[8:11]\n"
      "ds_read_b64_tr_b16 v[154:155], v218 offset:59392\n"
      "v_mov_b32 v246, 0x3f80\n"
      "v_mfma_f32_16x16x32_bf16 v[8:11], v[180:183], a[216:219], v[8:11]\n"
      "ds_read_b64_tr_b16 v[156:157], v218 offset:61440\n"
      "v_lshlrev_b32 v245, v245, v246\n"
      "v_mfma_f32_16x16x32_bf16 v[8:11], v[184:187], a[232:235], v[8:11]\n"
      "buffer_load_dwordx4 v233, s[68:71], s96 offen offset:3072 lds\n"
      "v_mfma_f32_16x16x32_bf16 v[8:11], v[188:191], a[248:251], v[8:11]\n"
      "ds_read_b64_tr_b16 v[158:159], v218 offset:63488\n"
      "v_bfe_u32 v246, v254, 1, 3\n"
      "v_mfma_f32_16x16x32_bf16 v[12:15], v[160:163], a[140:143], v[12:15]\n"
      "buffer_store_dwordx4 v[24:27], v238, s[72:75], s85 offen sc1\n"
      "v_mfma_f32_16x16x32_bf16 v[12:15], v[164:167], a[156:159], v[12:15]\n"
      "v_lshrrev_b32 v247, 4, v254\n"
      "v_mfma_f32_16x16x32_bf16 v[12:15], v[168:171], a[172:175], v[12:15]\n"
      "v_lshlrev_b32 v247, 2, v247\n"
      "v_mfma_f32_16x16x32_bf16 v[12:15], v[172:175], a[188:191], v[12:15]\n"
      "v_sub_u32 v246, v246, v247\n"
      "v_mfma_f32_16x16x32_bf16 v[12:15], v[176:179], a[204:207], v[12:15]\n"
      "v_cmp_eq_u32 vcc, 0, v246\n"
      "v_mfma_f32_16x16x32_bf16 v[12:15], v[180:183], a[220:223], v[12:15]\n"
      "s_nop 0\n"
      "v_mfma_f32_16x16x32_bf16 v[12:15], v[184:187], a[236:239], v[12:15]\n"
      "v_mfma_f32_16x16x32_bf16 v[12:15], v[188:191], a[252:255], v[12:15]\n"
      "s_waitcnt vmcnt(10) lgkmcnt(0)\n"
      "s_barrier\n"
      "v_mfma_f32_16x16x32_bf16 v[16:19], v[128:131], a[0:3], 0\n"
      "s_add_u32 m0, s81, 32768\n"
      "s_add_u32 s95, s91, 229376\n"
      "ds_read_b64_tr_b16 v[160:161], v218 offset:0\n"
      "v_mfma_f32_16x16x32_bf16 v[16:19], v[132:135], a[16:19], v[16:19]\n"
      "buffer_load_dwordx4 v230, s[68:71], s95 offen offset:0 lds\n"
      "v_mfma_f32_16x16x32_bf16 v[16:19], v[136:139], a[32:35], v[16:19]\n"
      "s_add_u32 s84, s82, s83\n"
      "ds_read_b64_tr_b16 v[162:163], v218 offset:2048\n"
      "v_cndmask_b32 v192, 0, v245, vcc\n"
      "v_mfma_f32_16x16x32_bf16 v[16:19], v[140:143], a[48:51], v[16:19]\n"
      "s_add_u32 s85, s84, 229376\n"
      "ds_read_b64_tr_b16 v[164:165], v218 offset:4096\n"
      "v_cmp_eq_u32 vcc, 1, v246\n"
      "v_mfma_f32_16x16x32_bf16 v[16:19], v[144:147], a[64:67], v[16:19]\n"
      "buffer_load_dwordx4 v231, s[68:71], s95 offen offset:1024 lds\n"
      "v_mfma_f32_16x16x32_bf16 v[16:19], v[148:151], a[80:83], v[16:19]\n"
      "s_nop 0\n"
      "ds_read_b64_tr_b16 v[166:167], v218 offset:6144\n"
      "v_cvt_pk_bf16_f32 v0, v0, v1\n"
      "v_mfma_f32_16x16x32_bf16 v[16:19], v[152:155], a[96:99], v[16:19]\n"
      "ds_read_b64_tr_b16 v[168:169], v218 offset:8192\n"
      "v_cvt_pk_bf16_f32 v1, v2, v3\n"
      "v_mfma_f32_16x16x32_bf16 v[16:19], v[156:159], a[112:115], v[16:19]\n"
      "buffer_load_dwordx4 v232, s[68:71], s95 offen offset:2048 lds\n"
      "v_mfma_f32_16x16x32_bf16 v[20:23], v[128:131], a[4:7], 0\n"
      "ds_read_b64_tr_b16 v[170:171], v218 offset:10240\n"
      "v_cvt_pk_bf16_f32 v2, v4, v5\n"
      "v_mfma_f32_16x16x32_bf16 v[20:23], v[132:135], a[20:23], v[20:23]\n"
      "s_add_u32 s96, s95, 28672\n"
      "ds_read_b64_tr_b16 v[172:173], v218 offset:12288\n"
      "v_cvt_pk_bf16_f32 v3, v6, v7\n"
      "v_mfma_f32_16x16x32_bf16 v[20:23], v[136:139], a[36:39], v[20:23]\n"
      "buffer_load_dwordx4 v233, s[68:71], s95 offen offset:3072 lds\n"
      "v_mfma_f32_16x16x32_bf16 v[20:23], v[140:143], a[52:55], v[20:23]\n"
      "ds_read_b64_tr_b16 v[174:175], v218 offset:14336\n"
      "v_cvt_pk_bf16_f32 v8, v8, v9\n"
      "v_mfma_f32_16x16x32_bf16 v[20:23], v[144:147], a[68:71], v[20:23]\n"
      "s_add_u32 m0, s81, 36864\n"
      "ds_read_b64_tr_b16 v[176:177], v218 offset:16384\n"
      "v_cvt_pk_bf16_f32 v9, v10, v11\n"
      "v_mfma_f32_16x16x32_bf16 v[20:23], v[148:151], a[84:87], v[20:23]\n"
      "buffer_load_dwordx4 v230, s[68:71], s96 offen offset:0 lds\n"
      "v_mfma_f32_16x16x32_bf16 v[20:23], v[152:155], a[100:103], v[20:23]\n"
      "ds_read_b64_tr_b16 v[178:179], v218 offset:18432\n"
      "v_cvt_pk_bf16_f32 v10, v12, v13\n"
      "v_mfma_f32_16x16x32_bf16 v[20:23], v[156:159], a[116:119], v[20:23]\n"
      "s_add_u32 s91, s91, 128\n"
      "ds_read_b64_tr_b16 v[180:181], v218 offset:20480\n"
      "v_cvt_pk_bf16_f32 v11, v14, v15\n"
      "v_mfma_f32_16x16x32_bf16 v[24:27], v[128:131], a[8:11], 0\n"
      "buffer_load_dwordx4 v231, s[68:71], s96 offen offset:1024 lds\n"
      "v_mfma_f32_16x16x32_bf16 v[24:27], v[132:135], a[24:27], v[24:27]\n"
      "ds_read_b64_tr_b16 v[182:183], v218 offset:22528\n"
      "v_cndmask_b32 v193, 0, v245, vcc\n"
      "v_mfma_f32_16x16x32_bf16 v[24:27], v[136:139], a[40:43], v[24:27]\n"
      "s_cmp_ge_u32 s91, 896\n"
      "s_cselect_b32 s91, 0, s91\n"
      "ds_read_b64_tr_b16 v[184:185], v218 offset:24576\n"
      "v_mfma_f32_16x16x32_bf16 v[24:27], v[140:143], a[56:59], v[24:27]\n"
      "buffer_load_dwordx4 v232, s[68:71], s96 offen offset:2048 lds\n"
      "v_mfma_f32_16x16x32_bf16 v[24:27], v[144:147], a[72:75], v[24:27]\n"
      "ds_read_b64_tr_b16 v[186:187], v218 offset:26624\n"
      "v_cmp_eq_u32 vcc, 2, v246\n"
      "v_mfma_f32_16x16x32_bf16 v[24:27], v[148:151], a[88:91], v[24:27]\n"
      "s_nop 0\n"
      "ds_read_b64_tr_b16 v[188:189], v218 offset:28672\n"
      "v_mfma_f32_16x16x32_bf16 v[24:27], v[152:155], a[104:107], v[24:27]\n"
      "buffer_load_dwordx4 v233, s[68:71], s96 offen offset:3072 lds\n"
      "v_mfma_f32_16x16x32_bf16 v[24:27], v[156:159], a[120:123], v[24:27]\n"
      "ds_read_b64_tr_b16 v[190:191], v218 offset:30720\n"
      "v_cndmask_b32 v194, 0, v245, vcc\n"
      "v_mfma_f32_16x16x32_bf16 v[28:31], v[128:131], a[12:15], 0\n"
      "buffer_store_dwordx4 v[0:3], v238, s[72:75], s84 offen sc1\n"
      "v_mfma_f32_16x16x32_bf16 v[28:31], v[132:135], a[28:31], v[28:31]\n"
      "v_cmp_eq_u32 vcc, 3, v246\n"
      "v_mfma_f32_16x16x32_bf16 v[28:31], v[136:139], a[44:47], v[28:31]\n"
      "s_nop 0\n"
      "v_mfma_f32_16x16x32_bf16 v[28:31], v[140:143], a[60:63], v[28:31]\n"
      "v_cndmask_b32 v195, 0, v245, vcc\n"
      "v_mfma_f32_16x16x32_bf16 v[28:31], v[144:147], a[76:79], v[28:31]\n"
      "v_cmp_eq_u32 vcc, -8, v246\n"
      "v_mfma_f32_16x16x32_bf16 v[28:31], v[148:151], a[92:95], v[28:31]\n"
      "s_nop 0\n"
      "v_mfma_f32_16x16x32_bf16 v[28:31], v[152:155], a[108:111], v[28:31]\n"
      "v_mfma_f32_16x16x32_bf16 v[28:31], v[156:159], a[124:127], v[28:31]\n"
      "s_waitcnt vmcnt(10) lgkmcnt(0)\n"
      "s_barrier\n"
      "v_mfma_f32_16x16x32_bf16 v[16:19], v[160:163], a[128:131], v[16:19]\n"
      "s_add_u32 m0, s81, 0\n"
      "s_add_u32 s95, s91, 0\n"
      "ds_read_b64_tr_b16 v[128:129], v219 offset:0\n"
      "v_mfma_f32_16x16x32_bf16 v[16:19], v[164:167], a[144:147], v[16:19]\n"
      "buffer_load_dwordx4 v230, s[68:71], s95 offen offset:0 lds\n"
      "v_mfma_f32_16x16x32_bf16 v[16:19], v[168:171], a[160:163], v[16:19]\n"
      "s_add_u32 s83, s83, 1024\n"
      "ds_read_b64_tr_b16 v[130:131], v219 offset:2048\n"
      "v_cndmask_b32 v196, 0, v245, vcc\n"
      "v_mfma_f32_16x16x32_bf16 v[16:19], v[172:175], a[176:179], v[16:19]\n"
      "s_cmp_ge_u32 s83, 7168\n"
      "s_cselect_b32 s83, 0, s83\n"
      "ds_read_b64_tr_b16 v[132:133], v219 offset:4096\n"
      "v_mfma_f32_16x16x32_bf16 v[16:19], v[176:179], a[192:195], v[16:19]\n"
      "buffer_load_dwordx4 v231, s[68:71], s95 offen offset:1024 lds\n"
      "v_mfma_f32_16x16x32_bf16 v[16:19], v[180:183], a[208:211], v[16:19]\n"
      "ds_read_b64_tr_b16 v[134:135], v219 offset:6144\n"
      "v_cmp_eq_u32 vcc, -7, v246\n"
      "v_mfma_f32_16x16x32_bf16 v[16:19], v[184:187], a[224:227], v[16:19]\n"
      "s_nop 0\n"
      "ds_read_b64_tr_b16 v[136:137], v219 offset:8192\n"
      "v_mfma_f32_16x16x32_bf16 v[16:19], v[188:191], a[240:243], v[16:19]\n"
      "buffer_load_dwordx4 v232, s[68:71], s95 offen offset:2048 lds\n"
      "v_mfma_f32_16x16x32_bf16 v[20:23], v[160:163], a[132:135], v[20:23]\n"
      "ds_read_b64_tr_b16 v[138:139], v219 offset:10240\n"
      "v_cndmask_b32 v197, 0, v245, vcc\n"
      "v_mfma_f32_16x16x32_bf16 v[20:23], v[164:167], a[148:151], v[20:23]\n"
      "s_add_u32 s96, s95, 28672\n"
      "ds_read_b64_tr_b16 v[140:141], v219 offset:12288\n"
      "v_cmp_eq_u32 vcc, -6, v246\n"
      "v_mfma_f32_16x16x32_bf16 v[20:23], v[168:171], a[164:167], v[20:23]\n"
      "buffer_load_dwordx4 v233, s[68:71], s95 offen offset:3072 lds\n"
      "v_mfma_f32_16x16x32_bf16 v[20:23], v[172:175], a[180:183], v[20:23]\n"
      "s_nop 0\n"
      "ds_read_b64_tr_b16 v[142:143], v219 offset:14336\n"
      "v_mfma_f32_16x16x32_bf16 v[20:23], v[176:179], a[196:199], v[20:23]\n"
      "s_add_u32 m0, s81, 4096\n"
      "ds_read_b64_tr_b16 v[144:145], v219 offset:16384\n"
      "v_cndmask_b32 v198, 0, v245, vcc\n"
      "v_mfma_f32_16x16x32_bf16 v[20:23], v[180:183], a[212:215], v[20:23]\n"
      "buffer_load_dwordx4 v230, s[68:71], s96 offen offset:0 lds\n"
      "v_mfma_f32_16x16x32_bf16 v[20:23], v[184:187], a[228:231], v[20:23]\n"
      "ds_read_b64_tr_b16 v[146:147], v219 offset:18432\n"
      "v_cmp_eq_u32 vcc, -5, v246\n"
      "v_mfma_f32_16x16x32_bf16 v[20:23], v[188:191], a[244:247], v[20:23]\n"
      "s_nop 0\n"
      "ds_read_b64_tr_b16 v[148:149], v219 offset:20480\n"
      "v_mfma_f32_16x16x32_bf16 v[24:27], v[160:163], a[136:139], v[24:27]\n"
      "buffer_load_dwordx4 v231, s[68:71], s96 offen offset:1024 lds\n"
      "v_mfma_f32_16x16x32_bf16 v[24:27], v[164:167], a[152:155], v[24:27]\n"
      "ds_read_b64_tr_b16 v[150:151], v219 offset:22528\n"
      "v_cndmask_b32 v199, 0, v245, vcc\n"
      "v_mfma_f32_16x16x32_bf16 v[24:27], v[168:171], a[168:171], v[24:27]\n"
      "s_and_b32 s37, s37, 0xffff\n"
      "ds_read_b64_tr_b16 v[152:153], v219 offset:24576\n"
      "v_mfma_f32_16x16x32_bf16 v[24:27], v[172:175], a[184:187], v[24:27]\n"
      "buffer_load_dwordx4 v232, s[68:71], s96 offen offset:2048 lds\n"
      "v_mfma_f32_16x16x32_bf16 v[24:27], v[176:179], a[200:203], v[24:27]\n"
      "s_mov_b32 s38, 1835008\n"
      "ds_read_b64_tr_b16 v[154:155], v219 offset:26624\n"
      "v_mfma_f32_16x16x32_bf16 v[24:27], v[180:183], a[216:219], v[24:27]\n"
      "s_mov_b32 s39, 0x20000\n"
      "ds_read_b64_tr_b16 v[156:157], v219 offset:28672\n"
      "v_mfma_f32_16x16x32_bf16 v[24:27], v[184:187], a[232:235], v[24:27]\n"
      "buffer_load_dwordx4 v233, s[68:71], s96 offen offset:3072 lds\n"
      "v_mfma_f32_16x16x32_bf16 v[24:27], v[188:191], a[248:251], v[24:27]\n"
      "s_mov_b32 s76, s56\n"
      "ds_read_b64_tr_b16 v[158:159], v219 offset:30720\n"
      "v_mfma_f32_16x16x32_bf16 v[28:31], v[160:163], a[140:143], v[28:31]\n"
      "buffer_store_dwordx4 v[8:11], v238, s[72:75], s85 offen sc1\n"
      "v_mfma_f32_16x16x32_bf16 v[28:31], v[164:167], a[156:159], v[28:31]\n"
      "s_and_b32 s77, s57, 0xffff\n"
      "v_mfma_f32_16x16x32_bf16 v[28:31], v[168:171], a[172:175], v[28:31]\n"
      "s_mov_b32 s78, 896\n"
      "v_mfma_f32_16x16x32_bf16 v[28:31], v[172:175], a[188:191], v[28:31]\n"
      "s_lshl_b32 s56, s80, 11\n"
      "v_mfma_f32_16x16x32_bf16 v[28:31], v[176:179], a[204:207], v[28:31]\n"
      "s_lshl_b32 s59, s92, 7\n"
      "v_mfma_f32_16x16x32_bf16 v[28:31], v[180:183], a[220:223], v[28:31]\n"
      "s_add_u32 s59, s59, 135168\n"
      "v_mfma_f32_16x16x32_bf16 v[28:31], v[184:187], a[236:239], v[28:31]\n"
      "v_mfma_f32_16x16x32_bf16 v[28:31], v[188:191], a[252:255], v[28:31]\n"
      "s_waitcnt vmcnt(10) lgkmcnt(0)\n"
      "s_barrier\n"
      "v_mfma_f32_16x16x32_bf16 v[0:3], v[128:131], a[0:3], 0\n"
      "s_add_u32 m0, s81, 65536\n"
      "s_add_u32 s95, s91, 229376\n"
      "ds_read_b64_tr_b16 v[160:161], v218 offset:32768\n"
      "v_mfma_f32_16x16x32_bf16 v[0:3], v[132:135], a[16:19], v[0:3]\n"
      "buffer_load_dwordx4 v230, s[68:71], s95 offen offset:0 lds\n"
      "v_mfma_f32_16x16x32_bf16 v[0:3], v[136:139], a[32:35], v[0:3]\n"
      "s_add_u32 s84, s82, s83\n"
      "ds_read_b64_tr_b16 v[162:163], v218 offset:34816\n"
      "v_mfma_f32_16x16x32_bf16 v[0:3], v[140:143], a[48:51], v[0:3]\n"
      "s_add_u32 s85, s84, 229376\n"
      "ds_read_b64_tr_b16 v[164:165], v218 offset:36864\n"
      "v_mfma_f32_16x16x32_bf16 v[0:3], v[144:147], a[64:67], v[0:3]\n"
      "buffer_load_dwordx4 v231, s[68:71], s95 offen offset:1024 lds\n"
      "v_mfma_f32_16x16x32_bf16 v[0:3], v[148:151], a[80:83], v[0:3]\n"
      "s_add_u32 s57, s56, s89\n"
      "ds_read_b64_tr_b16 v[166:167], v218 offset:38912\n"
      "v_cvt_pk_bf16_f32 v16, v16, v17\n"
      "v_mfma_f32_16x16x32_bf16 v[0:3], v[152:155], a[96:99], v[0:3]\n"
      "ds_read_b64_tr_b16 v[168:169], v218 offset:40960\n"
      "v_cvt_pk_bf16_f32 v17, v18, v19\n"
      "v_mfma_f32_16x16x32_bf16 v[0:3], v[156:159], a[112:115], v[0:3]\n"
      "buffer_load_dwordx4 v232, s[68:71], s95 offen offset:2048 lds\n"
      "v_mfma_f32_16x16x32_bf16 v[4:7], v[128:131], a[4:7], 0\n"
      "ds_read_b64_tr_b16 v[170:171], v218 offset:43008\n"
      "v_cvt_pk_bf16_f32 v18, v20, v21\n"
      "v_mfma_f32_16x16x32_bf16 v[4:7], v[132:135], a[20:23], v[4:7]\n"
      "s_add_u32 s96, s95, 28672\n"
      "ds_read_b64_tr_b16 v[172:173], v218 offset:45056\n"
      "v_cvt_pk_bf16_f32 v19, v22, v23\n"
      "v_mfma_f32_16x16x32_bf16 v[4:7], v[136:139], a[36:39], v[4:7]\n"
      "buffer_load_dwordx4 v233, s[68:71], s95 offen offset:3072 lds\n"
      "v_mfma_f32_16x16x32_bf16 v[4:7], v[140:143], a[52:55], v[4:7]\n"
      "ds_read_b64_tr_b16 v[174:175], v218 offset:47104\n"
      "v_cvt_pk_bf16_f32 v24, v24, v25\n"
      "v_mfma_f32_16x16x32_bf16 v[4:7], v[144:147], a[68:71], v[4:7]\n"
      "s_add_u32 m0, s81, 69632\n"
      "ds_read_b64_tr_b16 v[176:177], v218 offset:49152\n"
      "v_cvt_pk_bf16_f32 v25, v26, v27\n"
      "v_mfma_f32_16x16x32_bf16 v[4:7], v[148:151], a[84:87], v[4:7]\n"
      "buffer_load_dwordx4 v230, s[68:71], s96 offen offset:0 lds\n"
      "v_mfma_f32_16x16x32_bf16 v[4:7], v[152:155], a[100:103], v[4:7]\n"
      "ds_read_b64_tr_b16 v[178:179], v218 offset:51200\n"
      "v_cvt_pk_bf16_f32 v26, v28, v29\n"
      "v_mfma_f32_16x16x32_bf16 v[4:7], v[156:159], a[116:119], v[4:7]\n"
      "s_add_u32 s91, s91, 128\n"
      "ds_read_b64_tr_b16 v[180:181], v218 offset:53248\n"
      "v_cvt_pk_bf16_f32 v27, v30, v31\n"
      "v_mfma_f32_16x16x32_bf16 v[8:11], v[128:131], a[8:11], 0\n"
      "buffer_load_dwordx4 v231, s[68:71], s96 offen offset:1024 lds\n"
      "v_mfma_f32_16x16x32_bf16 v[8:11], v[132:135], a[24:27], v[8:11]\n"
      "ds_read_b64_tr_b16 v[182:183], v218 offset:55296\n"
      "v_bfe_u32 v245, v254, 2, 2\n"
      "v_mfma_f32_16x16x32_bf16 v[8:11], v[136:139], a[40:43], v[8:11]\n"
      "s_cmp_ge_u32 s91, 896\n"
      "s_cselect_b32 s91, 0, s91\n"
      "ds_read_b64_tr_b16 v[184:185], v218 offset:57344\n"
      "v_mfma_f32_16x16x32_bf16 v[8:11], v[140:143], a[56:59], v[8:11]\n"
      "buffer_load_dwordx4 v232, s[68:71], s96 offen offset:2048 lds\n"
      "v_mfma_f32_16x16x32_bf16 v[8:11], v[144:147], a[72:75], v[8:11]\n"
      "ds_read_b64_tr_b16 v[186:187], v218 offset:59392\n"
      "v_lshl_add_u32 v245, v247, 1, v245\n"
      "v_mfma_f32_16x16x32_bf16 v[8:11], v[148:151], a[88:91], v[8:11]\n"
      "ds_read_b64_tr_b16 v[188:189], v218 offset:61440\n"
      "v_and_b32 v246, 1, v254\n"
      "v_mfma_f32_16x16x32_bf16 v[8:11], v[152:155], a[104:107], v[8:11]\n"
      "buffer_load_dwordx4 v233, s[68:71], s96 offen offset:3072 lds\n"
      "v_mfma_f32_16x16x32_bf16 v[8:11], v[156:159], a[120:123], v[8:11]\n"
      "ds_read_b64_tr_b16 v[190:191], v218 offset:63488\n"
      "v_lshlrev_b32 v246, 3, v246\n"
      "v_mfma_f32_16x16x32_bf16 v[12:15], v[128:131], a[12:15], 0\n"
      "buffer_store_dwordx4 v[16:19], v238, s[72:75], s84 offen sc1\n"
      "v_mfma_f32_16x16x32_bf16 v[12:15], v[132:135], a[28:31], v[12:15]\n"
      "v_lshl_add_u32 v246, v245, 4, v246\n"
      "v_mfma_f32_16x16x32_bf16 v[12:15], v[136:139], a[44:47], v[12:15]\n"
      "v_and_b32 v245, 2, v254\n"
      "v_mfma_f32_16x16x32_bf16 v[12:15], v[140:143], a[60:63], v[12:15]\n"
      "v_lshl_add_u32 v246, v245, 10, v246\n"
      "v_mfma_f32_16x16x32_bf16 v[12:15], v[144:147], a[76:79], v[12:15]\n"
      "v_lshl_add_u32 v235, s80, 14, v246\n"
      "v_mfma_f32_16x16x32_bf16 v[12:15], v[148:151], a[92:95], v[12:15]\n"
      "v_add_u32 v235, 98304, v235\n"
      "v_mfma_f32_16x16x32_bf16 v[12:15], v[152:155], a[108:111], v[12:15]\n"
      "v_mfma_f32_16x16x32_bf16 v[12:15], v[156:159], a[124:127], v[12:15]\n"
      "s_waitcnt vmcnt(10) lgkmcnt(0)\n"
      "s_barrier\n"
      "v_mfma_f32_16x16x32_bf16 v[0:3], v[160:163], a[128:131], v[0:3]\n"
      "s_add_u32 m0, s81, 32768\n"
      "s_add_u32 s95, s91, 0\n"
      "ds_read_b64_tr_b16 v[128:129], v218 offset:0\n"
      "v_mfma_f32_16x16x32_bf16 v[0:3], v[164:167], a[144:147], v[0:3]\n"
      "buffer_load_dwordx4 v230, s[68:71], s95 offen offset:0 lds\n"
      "v_mfma_f32_16x16x32_bf16 v[0:3], v[168:171], a[160:163], v[0:3]\n"
      "s_add_u32 s83, s83, 1024\n"
      "ds_read_b64_tr_b16 v[130:131], v218 offset:2048\n"
      "v_lshlrev_b32 v247, 2, v247\n"
      "v_mfma_f32_16x16x32_bf16 v[0:3], v[172:175], a[176:179], v[0:3]\n"
      "s_cmp_ge_u32 s83, 7168\n"
      "s_cselect_b32 s83, 0, s83\n"
      "ds_read_b64_tr_b16 v[132:133], v218 offset:4096\n"
      "v_mfma_f32_16x16x32_bf16 v[0:3], v[176:179], a[192:195], v[0:3]\n"
      "buffer_load_dwordx4 v231, s[68:71], s95 offen offset:1024 lds\n"
      "v_mfma_f32_16x16x32_bf16 v[0:3], v[180:183], a[208:211], v[0:3]\n"
      "ds_read_b64_tr_b16 v[134:135], v218 offset:6144\n"
      "v_lshl_add_u32 v236, s80, 10, v247\n"
      "v_mfma_f32_16x16x32_bf16 v[0:3], v[184:187], a[224:227], v[0:3]\n"
      "ds_read_b64_tr_b16 v[136:137], v218 offset:8192\n"
      "v_add_u32 v236, 1024, v236\n"
      "v_mfma_f32_16x16x32_bf16 v[0:3], v[188:191], a[240:243], v[0:3]\n"
      "buffer_load_dwordx4 v232, s[68:71], s95 offen offset:2048 lds\n"
      "v_mfma_f32_16x16x32_bf16 v[4:7], v[160:163], a[132:135], v[4:7]\n"
      "ds_read_b64_tr_b16 v[138:139], v218 offset:10240\n"
      "v_mfma_f32_16x16x32_bf16 v[4:7], v[164:167], a[148:151], v[4:7]\n"
      "s_add_u32 s96, s95, 28672\n"
      "ds_read_b64_tr_b16 v[140:141], v218 offset:12288\n"
      "v_mfma_f32_16x16x32_bf16 v[4:7], v[168:171], a[164:167], v[4:7]\n"
      "buffer_load_dwordx4 v233, s[68:71], s95 offen offset:3072 lds\n"
      "v_mfma_f32_16x16x32_bf16 v[4:7], v[172:175], a[180:183], v[4:7]\n"
      "ds_read_b64_tr_b16 v[142:143], v218 offset:14336\n"
      "v_mfma_f32_16x16x32_bf16 v[4:7], v[176:179], a[196:199], v[4:7]\n"
      "s_add_u32 m0, s81, 36864\n"
      "ds_read_b64_tr_b16 v[144:145], v218 offset:16384\n"
      "v_mfma_f32_16x16x32_bf16 v[4:7], v[180:183], a[212:215], v[4:7]\n"
      "buffer_load_dwordx4 v230, s[68:71], s96 offen offset:0 lds\n"
      "v_mfma_f32_16x16x32_bf16 v[4:7], v[184:187], a[228:231], v[4:7]\n"
      "ds_read_b64_tr_b16 v[146:147], v218 offset:18432\n"
      "v_mfma_f32_16x16x32_bf16 v[4:7], v[188:191], a[244:247], v[4:7]\n"
      "ds_read_b64_tr_b16 v[148:149], v218 offset:20480\n"
      "v_mfma_f32_16x16x32_bf16 v[8:11], v[160:163], a[136:139], v[8:11]\n"
      "buffer_load_dwordx4 v231, s[68:71], s96 offen offset:1024 lds\n"
      "v_mfma_f32_16x16x32_bf16 v[8:11], v[164:167], a[152:155], v[8:11]\n"
      "ds_read_b64_tr_b16 v[150:151], v218 offset:22528\n"
      "v_mfma_f32_16x16x32_bf16 v[8:11], v[168:171], a[168:171], v[8:11]\n"
      "ds_read_b64_tr_b16 v[152:153], v218 offset:24576\n"
      "v_mfma_f32_16x16x32_bf16 v[8:11], v[172:175], a[184:187], v[8:11]\n"
      "buffer_load_dwordx4 v232, s[68:71], s96 offen offset:2048 lds\n"
      "v_mfma_f32_16x16x32_bf16 v[8:11], v[176:179], a[200:203], v[8:11]\n"
      "ds_read_b64_tr_b16 v[154:155], v218 offset:26624\n"
      "v_mfma_f32_16x16x32_bf16 v[8:11], v[180:183], a[216:219], v[8:11]\n"
      "ds_read_b64_tr_b16 v[156:157], v218 offset:28672\n"
      "v_mfma_f32_16x16x32_bf16 v[8:11], v[184:187], a[232:235], v[8:11]\n"
      "buffer_load_dwordx4 v233, s[68:71], s96 offen offset:3072 lds\n"
      "v_mfma_f32_16x16x32_bf16 v[8:11], v[188:191], a[248:251], v[8:11]\n"
      "ds_read_b64_tr_b16 v[158:159], v218 offset:30720\n"
      "v_mfma_f32_16x16x32_bf16 v[12:15], v[160:163], a[140:143], v[12:15]\n"
      "buffer_store_dwordx4 v[24:27], v238, s[72:75], s85 offen sc1\n"
      "v_mfma_f32_16x16x32_bf16 v[12:15], v[164:167], a[156:159], v[12:15]\n"
      "v_mfma_f32_16x16x32_bf16 v[12:15], v[168:171], a[172:175], v[12:15]\n"
      "v_mfma_f32_16x16x32_bf16 v[12:15], v[172:175], a[188:191], v[12:15]\n"
      "v_mfma_f32_16x16x32_bf16 v[12:15], v[176:179], a[204:207], v[12:15]\n"
      "v_mfma_f32_16x16x32_bf16 v[12:15], v[180:183], a[220:223], v[12:15]\n"
      "v_mfma_f32_16x16x32_bf16 v[12:15], v[184:187], a[236:239], v[12:15]\n"
      "v_mfma_f32_16x16x32_bf16 v[12:15], v[188:191], a[252:255], v[12:15]\n"
      "s_waitcnt vmcnt(10) lgkmcnt(0)\n"
      "s_barrier\n"
      "v_mfma_f32_16x16x32_bf16 v[16:19], v[128:131], a[0:3], 0\n"
      "s_add_u32 m0, s81, 0\n"
      "s_add_u32 s95, s91, 229376\n"
      "ds_read_b64_tr_b16 v[160:161], v219 offset:0\n"
      "v_mfma_f32_16x16x32_bf16 v[16:19], v[132:135], a[16:19], v[16:19]\n"
      "buffer_load_dwordx4 v230, s[68:71], s95 offen offset:0 lds\n"
      "v_mfma_f32_16x16x32_bf16 v[16:19], v[136:139], a[32:35], v[16:19]\n"
      "s_add_u32 s84, s82, s83\n"
      "ds_read_b64_tr_b16 v[162:163], v219 offset:2048\n"
      "v_mfma_f32_16x16x32_bf16 v[16:19], v[140:143], a[48:51], v[16:19]\n"
      "s_add_u32 s85, s84, 229376\n"
      "ds_read_b64_tr_b16 v[164:165], v219 offset:4096\n"
      "v_mfma_f32_16x16x32_bf16 v[16:19], v[144:147], a[64:67], v[16:19]\n"
      "buffer_load_dwordx4 v231, s[68:71], s95 offen offset:1024 lds\n"
      "v_mfma_f32_16x16x32_bf16 v[16:19], v[148:151], a[80:83], v[16:19]\n"
      "ds_read_b64_tr_b16 v[166:167], v219 offset:6144\n"
      "v_cvt_pk_bf16_f32 v0, v0, v1\n"
      "v_mfma_f32_16x16x32_bf16 v[16:19], v[152:155], a[96:99], v[16:19]\n"
      "ds_read_b64_tr_b16 v[168:169], v219 offset:8192\n"
      "v_cvt_pk_bf16_f32 v1, v2, v3\n"
      "v_mfma_f32_16x16x32_bf16 v[16:19], v[156:159], a[112:115], v[16:19]\n"
      "buffer_load_dwordx4 v232, s[68:71], s95 offen offset:2048 lds\n"
      "v_mfma_f32_16x16x32_bf16 v[20:23], v[128:131], a[4:7], 0\n"
      "ds_read_b64_tr_b16 v[170:171], v219 offset:10240\n"
      "v_cvt_pk_bf16_f32 v2, v4, v5\n"
      "v_mfma_f32_16x16x32_bf16 v[20:23], v[132:135], a[20:23], v[20:23]\n"
      "s_add_u32 s96, s95, 28672\n"
      "ds_read_b64_tr_b16 v[172:173], v219 offset:12288\n"
      "v_cvt_pk_bf16_f32 v3, v6, v7\n"
      "v_mfma_f32_16x16x32_bf16 v[20:23], v[136:139], a[36:39], v[20:23]\n"
      "buffer_load_dwordx4 v233, s[68:71], s95 offen offset:3072 lds\n"
      "v_mfma_f32_16x16x32_bf16 v[20:23], v[140:143], a[52:55], v[20:23]\n"
      "ds_read_b64_tr_b16 v[174:175], v219 offset:14336\n"
      "v_cvt_pk_bf16_f32 v8, v8, v9\n"
      "v_mfma_f32_16x16x32_bf16 v[20:23], v[144:147], a[68:71], v[20:23]\n"
      "s_add_u32 m0, s81, 4096\n"
      "ds_read_b64_tr_b16 v[176:177], v219 offset:16384\n"
      "v_cvt_pk_bf16_f32 v9, v10, v11\n"
      "v_mfma_f32_16x16x32_bf16 v[20:23], v[148:151], a[84:87], v[20:23]\n"
      "buffer_load_dwordx4 v230, s[68:71], s96 offen offset:0 lds\n"
      "v_mfma_f32_16x16x32_bf16 v[20:23], v[152:155], a[100:103], v[20:23]\n"
      "ds_read_b64_tr_b16 v[178:179], v219 offset:18432\n"
      "v_cvt_pk_bf16_f32 v10, v12, v13\n"
      "v_mfma_f32_16x16x32_bf16 v[20:23], v[156:159], a[116:119], v[20:23]\n"
      "s_add_u32 s91, s91, 128\n"
      "ds_read_b64_tr_b16 v[180:181], v219 offset:20480\n"
      "v_cvt_pk_bf16_f32 v11, v14, v15\n"
      "v_mfma_f32_16x16x32_bf16 v[24:27], v[128:131], a[8:11], 0\n"
      "buffer_load_dwordx4 v231, s[68:71], s96 offen offset:1024 lds\n"
      "v_mfma_f32_16x16x32_bf16 v[24:27], v[132:135], a[24:27], v[24:27]\n"
      "ds_read_b64_tr_b16 v[182:183], v219 offset:22528\n"
      "v_mfma_f32_16x16x32_bf16 v[24:27], v[136:139], a[40:43], v[24:27]\n"
      "s_cmp_ge_u32 s91, 896\n"
      "s_cselect_b32 s91, 0, s91\n"
      "ds_read_b64_tr_b16 v[184:185], v219 offset:24576\n"
      "v_mfma_f32_16x16x32_bf16 v[24:27], v[140:143], a[56:59], v[24:27]\n"
      "buffer_load_dwordx4 v232, s[68:71], s96 offen offset:2048 lds\n"
      "v_mfma_f32_16x16x32_bf16 v[24:27], v[144:147], a[72:75], v[24:27]\n"
      "ds_read_b64_tr_b16 v[186:187], v219 offset:26624\n"
      "v_mfma_f32_16x16x32_bf16 v[24:27], v[148:151], a[88:91], v[24:27]\n"
      "ds_read_b64_tr_b16 v[188:189], v219 offset:28672\n"
      "v_mfma_f32_16x16x32_bf16 v[24:27], v[152:155], a[104:107], v[24:27]\n"
      "buffer_load_dwordx4 v233, s[68:71], s96 offen offset:3072 lds\n"
      "v_mfma_f32_16x16x32_bf16 v[24:27], v[156:159], a[120:123], v[24:27]\n"
      "ds_read_b64_tr_b16 v[190:191], v219 offset:30720\n"
      "v_mfma_f32_16x16x32_bf16 v[28:31], v[128:131], a[12:15], 0\n"
      "buffer_store_dwordx4 v[0:3], v238, s[72:75], s84 offen sc1\n"
      "v_mfma_f32_16x16x32_bf16 v[28:31], v[132:135], a[28:31], v[28:31]\n"
      "v_mfma_f32_16x16x32_bf16 v[28:31], v[136:139], a[44:47], v[28:31]\n"
      "v_mfma_f32_16x16x32_bf16 v[28:31], v[140:143], a[60:63], v[28:31]\n"
      "v_mfma_f32_16x16x32_bf16 v[28:31], v[144:147], a[76:79], v[28:31]\n"
      "v_mfma_f32_16x16x32_bf16 v[28:31], v[148:151], a[92:95], v[28:31]\n"
      "v_mfma_f32_16x16x32_bf16 v[28:31], v[152:155], a[108:111], v[28:31]\n"
      "v_mfma_f32_16x16x32_bf16 v[28:31], v[156:159], a[124:127], v[28:31]\n"
      "s_waitcnt vmcnt(10) lgkmcnt(0)\n"
      "s_barrier\n"
      "v_mfma_f32_16x16x32_bf16 v[16:19], v[160:163], a[128:131], v[16:19]\n"
      "ds_read_b64_tr_b16 v[128:129], v218 offset:32768\n"
      "v_mfma_f32_16x16x32_bf16 v[16:19], v[164:167], a[144:147], v[16:19]\n"
      "s_add_u32 s83, s83, 1024\n"
      "ds_read_b64_tr_b16 v[130:131], v218 offset:34816\n"
      "v_mfma_f32_16x16x32_bf16 v[16:19], v[168:171], a[160:163], v[16:19]\n"
      "s_cmp_ge_u32 s83, 7168\n"
      "s_cselect_b32 s83, 0, s83\n"
      "v_mfma_f32_16x16x32_bf16 v[16:19], v[172:175], a[176:179], v[16:19]\n"
      "ds_read_b64_tr_b16 v[132:133], v218 offset:36864\n"
      "v_mfma_f32_16x16x32_bf16 v[16:19], v[176:179], a[192:195], v[16:19]\n"
      "s_add_u32 m0, s56, 65536\n"
      "ds_read_b64_tr_b16 v[134:135], v218 offset:38912\n"
      "v_mfma_f32_16x16x32_bf16 v[16:19], v[180:183], a[208:211], v[16:19]\n"
      "buffer_load_dwordx4 v242, s[72:75], s57 offen offset:0 sc1 lds\n"
      "v_mfma_f32_16x16x32_bf16 v[16:19], v[184:187], a[224:227], v[16:19]\n"
      "ds_read_b64_tr_b16 v[136:137], v218 offset:40960\n"
      "v_mfma_f32_16x16x32_bf16 v[16:19], v[188:191], a[240:243], v[16:19]\n"
      "ds_read_b64_tr_b16 v[138:139], v218 offset:43008\n"
      "v_mfma_f32_16x16x32_bf16 v[20:23], v[160:163], a[132:135], v[20:23]\n"
      "buffer_load_dwordx4 v242, s[72:75], s57 offen offset:1024 sc1 lds\n"
      "v_mfma_f32_16x16x32_bf16 v[20:23], v[164:167], a[148:151], v[20:23]\n"
      "ds_read_b64_tr_b16 v[140:141], v218 offset:45056\n"
      "v_mfma_f32_16x16x32_bf16 v[20:23], v[168:171], a[164:167], v[20:23]\n"
      "s_add_u32 m0, s56, 73728\n"
      "ds_read_b64_tr_b16 v[142:143], v218 offset:47104\n"
      "v_mfma_f32_16x16x32_bf16 v[20:23], v[172:175], a[180:183], v[20:23]\n"
      "buffer_load_dwordx4 v242, s[36:39], s58 offen lds\n"
      "v_mfma_f32_16x16x32_bf16 v[20:23], v[176:179], a[196:199], v[20:23]\n"
      "ds_read_b64_tr_b16 v[144:145], v218 offset:49152\n"
      "v_mfma_f32_16x16x32_bf16 v[20:23], v[180:183], a[212:215], v[20:23]\n"
      "v_mfma_f32_16x16x32_bf16 v[20:23], v[184:187], a[228:231], v[20:23]\n"
      "ds_read_b64_tr_b16 v[146:147], v218 offset:51200\n"
      "v_mfma_f32_16x16x32_bf16 v[20:23], v[188:191], a[244:247], v[20:23]\n"
      "s_add_u32 m0, s56, 74752\n"
      "s_add_u32 s58, s58, 14336\n"
      "ds_read_b64_tr_b16 v[148:149], v218 offset:53248\n"
      "v_mfma_f32_16x16x32_bf16 v[24:27], v[160:163], a[136:139], v[24:27]\n"
      "buffer_load_dwordx4 v242, s[36:39], s58 offen lds\n"
      "v_mfma_f32_16x16x32_bf16 v[24:27], v[164:167], a[152:155], v[24:27]\n"
      "ds_read_b64_tr_b16 v[150:151], v218 offset:55296\n"
      "v_mfma_f32_16x16x32_bf16 v[24:27], v[168:171], a[168:171], v[24:27]\n"
      "ds_read_b64_tr_b16 v[152:153], v218 offset:57344\n"
      "v_mfma_f32_16x16x32_bf16 v[24:27], v[172:175], a[184:187], v[24:27]\n"
      "buffer_load_dwordx2 v[48:49], v225, s[76:79], 0 offen offset:0\n"
      "v_mfma_f32_16x16x32_bf16 v[24:27], v[176:179], a[200:203], v[24:27]\n"
      "buffer_store_dwordx4 v[8:11], v238, s[72:75], s85 offen sc1\n"
      "v_mfma_f32_16x16x32_bf16 v[24:27], v[180:183], a[216:219], v[24:27]\n"
      "ds_read_b64_tr_b16 v[154:155], v218 offset:59392\n"
      "v_mfma_f32_16x16x32_bf16 v[24:27], v[184:187], a[232:235], v[24:27]\n"
      "ds_read_b64_tr_b16 v[156:157], v218 offset:61440\n"
      "v_mfma_f32_16x16x32_bf16 v[24:27], v[188:191], a[248:251], v[24:27]\n"
      "ds_read_b64_tr_b16 v[158:159], v218 offset:63488\n"
      "v_mfma_f32_16x16x32_bf16 v[28:31], v[160:163], a[140:143], v[28:31]\n"
      "buffer_load_dwordx2 v[52:53], v225, s[76:79], 0 offen offset:64\n"
      "v_mfma_f32_16x16x32_bf16 v[28:31], v[164:167], a[156:159], v[28:31]\n"
      "v_mfma_f32_16x16x32_bf16 v[28:31], v[168:171], a[172:175], v[28:31]\n"
      "v_mfma_f32_16x16x32_bf16 v[28:31], v[172:175], a[188:191], v[28:31]\n"
      "buffer_load_dwordx2 v[56:57], v226, s[76:79], 0 offen offset:512\n"
      "v_mfma_f32_16x16x32_bf16 v[28:31], v[176:179], a[204:207], v[28:31]\n"
      "v_mfma_f32_16x16x32_bf16 v[28:31], v[180:183], a[220:223], v[28:31]\n"
      "v_mfma_f32_16x16x32_bf16 v[28:31], v[184:187], a[236:239], v[28:31]\n"
      "v_mfma_f32_16x16x32_bf16 v[28:31], v[188:191], a[252:255], v[28:31]\n"
      "s_waitcnt vmcnt(9) lgkmcnt(0)\n"
      "s_barrier\n"
      "v_mfma_f32_16x16x32_bf16 v[0:3], v[128:131], a[0:3], 0\n"
      "ds_read_b64_tr_b16 v[160:161], v218 offset:0\n"
      "v_mfma_f32_16x16x32_bf16 v[0:3], v[132:135], a[16:19], v[0:3]\n"
      "ds_read_b64_tr_b16 v[162:163], v218 offset:2048\n"
      "v_mfma_f32_16x16x32_bf16 v[0:3], v[136:139], a[32:35], v[0:3]\n"
      "s_add_u32 s84, s82, s83\n"
      "v_mfma_f32_16x16x32_bf16 v[0:3], v[140:143], a[48:51], v[0:3]\n"
      "s_add_u32 s85, s84, 229376\n"
      "ds_read_b64_tr_b16 v[164:165], v218 offset:4096\n"
      "v_mfma_f32_16x16x32_bf16 v[0:3], v[144:147], a[64:67], v[0:3]\n"
      "ds_read_b64_tr_b16 v[166:167], v218 offset:6144\n"
      "v_mfma_f32_16x16x32_bf16 v[0:3], v[148:151], a[80:83], v[0:3]\n"
      "v_cvt_pk_bf16_f32 v16, v16, v17\n"
      "v_mfma_f32_16x16x32_bf16 v[0:3], v[152:155], a[96:99], v[0:3]\n"
      "ds_read_b64_tr_b16 v[168:169], v218 offset:8192\n"
      "v_cvt_pk_bf16_f32 v17, v18, v19\n"
      "v_mfma_f32_16x16x32_bf16 v[0:3], v[156:159], a[112:115], v[0:3]\n"
      "ds_read_b64_tr_b16 v[170:171], v218 offset:10240\n"
      "v_mfma_f32_16x16x32_bf16 v[4:7], v[128:131], a[4:7], 0\n"
      "v_cvt_pk_bf16_f32 v18, v20, v21\n"
      "v_mfma_f32_16x16x32_bf16 v[4:7], v[132:135], a[20:23], v[4:7]\n"
      "ds_read_b64_tr_b16 v[172:173], v218 offset:12288\n"
      "v_cvt_pk_bf16_f32 v19, v22, v23\n"
      "v_mfma_f32_16x16x32_bf16 v[4:7], v[136:139], a[36:39], v[4:7]\n"
      "ds_read_b64_tr_b16 v[174:175], v218 offset:14336\n"
      "v_mfma_f32_16x16x32_bf16 v[4:7], v[140:143], a[52:55], v[4:7]\n"
      "v_cvt_pk_bf16_f32 v24, v24, v25\n"
      "v_mfma_f32_16x16x32_bf16 v[4:7], v[144:147], a[68:71], v[4:7]\n"
      "ds_read_b64_tr_b16 v[176:177], v218 offset:16384\n"
      "v_cvt_pk_bf16_f32 v25, v26, v27\n"
      "v_mfma_f32_16x16x32_bf16 v[4:7], v[148:151], a[84:87], v[4:7]\n"
      "buffer_load_dwordx2 v[60:61], v226, s[76:79], 0 offen offset:576\n"
      "v_mfma_f32_16x16x32_bf16 v[4:7], v[152:155], a[100:103], v[4:7]\n"
      "ds_read_b64_tr_b16 v[178:179], v218 offset:18432\n"
      "v_cvt_pk_bf16_f32 v26, v28, v29\n"
      "v_mfma_f32_16x16x32_bf16 v[4:7], v[156:159], a[116:119], v[4:7]\n"
      "ds_read_b64_tr_b16 v[180:181], v218 offset:20480\n"
      "v_cvt_pk_bf16_f32 v27, v30, v31\n"
      "v_mfma_f32_16x16x32_bf16 v[8:11], v[128:131], a[8:11], 0\n"
      "v_mfma_f32_16x16x32_bf16 v[8:11], v[132:135], a[24:27], v[8:11]\n"
      "ds_read_b64_tr_b16 v[182:183], v218 offset:22528\n"
      "v_mfma_f32_16x16x32_bf16 v[8:11], v[136:139], a[40:43], v[8:11]\n"
      "s_add_u32 m0, s98, 102400\n"
      "s_add_u32 s97, s97, s67\n"
      "ds_read_b64_tr_b16 v[184:185], v218 offset:24576\n"
      "v_mfma_f32_16x16x32_bf16 v[8:11], v[140:143], a[56:59], v[8:11]\n"
      "buffer_load_dwordx4 v234, s[44:47], s97 offen lds\n"
      "v_mfma_f32_16x16x32_bf16 v[8:11], v[144:147], a[72:75], v[8:11]\n"
      "buffer_store_dwordx4 v[16:19], v238, s[72:75], s84 offen sc1\n"
      "v_mfma_f32_16x16x32_bf16 v[8:11], v[148:151], a[88:91], v[8:11]\n"
      "ds_read_b64_tr_b16 v[186:187], v218 offset:26624\n"
      "v_mfma_f32_16x16x32_bf16 v[8:11], v[152:155], a[104:107], v[8:11]\n"
      "ds_read_b64_tr_b16 v[188:189], v218 offset:28672\n"
      "v_mfma_f32_16x16x32_bf16 v[8:11], v[156:159], a[120:123], v[8:11]\n"
      "s_add_u32 m0, s98, 103424\n"
      "s_add_u32 s97, s97, s99\n"
      "ds_read_b64_tr_b16 v[190:191], v218 offset:30720\n"
      "v_mfma_f32_16x16x32_bf16 v[12:15], v[128:131], a[12:15], 0\n"
      "buffer_load_dwordx4 v234, s[44:47], s97 offen lds\n"
      "v_mfma_f32_16x16x32_bf16 v[12:15], v[132:135], a[28:31], v[12:15]\n"
      "v_mfma_f32_16x16x32_bf16 v[12:15], v[136:139], a[44:47], v[12:15]\n"
      "s_add_u32 m0, s98, 104448\n"
      "s_add_u32 s97, s97, s99\n"
      "v_mfma_f32_16x16x32_bf16 v[12:15], v[140:143], a[60:63], v[12:15]\n"
      "buffer_load_dwordx4 v234, s[44:47], s97 offen lds\n"
      "v_mfma_f32_16x16x32_bf16 v[12:15], v[144:147], a[76:79], v[12:15]\n"
      "v_mfma_f32_16x16x32_bf16 v[12:15], v[148:151], a[92:95], v[12:15]\n"
      "v_mfma_f32_16x16x32_bf16 v[12:15], v[152:155], a[108:111], v[12:15]\n"
      "v_mfma_f32_16x16x32_bf16 v[12:15], v[156:159], a[124:127], v[12:15]\n"
      "s_waitcnt lgkmcnt(0)\n"
      "v_mfma_f32_16x16x32_bf16 v[0:3], v[160:163], a[128:131], v[0:3]\n"
      "v_mfma_f32_16x16x32_bf16 v[0:3], v[164:167], a[144:147], v[0:3]\n"
      "s_add_u32 s83, s83, 1024\n"
      "v_mfma_f32_16x16x32_bf16 v[0:3], v[168:171], a[160:163], v[0:3]\n"
      "s_cmp_ge_u32 s83, 7168\n"
      "s_cselect_b32 s83, 0, s83\n"
      "v_mfma_f32_16x16x32_bf16 v[0:3], v[172:175], a[176:179], v[0:3]\n"
      "s_add_u32 m0, s98, 105472\n"
      "s_add_u32 s97, s97, s99\n"
      "v_mfma_f32_16x16x32_bf16 v[0:3], v[176:179], a[192:195], v[0:3]\n"
      "buffer_load_dwordx4 v234, s[44:47], s97 offen lds\n"
      "v_mfma_f32_16x16x32_bf16 v[0:3], v[180:183], a[208:211], v[0:3]\n"
      "s_add_u32 m0, s98, 106496\n"
      "s_add_u32 s97, s97, s99\n"
      "v_mfma_f32_16x16x32_bf16 v[0:3], v[184:187], a[224:227], v[0:3]\n"
      "buffer_load_dwordx4 v234, s[44:47], s97 offen lds\n"
      "v_mfma_f32_16x16x32_bf16 v[0:3], v[188:191], a[240:243], v[0:3]\n"
      "s_add_u32 m0, s98, 107520\n"
      "s_add_u32 s97, s97, s99\n"
      "v_mfma_f32_16x16x32_bf16 v[4:7], v[160:163], a[132:135], v[4:7]\n"
      "buffer_load_dwordx4 v234, s[44:47], s97 offen lds\n"
      "v_mfma_f32_16x16x32_bf16 v[4:7], v[164:167], a[148:151], v[4:7]\n"
      "s_add_u32 m0, s98, 108544\n"
      "s_add_u32 s97, s97, s99\n"
      "v_mfma_f32_16x16x32_bf16 v[4:7], v[168:171], a[164:167], v[4:7]\n"
      "buffer_load_dwordx4 v234, s[44:47], s97 offen lds\n"
      "v_mfma_f32_16x16x32_bf16 v[4:7], v[172:175], a[180:183], v[4:7]\n"
      "s_add_u32 m0, s98, 109568\n"
      "s_add_u32 s97, s97, s99\n"
      "v_mfma_f32_16x16x32_bf16 v[4:7], v[176:179], a[196:199], v[4:7]\n"
      "buffer_load_dwordx4 v234, s[44:47], s97 offen lds\n"
      "v_mfma_f32_16x16x32_bf16 v[4:7], v[180:183], a[212:215], v[4:7]\n"
      "s_add_u32 m0, s98, 110592\n"
      "s_add_u32 s97, s97, s99\n"
      "v_mfma_f32_16x16x32_bf16 v[4:7], v[184:187], a[228:231], v[4:7]\n"
      "buffer_load_dwordx4 v234, s[44:47], s97 offen lds\n"
      "v_mfma_f32_16x16x32_bf16 v[4:7], v[188:191], a[244:247], v[4:7]\n"
      "s_add_u32 m0, s98, 111616\n"
      "s_add_u32 s97, s97, s99\n"
      "v_mfma_f32_16x16x32_bf16 v[8:11], v[160:163], a[136:139], v[8:11]\n"
      "buffer_load_dwordx4 v234, s[44:47], s97 offen lds\n"
      "v_mfma_f32_16x16x32_bf16 v[8:11], v[164:167], a[152:155], v[8:11]\n"
      "ds_write_b128 v239, v[64:67] offset:0\n"
      "v_mfma_f32_16x16x32_bf16 v[8:11], v[168:171], a[168:171], v[8:11]\n"
      "v_mfma_f32_16x16x32_bf16 v[8:11], v[172:175], a[184:187], v[8:11]\n"
      "v_mfma_f32_16x16x32_bf16 v[8:11], v[176:179], a[200:203], v[8:11]\n"
      "buffer_store_dwordx4 v[24:27], v238, s[72:75], s85 offen sc1\n"
      "v_mfma_f32_16x16x32_bf16 v[8:11], v[180:183], a[216:219], v[8:11]\n"
      "ds_write_b128 v239, v[68:71] offset:1024\n"
      "v_mfma_f32_16x16x32_bf16 v[8:11], v[184:187], a[232:235], v[8:11]\n"
      "v_mfma_f32_16x16x32_bf16 v[8:11], v[188:191], a[248:251], v[8:11]\n"
      "v_mfma_f32_16x16x32_bf16 v[12:15], v[160:163], a[140:143], v[12:15]\n"
      "buffer_load_dword v252, v255, s[72:75], s59 offen sc1\n"
      "v_mfma_f32_16x16x32_bf16 v[12:15], v[164:167], a[156:159], v[12:15]\n"
      "ds_write_b128 v239, v[72:75] offset:2048\n"
      "v_mfma_f32_16x16x32_bf16 v[12:15], v[168:171], a[172:175], v[12:15]\n"
      "v_mfma_f32_16x16x32_bf16 v[12:15], v[172:175], a[188:191], v[12:15]\n"
      "v_mfma_f32_16x16x32_bf16 v[12:15], v[176:179], a[204:207], v[12:15]\n"
      "ds_write_b128 v239, v[76:79] offset:3072\n"
      "v_mfma_f32_16x16x32_bf16 v[12:15], v[180:183], a[220:223], v[12:15]\n"
      "v_mfma_f32_16x16x32_bf16 v[12:15], v[184:187], a[236:239], v[12:15]\n"
      "v_mfma_f32_16x16x32_bf16 v[12:15], v[188:191], a[252:255], v[12:15]\n"
      "s_nop 7\n"
      "s_add_u32 s84, s82, s83\n"
      "s_add_u32 s85, s84, 229376\n"
      "v_cvt_pk_bf16_f32 v0, v0, v1\n"
      "v_cvt_pk_bf16_f32 v1, v2, v3\n"
      "v_cvt_pk_bf16_f32 v2, v4, v5\n"
      "v_cvt_pk_bf16_f32 v3, v6, v7\n"
      "buffer_store_dwordx4 v[0:3], v238, s[72:75], s84 offen sc1\n"
      "v_cvt_pk_bf16_f32 v8, v8, v9\n"
      "v_cvt_pk_bf16_f32 v9, v10, v11\n"
      "v_cvt_pk_bf16_f32 v10, v12, v13\n"
      "v_cvt_pk_bf16_f32 v11, v14, v15\n"
      "buffer_store_dwordx4 v[8:11], v238, s[72:75], s85 offen sc1\n"
      "v_mov_b32 v253, 0xffff0000\n"
      "v_mov_b32 v177, s53\n"
      "v_mov_b32 v201, s53\n"
      "v_mov_b32 v179, s53\n"
      "v_mov_b32 v203, s53\n"
      "v_mov_b32 v181, s53\n"
      "v_mov_b32 v205, s53\n"
      "v_mov_b32 v183, s53\n"
      "v_mov_b32 v207, s53\n"
      "v_mov_b32 v185, s53\n"
      "v_mov_b32 v209, s53\n"
      "v_mov_b32 v187, s53\n"
      "v_mov_b32 v211, s53\n"
      "v_mov_b32 v189, s53\n"
      "v_mov_b32 v213, s53\n"
      "v_mov_b32 v191, s53\n"
      "v_mov_b32 v215, s53\n"
      "s_lshl_b32 s87, s93, 2\n"
      "s_add_u32 s87, s87, s59\n"
      "v_add_u32 v244, 65536, v223\n"
      "v_add_u32 v245, 65536, v224\n"
      "s_waitcnt vmcnt(2)\n"
      "v_cmp_eq_u32 vcc, s53, v252\n"
      "v_and_b32 v51, v253, v49\n"
      "v_lshlrev_b32 v50, 16, v49\n"
      "v_and_b32 v49, v253, v48\n"
      "v_lshlrev_b32 v48, 16, v48\n"
      "v_and_b32 v55, v253, v53\n"
      "v_lshlrev_b32 v54, 16, v53\n"
      "v_and_b32 v53, v253, v52\n"
      "v_lshlrev_b32 v52, 16, v52\n"
      "v_and_b32 v59, v253, v57\n"
      "v_lshlrev_b32 v58, 16, v57\n"
      "v_and_b32 v57, v253, v56\n"
      "v_lshlrev_b32 v56, 16, v56\n"
      "v_and_b32 v63, v253, v61\n"
      "v_lshlrev_b32 v62, 16, v61\n"
      "v_and_b32 v61, v253, v60\n"
      "v_lshlrev_b32 v60, 16, v60\n"
      "s_bitset1_b32 vcc_lo, s93\n"
      "s_mov_b32 s60, 0\n"
      "s_waitcnt vmcnt(0) lgkmcnt(0)\n"
      "s_barrier\n"
      "buffer_store_dword v177, v216, s[72:75], s87 offen sc1\n"
      "buffer_load_dword v252, v255, s[72:75], s59 offen sc1\n"
      "s_andn2_b32 s86, vcc_lo, s60\n"
      "s_or_b32 s60, s60, vcc_lo\n"
      "s_bitcmp0_b32 s86, 0\n"
      "s_cbranch_scc1 TP0_%=\n"
      "s_mov_b32 s84, 0\n"
      "buffer_load_dwordx4 a[0:3], v228, s[72:75], s84 offen sc1\n"
      "buffer_load_dwordx4 a[4:7], v229, s[72:75], s84 offen sc1\n"
      "TP0_%=:\n"
      "s_bitcmp0_b32 s86, 1\n"
      "s_cbranch_scc1 TP1_%=\n"
      "s_mov_b32 s84, 7168\n"
      "buffer_load_dwordx4 a[8:11], v228, s[72:75], s84 offen sc1\n"
      "buffer_load_dwordx4 a[12:15], v229, s[72:75], s84 offen sc1\n"
      "TP1_%=:\n"
      "s_bitcmp0_b32 s86, 2\n"
      "s_cbranch_scc1 TP2_%=\n"
      "s_mov_b32 s84, 14336\n"
      "buffer_load_dwordx4 a[16:19], v228, s[72:75], s84 offen sc1\n"
      "buffer_load_dwordx4 a[20:23], v229, s[72:75], s84 offen sc1\n"
      "TP2_%=:\n"
      "s_bitcmp0_b32 s86, 3\n"
      "s_cbranch_scc1 TP3_%=\n"
      "s_mov_b32 s84, 21504\n"
      "buffer_load_dwordx4 a[24:27], v228, s[72:75], s84 offen sc1\n"
      "buffer_load_dwordx4 a[28:31], v229, s[72:75], s84 offen sc1\n"
      "TP3_%=:\n"
      "s_bitcmp0_b32 s86, 4\n"
      "s_cbranch_scc1 TP4_%=\n"
      "s_mov_b32 s84, 28672\n"
      "buffer_load_dwordx4 a[32:35], v228, s[72:75], s84 offen sc1\n"
      "buffer_load_dwordx4 a[36:39], v229, s[72:75], s84 offen sc1\n"
      "TP4_%=:\n"
      "s_bitcmp0_b32 s86, 5\n"
      "s_cbranch_scc1 TP5_%=\n"
      "s_mov_b32 s84, 35840\n"
      "buffer_load_dwordx4 a[40:43], v228, s[72:75], s84 offen sc1\n"
      "buffer_load_dwordx4 a[44:47], v229, s[72:75], s84 offen sc1\n"
      "TP5_%=:\n"
      "s_bitcmp0_b32 s86, 6\n"
      "s_cbranch_scc1 TP6_%=\n"
      "s_mov_b32 s84, 43008\n"
      "buffer_load_dwordx4 a[48:51], v228, s[72:75], s84 offen sc1\n"
      "buffer_load_dwordx4 a[52:55], v229, s[72:75], s84 offen sc1\n"
      "TP6_%=:\n"
      "s_bitcmp0_b32 s86, 7\n"
      "s_cbranch_scc1 TP7_%=\n"
      "s_mov_b32 s84, 50176\n"
      "buffer_load_dwordx4 a[56:59], v228, s[72:75], s84 offen sc1\n"
      "buffer_load_dwordx4 a[60:63], v229, s[72:75], s84 offen sc1\n"
      "TP7_%=:\n"
      "s_and_b32 s60, s60, 0xff\n"
      "ds_read_b128 v[64:67], v244 offset:0\n"
      "ds_read_b128 v[68:71], v244 offset:128\n"
      "ds_read_b128 v[72:75], v245 offset:0\n"
      "ds_read_b128 v[76:79], v245 offset:128\n"
      "ds_read_b128 v[80:83], v244 offset:1792\n"
      "ds_read_b128 v[84:87], v244 offset:1920\n"
      "ds_read_b128 v[88:91], v245 offset:1792\n"
      "ds_read_b128 v[92:95], v245 offset:1920\n"
      "ds_read_b128 v[96:99], v244 offset:3584\n"
      "ds_read_b128 v[100:103], v244 offset:3712\n"
      "ds_read_b128 v[104:107], v245 offset:3584\n"
      "ds_read_b128 v[108:111], v245 offset:3712\n"
      "ds_read_b128 v[112:115], v244 offset:5376\n"
      "ds_read_b128 v[116:119], v244 offset:5504\n"
      "ds_read_b128 v[120:123], v245 offset:5376\n"
      "ds_read_b128 v[124:127], v245 offset:5504\n"
      "ds_read_b64 v[32:33], v243 offset:0\n"
      "ds_read_b64 v[36:37], v243 offset:64\n"
      "ds_read_b64 v[40:41], v243 offset:512\n"
      "ds_read_b64 v[44:45], v243 offset:576\n"
      "s_mov_b32 s85, 0x40000\n"
      "s_waitcnt lgkmcnt(0)\n"
      "TMA_%=:\n"
      "v_xor_b32 v243, s53, v65\n"
      "v_bitop3_b32 v243, v67, v177, v243 bitop3:0xbe\n"
      "v_bitop3_b32 v243, v69, v177, v243 bitop3:0xbe\n"
      "v_bitop3_b32 v243, v71, v177, v243 bitop3:0xbe\n"
      "v_bitop3_b32 v243, v73, v177, v243 bitop3:0xbe\n"
      "v_bitop3_b32 v243, v75, v177, v243 bitop3:0xbe\n"
      "v_bitop3_b32 v243, v77, v177, v243 bitop3:0xbe\n"
      "v_bitop3_b32 v243, v79, v177, v243 bitop3:0xbe\n"
      "v_bitop3_b32 v243, v81, v177, v243 bitop3:0xbe\n"
      "v_bitop3_b32 v243, v83, v177, v243 bitop3:0xbe\n"
      "v_bitop3_b32 v243, v85, v177, v243 bitop3:0xbe\n"
      "v_bitop3_b32 v243, v87, v177, v243 bitop3:0xbe\n"
      "v_bitop3_b32 v243, v89, v177, v243 bitop3:0xbe\n"
      "v_bitop3_b32 v243, v91, v177, v243 bitop3:0xbe\n"
      "v_bitop3_b32 v243, v93, v177, v243 bitop3:0xbe\n"
      "v_bitop3_b32 v243, v95, v177, v243 bitop3:0xbe\n"
      "v_bitop3_b32 v243, v97, v177, v243 bitop3:0xbe\n"
      "v_bitop3_b32 v243, v99, v177, v243 bitop3:0xbe\n"
      "v_bitop3_b32 v243, v101, v177, v243 bitop3:0xbe\n"
      "v_bitop3_b32 v243, v103, v177, v243 bitop3:0xbe\n"
      "v_bitop3_b32 v243, v105, v177, v243 bitop3:0xbe\n"
      "v_bitop3_b32 v243, v107, v177, v243 bitop3:0xbe\n"
      "v_bitop3_b32 v243, v109, v177, v243 bitop3:0xbe\n"
      "v_bitop3_b32 v243, v111, v177, v243 bitop3:0xbe\n"
      "v_bitop3_b32 v243, v113, v177, v243 bitop3:0xbe\n"
      "v_bitop3_b32 v243, v115, v177, v243 bitop3:0xbe\n"
      "v_bitop3_b32 v243, v117, v177, v243 bitop3:0xbe\n"
      "v_bitop3_b32 v243, v119, v177, v243 bitop3:0xbe\n"
      "v_bitop3_b32 v243, v121, v177, v243 bitop3:0xbe\n"
      "v_bitop3_b32 v243, v123, v177, v243 bitop3:0xbe\n"
      "v_bitop3_b32 v243, v125, v177, v243 bitop3:0xbe\n"
      "v_bitop3_b32 v243, v127, v177, v243 bitop3:0xbe\n"
      "v_cmp_ne_u32 vcc, 0, v243\n"
      "s_cbranch_vccz TMB_%=\n"
      "s_sub_u32 s85, s85, 1\n"
      "s_cbranch_scc1 TMB_%=\n"
      "s_sleep 1\n"
      "buffer_load_dwordx4 v[64:67], v223, s[72:75], s89 offen offset:0 sc1\n"
      "buffer_load_dwordx4 v[68:71], v223, s[72:75], s89 offen offset:128 sc1\n"
      "buffer_load_dwordx4 v[72:75], v224, s[72:75], s89 offen offset:0 sc1\n"
      "buffer_load_dwordx4 v[76:79], v224, s[72:75], s89 offen offset:128 sc1\n"
      "buffer_load_dwordx4 v[80:83], v223, s[72:75], s89 offen offset:1792 sc1\n"
      "buffer_load_dwordx4 v[84:87], v223, s[72:75], s89 offen offset:1920 sc1\n"
      "buffer_load_dwordx4 v[88:91], v224, s[72:75], s89 offen offset:1792 sc1\n"
      "buffer_load_dwordx4 v[92:95], v224, s[72:75], s89 offen offset:1920 sc1\n"
      "buffer_load_dwordx4 v[96:99], v223, s[72:75], s90 offen offset:0 sc1\n"
      "buffer_load_dwordx4 v[100:103], v223, s[72:75], s90 offen offset:128 sc1\n"
      "buffer_load_dwordx4 v[104:107], v224, s[72:75], s90 offen offset:0 sc1\n"
      "buffer_load_dwordx4 v[108:111], v224, s[72:75], s90 offen offset:128 sc1\n"
      "buffer_load_dwordx4 v[112:115], v223, s[72:75], s90 offen offset:1792 sc1\n"
      "buffer_load_dwordx4 v[116:119], v223, s[72:75], s90 offen offset:1920 sc1\n"
      "buffer_load_dwordx4 v[120:123], v224, s[72:75], s90 offen offset:1792 sc1\n"
      "buffer_load_dwordx4 v[124:127], v224, s[72:75], s90 offen offset:1920 sc1\n"
      "s_waitcnt vmcnt(0)\n"
      "s_branch TMA_%=\n"
      "TMB_%=:\n"
      "v_and_b32 v65, v253, v64\n"
      "v_lshlrev_b32 v64, 16, v64\n"
      "v_and_b32 v67, v253, v66\n"
      "v_lshlrev_b32 v66, 16, v66\n"
      "v_and_b32 v69, v253, v68\n"
      "v_lshlrev_b32 v68, 16, v68\n"
      "v_and_b32 v71, v253, v70\n"
      "v_lshlrev_b32 v70, 16, v70\n"
      "v_and_b32 v73, v253, v72\n"
      "v_lshlrev_b32 v72, 16, v72\n"
      "v_and_b32 v75, v253, v74\n"
      "v_lshlrev_b32 v74, 16, v74\n"
      "v_and_b32 v77, v253, v76\n"
      "v_lshlrev_b32 v76, 16, v76\n"
      "v_and_b32 v79, v253, v78\n"
      "v_lshlrev_b32 v78, 16, v78\n"
      "v_and_b32 v81, v253, v80\n"
      "v_lshlrev_b32 v80, 16, v80\n"
      "v_and_b32 v83, v253, v82\n"
      "v_lshlrev_b32 v82, 16, v82\n"
      "v_and_b32 v85, v253, v84\n"
      "v_lshlrev_b32 v84, 16, v84\n"
      "v_and_b32 v87, v253, v86\n"
      "v_lshlrev_b32 v86, 16, v86\n"
      "v_and_b32 v89, v253, v88\n"
      "v_lshlrev_b32 v88, 16, v88\n"
      "v_and_b32 v91, v253, v90\n"
      "v_lshlrev_b32 v90, 16, v90\n"
      "v_and_b32 v93, v253, v92\n"
      "v_lshlrev_b32 v92, 16, v92\n"
      "v_and_b32 v95, v253, v94\n"
      "v_lshlrev_b32 v94, 16, v94\n"
      "v_and_b32 v97, v253, v96\n"
      "v_lshlrev_b32 v96, 16, v96\n"
      "v_and_b32 v99, v253, v98\n"
      "v_lshlrev_b32 v98, 16, v98\n"
      "v_and_b32 v101, v253, v100\n"
      "v_lshlrev_b32 v100, 16, v100\n"
      "v_and_b32 v103, v253, v102\n"
      "v_lshlrev_b32 v102, 16, v102\n"
      "v_and_b32 v105, v253, v104\n"
      "v_lshlrev_b32 v104, 16, v104\n"
      "v_and_b32 v107, v253, v106\n"
      "v_lshlrev_b32 v106, 16, v106\n"
      "v_and_b32 v109, v253, v108\n"
      "v_lshlrev_b32 v108, 16, v108\n"
      "v_and_b32 v111, v253, v110\n"
      "v_lshlrev_b32 v110, 16, v110\n"
      "v_and_b32 v113, v253, v112\n"
      "v_lshlrev_b32 v112, 16, v112\n"
      "v_and_b32 v115, v253, v114\n"
      "v_lshlrev_b32 v114, 16, v114\n"
      "v_and_b32 v117, v253, v116\n"
      "v_lshlrev_b32 v116, 16, v116\n"
      "v_and_b32 v119, v253, v118\n"
      "v_lshlrev_b32 v118, 16, v118\n"
      "v_and_b32 v121, v253, v120\n"
      "v_lshlrev_b32 v120, 16, v120\n"
      "v_and_b32 v123, v253, v122\n"
      "v_lshlrev_b32 v122, 16, v122\n"
      "v_and_b32 v125, v253, v124\n"
      "v_lshlrev_b32 v124, 16, v124\n"
      "v_and_b32 v127, v253, v126\n"
      "v_lshlrev_b32 v126, 16, v126\n"
      "v_and_b32 v35, v253, v33\n"
      "v_lshlrev_b32 v34, 16, v33\n"
      "v_and_b32 v33, v253, v32\n"
      "v_lshlrev_b32 v32, 16, v32\n"
      "v_and_b32 v39, v253, v37\n"
      "v_lshlrev_b32 v38, 16, v37\n"
      "v_and_b32 v37, v253, v36\n"
      "v_lshlrev_b32 v36, 16, v36\n"
      "v_and_b32 v43, v253, v41\n"
      "v_lshlrev_b32 v42, 16, v41\n"
      "v_and_b32 v41, v253, v40\n"
      "v_lshlrev_b32 v40, 16, v40\n"
      "v_and_b32 v47, v253, v45\n"
      "v_lshlrev_b32 v46, 16, v45\n"
      "v_and_b32 v45, v253, v44\n"
      "v_lshlrev_b32 v44, 16, v44\n"
      "v_mul_f32 v40, s88, v40\n"
      "v_mul_f32 v41, s88, v41\n"
      "v_mul_f32 v42, s88, v42\n"
      "v_mul_f32 v43, s88, v43\n"
      "v_mul_f32 v44, s88, v44\n"
      "v_mul_f32 v45, s88, v45\n"
      "v_mul_f32 v46, s88, v46\n"
      "v_mul_f32 v47, s88, v47\n"
      "v_pk_mul_f32 v[144:145], v[80:81], v[48:49]\n"
      "v_pk_mul_f32 v[146:147], v[82:83], v[50:51]\n"
      "v_pk_mul_f32 v[148:149], v[84:85], v[52:53]\n"
      "v_pk_mul_f32 v[150:151], v[86:87], v[54:55]\n"
      "v_pk_mul_f32 v[152:153], v[88:89], v[56:57]\n"
      "v_pk_mul_f32 v[154:155], v[90:91], v[58:59]\n"
      "v_pk_mul_f32 v[156:157], v[92:93], v[60:61]\n"
      "v_pk_mul_f32 v[158:159], v[94:95], v[62:63]\n"
      "v_pk_add_f32 v[244:245], v[144:145], v[146:147]\n"
      "v_pk_add_f32 v[246:247], v[148:149], v[150:151]\n"
      "v_pk_add_f32 v[248:249], v[152:153], v[154:155]\n"
      "v_pk_add_f32 v[250:251], v[156:157], v[158:159]\n"
      "v_pk_add_f32 v[244:245], v[244:245], v[246:247]\n"
      "v_pk_add_f32 v[248:249], v[248:249], v[250:251]\n"
      "v_add_f32 v244, v244, v245\n"
      "v_add_f32 v248, v248, v249\n"
      "v_fma_f32 v219, v248, 1.0, v244\n"
      "v_pk_mul_f32 v[48:49], v[48:49], v[96:97]\n"
      "v_pk_mul_f32 v[50:51], v[50:51], v[98:99]\n"
      "v_pk_mul_f32 v[52:53], v[52:53], v[100:101]\n"
      "v_pk_mul_f32 v[54:55], v[54:55], v[102:103]\n"
      "v_pk_mul_f32 v[56:57], v[56:57], v[104:105]\n"
      "v_pk_mul_f32 v[58:59], v[58:59], v[106:107]\n"
      "v_pk_mul_f32 v[60:61], v[60:61], v[108:109]\n"
      "v_pk_mul_f32 v[62:63], v[62:63], v[110:111]\n"
      "s_mov_b32 s85, 0x20000\n"
      "s_cmp_eq_u32 s60, 0xff\n"
      "s_cbranch_scc1 TFB_%=\n"
      "TFA_%=:\n"
      "buffer_load_dword v10, v255, s[72:75], s59 offen sc1\n"
      "s_waitcnt vmcnt(1)\n"
      "v_cmp_eq_u32 vcc, s53, v252\n"
      "s_nop 4\n"
      "s_andn2_b32 s86, vcc_lo, s60\n"
      "s_and_b32 s86, s86, 0xff\n"
      "s_cbranch_scc0 TQS_%=\n"
      "s_andn2_b32 s86, vcc_lo, s60\n"
      "s_or_b32 s60, s60, vcc_lo\n"
      "s_bitcmp0_b32 s86, 0\n"
      "s_cbranch_scc1 TQ0_%=\n"
      "s_mov_b32 s84, 0\n"
      "buffer_load_dwordx4 a[0:3], v228, s[72:75], s84 offen sc1\n"
      "buffer_load_dwordx4 a[4:7], v229, s[72:75], s84 offen sc1\n"
      "TQ0_%=:\n"
      "s_bitcmp0_b32 s86, 1\n"
      "s_cbranch_scc1 TQ1_%=\n"
      "s_mov_b32 s84, 7168\n"
      "buffer_load_dwordx4 a[8:11], v228, s[72:75], s84 offen sc1\n"
      "buffer_load_dwordx4 a[12:15], v229, s[72:75], s84 offen sc1\n"
      "TQ1_%=:\n"
      "s_bitcmp0_b32 s86, 2\n"
      "s_cbranch_scc1 TQ2_%=\n"
      "s_mov_b32 s84, 14336\n"
      "buffer_load_dwordx4 a[16:19], v228, s[72:75], s84 offen sc1\n"
      "buffer_load_dwordx4 a[20:23], v229, s[72:75], s84 offen sc1\n"
      "TQ2_%=:\n"
      "s_bitcmp0_b32 s86, 3\n"
      "s_cbranch_scc1 TQ3_%=\n"
      "s_mov_b32 s84, 21504\n"
      "buffer_load_dwordx4 a[24:27], v228, s[72:75], s84 offen sc1\n"
      "buffer_load_dwordx4 a[28:31], v229, s[72:75], s84 offen sc1\n"
      "TQ3_%=:\n"
      "s_bitcmp0_b32 s86, 4\n"
      "s_cbranch_scc1 TQ4_%=\n"
      "s_mov_b32 s84, 28672\n"
      "buffer_load_dwordx4 a[32:35], v228, s[72:75], s84 offen sc1\n"
      "buffer_load_dwordx4 a[36:39], v229, s[72:75], s84 offen sc1\n"
      "TQ4_%=:\n"
      "s_bitcmp0_b32 s86, 5\n"
      "s_cbranch_scc1 TQ5_%=\n"
      "s_mov_b32 s84, 35840\n"
      "buffer_load_dwordx4 a[40:43], v228, s[72:75], s84 offen sc1\n"
      "buffer_load_dwordx4 a[44:47], v229, s[72:75], s84 offen sc1\n"
      "TQ5_%=:\n"
      "s_bitcmp0_b32 s86, 6\n"
      "s_cbranch_scc1 TQ6_%=\n"
      "s_mov_b32 s84, 43008\n"
      "buffer_load_dwordx4 a[48:51], v228, s[72:75], s84 offen sc1\n"
      "buffer_load_dwordx4 a[52:55], v229, s[72:75], s84 offen sc1\n"
      "TQ6_%=:\n"
      "s_bitcmp0_b32 s86, 7\n"
      "s_cbranch_scc1 TQ7_%=\n"
      "s_mov_b32 s84, 50176\n"
      "buffer_load_dwordx4 a[56:59], v228, s[72:75], s84 offen sc1\n"
      "buffer_load_dwordx4 a[60:63], v229, s[72:75], s84 offen sc1\n"
      "TQ7_%=:\n"
      "s_and_b32 s60, s60, 0xff\n"
      "s_cmp_eq_u32 s60, 0xff\n"
      "s_cbranch_scc1 TFB_%=\n"
      "TQS_%=:\n"
      "buffer_load_dword v252, v255, s[72:75], s59 offen sc1\n"
      "s_waitcnt vmcnt(1)\n"
      "v_cmp_eq_u32 vcc, s53, v10\n"
      "s_nop 4\n"
      "s_andn2_b32 s86, vcc_lo, s60\n"
      "s_and_b32 s86, s86, 0xff\n"
      "s_cbranch_scc0 TRS_%=\n"
      "s_andn2_b32 s86, vcc_lo, s60\n"
      "s_or_b32 s60, s60, vcc_lo\n"
      "s_bitcmp0_b32 s86, 0\n"
      "s_cbranch_scc1 TR0_%=\n"
      "s_mov_b32 s84, 0\n"
      "buffer_load_dwordx4 a[0:3], v228, s[72:75], s84 offen sc1\n"
      "buffer_load_dwordx4 a[4:7], v229, s[72:75], s84 offen sc1\n"
      "TR0_%=:\n"
      "s_bitcmp0_b32 s86, 1\n"
      "s_cbranch_scc1 TR1_%=\n"
      "s_mov_b32 s84, 7168\n"
      "buffer_load_dwordx4 a[8:11], v228, s[72:75], s84 offen sc1\n"
      "buffer_load_dwordx4 a[12:15], v229, s[72:75], s84 offen sc1\n"
      "TR1_%=:\n"
      "s_bitcmp0_b32 s86, 2\n"
      "s_cbranch_scc1 TR2_%=\n"
      "s_mov_b32 s84, 14336\n"
      "buffer_load_dwordx4 a[16:19], v228, s[72:75], s84 offen sc1\n"
      "buffer_load_dwordx4 a[20:23], v229, s[72:75], s84 offen sc1\n"
      "TR2_%=:\n"
      "s_bitcmp0_b32 s86, 3\n"
      "s_cbranch_scc1 TR3_%=\n"
      "s_mov_b32 s84, 21504\n"
      "buffer_load_dwordx4 a[24:27], v228, s[72:75], s84 offen sc1\n"
      "buffer_load_dwordx4 a[28:31], v229, s[72:75], s84 offen sc1\n"
      "TR3_%=:\n"
      "s_bitcmp0_b32 s86, 4\n"
      "s_cbranch_scc1 TR4_%=\n"
      "s_mov_b32 s84, 28672\n"
      "buffer_load_dwordx4 a[32:35], v228, s[72:75], s84 offen sc1\n"
      "buffer_load_dwordx4 a[36:39], v229, s[72:75], s84 offen sc1\n"
      "TR4_%=:\n"
      "s_bitcmp0_b32 s86, 5\n"
      "s_cbranch_scc1 TR5_%=\n"
      "s_mov_b32 s84, 35840\n"
      "buffer_load_dwordx4 a[40:43], v228, s[72:75], s84 offen sc1\n"
      "buffer_load_dwordx4 a[44:47], v229, s[72:75], s84 offen sc1\n"
      "TR5_%=:\n"
      "s_bitcmp0_b32 s86, 6\n"
      "s_cbranch_scc1 TR6_%=\n"
      "s_mov_b32 s84, 43008\n"
      "buffer_load_dwordx4 a[48:51], v228, s[72:75], s84 offen sc1\n"
      "buffer_load_dwordx4 a[52:55], v229, s[72:75], s84 offen sc1\n"
      "TR6_%=:\n"
      "s_bitcmp0_b32 s86, 7\n"
      "s_cbranch_scc1 TR7_%=\n"
      "s_mov_b32 s84, 50176\n"
      "buffer_load_dwordx4 a[56:59], v228, s[72:75], s84 offen sc1\n"
      "buffer_load_dwordx4 a[60:63], v229, s[72:75], s84 offen sc1\n"
      "TR7_%=:\n"
      "s_and_b32 s60, s60, 0xff\n"
      "s_cmp_eq_u32 s60, 0xff\n"
      "s_cbranch_scc1 TFB_%=\n"
      "TRS_%=:\n"
      "s_sub_u32 s85, s85, 1\n"
      "s_cbranch_scc0 TFA_%=\n"
      "TFB_%=:\n"
      "s_waitcnt vmcnt(0)\n"
      "v_mfma_f32_16x16x32_bf16 v[128:131], v[192:195], a[0:3], 0\n"
      "v_mfma_f32_16x16x32_bf16 v[132:135], v[196:199], a[0:3], 0\n"
      "v_mfma_f32_16x16x32_bf16 v[136:139], v[192:195], a[4:7], 0\n"
      "v_mfma_f32_16x16x32_bf16 v[140:143], v[196:199], a[4:7], 0\n"
      "v_mfma_f32_16x16x32_bf16 v[128:131], v[192:195], a[8:11], v[128:131]\n"
      "v_mfma_f32_16x16x32_bf16 v[132:135], v[196:199], a[8:11], v[132:135]\n"
      "v_mfma_f32_16x16x32_bf16 v[136:139], v[192:195], a[12:15], v[136:139]\n"
      "v_mfma_f32_16x16x32_bf16 v[140:143], v[196:199], a[12:15], v[140:143]\n"
      "v_mfma_f32_16x16x32_bf16 v[128:131], v[192:195], a[16:19], v[128:131]\n"
      "v_mfma_f32_16x16x32_bf16 v[132:135], v[196:199], a[16:19], v[132:135]\n"
      "v_mfma_f32_16x16x32_bf16 v[136:139], v[192:195], a[20:23], v[136:139]\n"
      "v_mfma_f32_16x16x32_bf16 v[140:143], v[196:199], a[20:23], v[140:143]\n"
      "v_mfma_f32_16x16x32_bf16 v[128:131], v[192:195], a[24:27], v[128:131]\n"
      "v_mfma_f32_16x16x32_bf16 v[132:135], v[196:199], a[24:27], v[132:135]\n"
      "v_mfma_f32_16x16x32_bf16 v[136:139], v[192:195], a[28:31], v[136:139]\n"
      "v_mfma_f32_16x16x32_bf16 v[140:143], v[196:199], a[28:31], v[140:143]\n"
      "v_mfma_f32_16x16x32_bf16 v[128:131], v[192:195], a[32:35], v[128:131]\n"
      "v_mfma_f32_16x16x32_bf16 v[132:135], v[196:199], a[32:35], v[132:135]\n"
      "v_mfma_f32_16x16x32_bf16 v[136:139], v[192:195], a[36:39], v[136:139]\n"
      "v_mfma_f32_16x16x32_bf16 v[140:143], v[196:199], a[36:39], v[140:143]\n"
      "v_mfma_f32_16x16x32_bf16 v[128:131], v[192:195], a[40:43], v[128:131]\n"
      "v_mfma_f32_16x16x32_bf16 v[132:135], v[196:199], a[40:43], v[132:135]\n"
      "v_mfma_f32_16x16x32_bf16 v[136:139], v[192:195], a[44:47], v[136:139]\n"
      "v_mfma_f32_16x16x32_bf16 v[140:143], v[196:199], a[44:47], v[140:143]\n"
      "v_mfma_f32_16x16x32_bf16 v[128:131], v[192:195], a[48:51], v[128:131]\n"
      "v_mfma_f32_16x16x32_bf16 v[132:135], v[196:199], a[48:51], v[132:135]\n"
      "v_mfma_f32_16x16x32_bf16 v[136:139], v[192:195], a[52:55], v[136:139]\n"
      "v_mfma_f32_16x16x32_bf16 v[140:143], v[196:199], a[52:55], v[140:143]\n"
      "v_mfma_f32_16x16x32_bf16 v[128:131], v[192:195], a[56:59], v[128:131]\n"
      "v_mfma_f32_16x16x32_bf16 v[132:135], v[196:199], a[56:59], v[132:135]\n"
      "v_mfma_f32_16x16x32_bf16 v[136:139], v[192:195], a[60:63], v[136:139]\n"
      "v_mfma_f32_16x16x32_bf16 v[140:143], v[196:199], a[60:63], v[140:143]\n"
      "s_waitcnt vmcnt(0)\n"
      "s_nop 7\n"
      "v_cvt_pk_bf16_f32 v128, 0, v128\n"
      "v_cvt_pk_bf16_f32 v129, 0, v129\n"
      "v_cvt_pk_bf16_f32 v130, 0, v130\n"
      "v_cvt_pk_bf16_f32 v131, 0, v131\n"
      "v_cvt_pk_bf16_f32 v132, 0, v132\n"
      "v_cvt_pk_bf16_f32 v133, 0, v133\n"
      "v_cvt_pk_bf16_f32 v134, 0, v134\n"
      "v_cvt_pk_bf16_f32 v135, 0, v135\n"
      "v_cvt_pk_bf16_f32 v136, 0, v136\n"
      "v_cvt_pk_bf16_f32 v137, 0, v137\n"
      "v_cvt_pk_bf16_f32 v138, 0, v138\n"
      "v_cvt_pk_bf16_f32 v139, 0, v139\n"
      "v_cvt_pk_bf16_f32 v140, 0, v140\n"
      "v_cvt_pk_bf16_f32 v141, 0, v141\n"
      "v_cvt_pk_bf16_f32 v142, 0, v142\n"
      "v_cvt_pk_bf16_f32 v143, 0, v143\n"
      "v_pk_mul_f32 v[128:129], v[128:129], v[64:65]\n"
      "v_pk_mul_f32 v[130:131], v[130:131], v[66:67]\n"
      "v_pk_mul_f32 v[132:133], v[132:133], v[68:69]\n"
      "v_pk_mul_f32 v[134:135], v[134:135], v[70:71]\n"
      "v_pk_mul_f32 v[136:137], v[136:137], v[72:73]\n"
      "v_pk_mul_f32 v[138:139], v[138:139], v[74:75]\n"
      "v_pk_mul_f32 v[140:141], v[140:141], v[76:77]\n"
      "v_pk_mul_f32 v[142:143], v[142:143], v[78:79]\n"
      "v_cvt_pk_bf16_f32 v128, 0, v128\n"
      "v_cvt_pk_bf16_f32 v129, 0, v129\n"
      "v_cvt_pk_bf16_f32 v130, 0, v130\n"
      "v_cvt_pk_bf16_f32 v131, 0, v131\n"
      "v_cvt_pk_bf16_f32 v132, 0, v132\n"
      "v_cvt_pk_bf16_f32 v133, 0, v133\n"
      "v_cvt_pk_bf16_f32 v134, 0, v134\n"
      "v_cvt_pk_bf16_f32 v135, 0, v135\n"
      "v_cvt_pk_bf16_f32 v136, 0, v136\n"
      "v_cvt_pk_bf16_f32 v137, 0, v137\n"
      "v_cvt_pk_bf16_f32 v138, 0, v138\n"
      "v_cvt_pk_bf16_f32 v139, 0, v139\n"
      "v_cvt_pk_bf16_f32 v140, 0, v140\n"
      "v_cvt_pk_bf16_f32 v141, 0, v141\n"
      "v_cvt_pk_bf16_f32 v142, 0, v142\n"
      "v_cvt_pk_bf16_f32 v143, 0, v143\n"
      "v_pk_add_f32 v[128:129], v[128:129], v[32:33]\n"
      "v_pk_add_f32 v[130:131], v[130:131], v[34:35]\n"
      "v_pk_add_f32 v[132:133], v[132:133], v[36:37]\n"
      "v_pk_add_f32 v[134:135], v[134:135], v[38:39]\n"
      "v_pk_add_f32 v[136:137], v[136:137], v[40:41]\n"
      "v_pk_add_f32 v[138:139], v[138:139], v[42:43]\n"
      "v_pk_add_f32 v[140:141], v[140:141], v[44:45]\n"
      "v_pk_add_f32 v[142:143], v[142:143], v[46:47]\n"
      "v_cvt_pk_bf16_f32 v128, 0, v128\n"
      "v_cvt_pk_bf16_f32 v129, 0, v129\n"
      "v_cvt_pk_bf16_f32 v130, 0, v130\n"
      "v_cvt_pk_bf16_f32 v131, 0, v131\n"
      "v_cvt_pk_bf16_f32 v132, 0, v132\n"
      "v_cvt_pk_bf16_f32 v133, 0, v133\n"
      "v_cvt_pk_bf16_f32 v134, 0, v134\n"
      "v_cvt_pk_bf16_f32 v135, 0, v135\n"
      "v_cvt_pk_bf16_f32 v136, 0, v136\n"
      "v_cvt_pk_bf16_f32 v137, 0, v137\n"
      "v_cvt_pk_bf16_f32 v138, 0, v138\n"
      "v_cvt_pk_bf16_f32 v139, 0, v139\n"
      "v_cvt_pk_bf16_f32 v140, 0, v140\n"
      "v_cvt_pk_bf16_f32 v141, 0, v141\n"
      "v_cvt_pk_bf16_f32 v142, 0, v142\n"
      "v_cvt_pk_bf16_f32 v143, 0, v143\n"
      "v_pk_add_f32 v[244:245], v[128:129], v[130:131]\n"
      "v_pk_add_f32 v[246:247], v[132:133], v[134:135]\n"
      "v_pk_add_f32 v[248:249], v[136:137], v[138:139]\n"
      "v_pk_add_f32 v[250:251], v[140:141], v[142:143]\n"
      "v_pk_add_f32 v[244:245], v[244:245], v[246:247]\n"
      "v_pk_add_f32 v[248:249], v[248:249], v[250:251]\n"
      "v_add_f32 v244, v244, v245\n"
      "v_add_f32 v248, v248, v249\n"
      "v_fma_f32 v238, v248, 1.0, v244\n"
      "s_nop 0\n"
      "v_add_f32_dpp v219, v219, v219 row_ror:4 row_mask:0xf bank_mask:0xf\n"
      "v_add_f32_dpp v238, v238, v238 row_ror:4 row_mask:0xf bank_mask:0xf\n"
      "s_nop 0\n"
      "v_add_f32_dpp v219, v219, v219 row_ror:8 row_mask:0xf bank_mask:0xf\n"
      "v_add_f32_dpp v238, v238, v238 row_ror:8 row_mask:0xf bank_mask:0xf\n"
      "s_nop 0\n"
      "v_mov_b32 v8, v219\n"
      "v_mov_b32 v9, v238\n"
      "s_nop 0\n"
      "v_permlane32_swap_b32 v219, v8\n"
      "v_permlane32_swap_b32 v238, v9\n"
      "v_add_f32 v219, v219, v8\n"
      "v_add_f32 v238, v238, v9\n"
      "ds_write_b32 v237, v219 offset:0\n"
      "ds_write_b32 v237, v238 offset:128\n"
      "s_waitcnt lgkmcnt(0)\n"
      "s_barrier\n"
      "ds_read_b128 v[0:3], v241 offset:0\n"
      "ds_read_b128 v[4:7], v241 offset:128\n"
      "s_waitcnt lgkmcnt(0)\n"
      "v_pk_add_f32 v[0:1], v[0:1], v[2:3]\n"
      "v_pk_add_f32 v[4:5], v[4:5], v[6:7]\n"
      "v_add_f32 v219, v0, v1\n"
      "v_add_f32 v238, v4, v5\n"
      "v_mul_f32 v238, 0xbb124925, v238\n"
      "v_pk_add_f32 v[144:145], v[128:129], v[238:239] op_sel:[0,0] op_sel_hi:[1,0]\n"
      "v_pk_add_f32 v[146:147], v[130:131], v[238:239] op_sel:[0,0] op_sel_hi:[1,0]\n"
      "v_pk_add_f32 v[148:149], v[132:133], v[238:239] op_sel:[0,0] op_sel_hi:[1,0]\n"
      "v_pk_add_f32 v[150:151], v[134:135], v[238:239] op_sel:[0,0] op_sel_hi:[1,0]\n"
      "v_pk_add_f32 v[152:153], v[136:137], v[238:239] op_sel:[0,0] op_sel_hi:[1,0]\n"
      "v_pk_add_f32 v[154:155], v[138:139], v[238:239] op_sel:[0,0] op_sel_hi:[1,0]\n"
      "v_pk_add_f32 v[156:157], v[140:141], v[238:239] op_sel:[0,0] op_sel_hi:[1,0]\n"
      "v_pk_add_f32 v[158:159], v[142:143], v[238:239] op_sel:[0,0] op_sel_hi:[1,0]\n"
      "v_pk_mul_f32 v[144:145], v[144:145], v[144:145]\n"
      "v_pk_mul_f32 v[146:147], v[146:147], v[146:147]\n"
      "v_pk_mul_f32 v[148:149], v[148:149], v[148:149]\n"
      "v_pk_mul_f32 v[150:151], v[150:151], v[150:151]\n"
      "v_pk_mul_f32 v[152:153], v[152:153], v[152:153]\n"
      "v_pk_mul_f32 v[154:155], v[154:155], v[154:155]\n"
      "v_pk_mul_f32 v[156:157], v[156:157], v[156:157]\n"
      "v_pk_mul_f32 v[158:159], v[158:159], v[158:159]\n"
      "v_cvt_pk_bf16_f32 v238, 0, v238\n"
      "v_pk_add_f32 v[244:245], v[144:145], v[146:147]\n"
      "v_pk_add_f32 v[246:247], v[148:149], v[150:151]\n"
      "v_pk_add_f32 v[248:249], v[152:153], v[154:155]\n"
      "v_pk_add_f32 v[250:251], v[156:157], v[158:159]\n"
      "v_pk_add_f32 v[244:245], v[244:245], v[246:247]\n"
      "v_pk_add_f32 v[248:249], v[248:249], v[250:251]\n"
      "v_add_f32 v244, v244, v245\n"
      "v_add_f32 v248, v248, v249\n"
      "v_fma_f32 v239, v248, s88, v244\n"
      "v_pk_add_f32 v[160:161], v[128:129], v[238:239] op_sel:[0,0] op_sel_hi:[1,0]\n"
      "v_pk_add_f32 v[162:163], v[130:131], v[238:239] op_sel:[0,0] op_sel_hi:[1,0]\n"
      "v_pk_add_f32 v[164:165], v[132:133], v[238:239] op_sel:[0,0] op_sel_hi:[1,0]\n"
      "v_pk_add_f32 v[166:167], v[134:135], v[238:239] op_sel:[0,0] op_sel_hi:[1,0]\n"
      "v_pk_add_f32 v[168:169], v[136:137], v[238:239] op_sel:[0,0] op_sel_hi:[1,0]\n"
      "v_pk_add_f32 v[170:171], v[138:139], v[238:239] op_sel:[0,0] op_sel_hi:[1,0]\n"
      "v_pk_add_f32 v[172:173], v[140:141], v[238:239] op_sel:[0,0] op_sel_hi:[1,0]\n"
      "v_pk_add_f32 v[174:175], v[142:143], v[238:239] op_sel:[0,0] op_sel_hi:[1,0]\n"
      "v_pk_mul_f32 v[144:145], v[160:161], v[48:49]\n"
      "v_pk_mul_f32 v[146:147], v[162:163], v[50:51]\n"
      "v_pk_mul_f32 v[148:149], v[164:165], v[52:53]\n"
      "v_pk_mul_f32 v[150:151], v[166:167], v[54:55]\n"
      "v_pk_mul_f32 v[152:153], v[168:169], v[56:57]\n"
      "v_pk_mul_f32 v[154:155], v[170:171], v[58:59]\n"
      "v_pk_mul_f32 v[156:157], v[172:173], v[60:61]\n"
      "v_pk_mul_f32 v[158:159], v[174:175], v[62:63]\n"
      "v_pk_add_f32 v[244:245], v[144:145], v[146:147]\n"
      "v_pk_add_f32 v[246:247], v[148:149], v[150:151]\n"
      "v_pk_add_f32 v[248:249], v[152:153], v[154:155]\n"
      "v_pk_add_f32 v[250:251], v[156:157], v[158:159]\n"
      "v_pk_add_f32 v[244:245], v[244:245], v[246:247]\n"
      "v_pk_add_f32 v[248:249], v[248:249], v[250:251]\n"
      "v_add_f32 v244, v244, v245\n"
      "v_add_f32 v248, v248, v249\n"
      "v_fma_f32 v243, v248, 1.0, v244\n"
      "s_nop 0\n"
      "v_add_f32_dpp v239, v239, v239 row_ror:4 row_mask:0xf bank_mask:0xf\n"
      "v_add_f32_dpp v243, v243, v243 row_ror:4 row_mask:0xf bank_mask:0xf\n"
      "s_nop 0\n"
      "v_add_f32_dpp v239, v239, v239 row_ror:8 row_mask:0xf bank_mask:0xf\n"
      "v_add_f32_dpp v243, v243, v243 row_ror:8 row_mask:0xf bank_mask:0xf\n"
      "s_nop 0\n"
      "v_mov_b32 v8, v239\n"
      "v_mov_b32 v9, v243\n"
      "s_nop 0\n"
      "v_permlane32_swap_b32 v239, v8\n"
      "v_permlane32_swap_b32 v243, v9\n"
      "v_add_f32 v239, v239, v8\n"
      "v_add_f32 v243, v243, v9\n"
      "ds_write_b32 v237, v239 offset:512\n"
      "ds_write_b32 v237, v243 offset:640\n"
      "s_waitcnt lgkmcnt(0)\n"
      "s_barrier\n"
      "ds_read_b128 v[0:3], v241 offset:512\n"
      "ds_read_b128 v[4:7], v241 offset:640\n"
      "v_cvt_pk_bf16_f32 v160, 0, v160\n"
      "v_cvt_pk_bf16_f32 v161, 0, v161\n"
      "v_cvt_pk_bf16_f32 v162, 0, v162\n"
      "v_cvt_pk_bf16_f32 v163, 0, v163\n"
      "v_cvt_pk_bf16_f32 v164, 0, v164\n"
      "v_cvt_pk_bf16_f32 v165, 0, v165\n"
      "v_cvt_pk_bf16_f32 v166, 0, v166\n"
      "v_cvt_pk_bf16_f32 v167, 0, v167\n"
      "v_cvt_pk_bf16_f32 v168, 0, v168\n"
      "v_cvt_pk_bf16_f32 v169, 0, v169\n"
      "v_cvt_pk_bf16_f32 v170, 0, v170\n"
      "v_cvt_pk_bf16_f32 v171, 0, v171\n"
      "v_cvt_pk_bf16_f32 v172, 0, v172\n"
      "v_cvt_pk_bf16_f32 v173, 0, v173\n"
      "v_cvt_pk_bf16_f32 v174, 0, v174\n"
      "v_cvt_pk_bf16_f32 v175, 0, v175\n"
      "s_waitcnt lgkmcnt(0)\n"
      "v_pk_add_f32 v[0:1], v[0:1], v[2:3]\n"
      "v_pk_add_f32 v[4:5], v[4:5], v[6:7]\n"
      "v_add_f32 v239, v0, v1\n"
      "v_add_f32 v243, v4, v5\n"
      "v_mul_f32 v239, 0x3b124925, v239\n"
      "v_cvt_pk_bf16_f32 v239, 0, v239\n"
      "v_add_f32 v239, 0x3727c5ac, v239\n"
      "v_cvt_pk_bf16_f32 v239, 0, v239\n"
      "v_sqrt_f32 v239, v239\n"
      "s_nop 0\n"
      "v_cvt_pk_bf16_f32 v239, 0, v239\n"
      "v_rcp_f32 v239, v239\n"
      "s_nop 0\n"
      "v_fma_f32 v243, v243, v239, v219\n"
      "v_cvt_pk_bf16_f32 v243, 0, v243\n"
      "v_ashrrev_i32 v252, 31, v243\n"
      "v_or_b32 v252, 0x80000000, v252\n"
      "v_xor_b32 v243, v243, v252\n"
      "v_and_or_b32 v218, v243, v253, v220\n"
      "s_mul_i32 s84, s52, 3584\n"
      "s_add_u32 s84, s84, 196608\n"
      "v_max_u32_dpp v244, v218, v218 row_ror:1 row_mask:0xf bank_mask:0xf\n"
      "s_lshl_b32 s86, s52, 3\n"
      "s_add_u32 s86, s86, 147456\n"
      "v_max_u32_dpp v244, v244, v244 row_ror:2 row_mask:0xf bank_mask:0xf\n"
      "v_mov_b32 v252, v244\n"
      "v_mov_b32 v245, s53\n"
      "v_mov_b32 v243, 1073741824\n"
      "v_permlane16_swap_b32 v244, v252\n"
      "v_max_u32 v244, v244, v252\n"
      "v_cmp_eq_u32 vcc, v244, v218\n"
      "buffer_store_dwordx2 v[244:245], v240, s[72:75], s86 offen sc1\n"
      "s_nop 0\n"
      "v_cndmask_b32 v223, v243, v223, vcc\n"
      "v_cndmask_b32 v227, v243, v227, vcc\n"
      "v_pk_mul_f32 v[160:161], v[160:161], v[238:239] op_sel:[0,1] op_sel_hi:[1,1]\n"
      "v_pk_mul_f32 v[162:163], v[162:163], v[238:239] op_sel:[0,1] op_sel_hi:[1,1]\n"
      "v_pk_mul_f32 v[164:165], v[164:165], v[238:239] op_sel:[0,1] op_sel_hi:[1,1]\n"
      "v_pk_mul_f32 v[166:167], v[166:167], v[238:239] op_sel:[0,1] op_sel_hi:[1,1]\n"
      "v_pk_mul_f32 v[168:169], v[168:169], v[238:239] op_sel:[0,1] op_sel_hi:[1,1]\n"
      "v_pk_mul_f32 v[170:171], v[170:171], v[238:239] op_sel:[0,1] op_sel_hi:[1,1]\n"
      "v_pk_mul_f32 v[172:173], v[172:173], v[238:239] op_sel:[0,1] op_sel_hi:[1,1]\n"
      "v_pk_mul_f32 v[174:175], v[174:175], v[238:239] op_sel:[0,1] op_sel_hi:[1,1]\n"
      "v_cvt_pk_bf16_f32 v160, 0, v160\n"
      "v_cvt_pk_bf16_f32 v161, 0, v161\n"
      "v_cvt_pk_bf16_f32 v162, 0, v162\n"
      "v_cvt_pk_bf16_f32 v163, 0, v163\n"
      "v_cvt_pk_bf16_f32 v164, 0, v164\n"
      "v_cvt_pk_bf16_f32 v165, 0, v165\n"
      "v_cvt_pk_bf16_f32 v166, 0, v166\n"
      "v_cvt_pk_bf16_f32 v167, 0, v167\n"
      "v_cvt_pk_bf16_f32 v168, 0, v168\n"
      "v_cvt_pk_bf16_f32 v169, 0, v169\n"
      "v_cvt_pk_bf16_f32 v170, 0, v170\n"
      "v_cvt_pk_bf16_f32 v171, 0, v171\n"
      "v_cvt_pk_bf16_f32 v172, 0, v172\n"
      "v_cvt_pk_bf16_f32 v173, 0, v173\n"
      "v_cvt_pk_bf16_f32 v174, 0, v174\n"
      "v_cvt_pk_bf16_f32 v175, 0, v175\n"
      "v_pk_mul_f32 v[160:161], v[160:161], v[96:97]\n"
      "v_pk_mul_f32 v[162:163], v[162:163], v[98:99]\n"
      "v_pk_mul_f32 v[164:165], v[164:165], v[100:101]\n"
      "v_pk_mul_f32 v[166:167], v[166:167], v[102:103]\n"
      "v_pk_mul_f32 v[168:169], v[168:169], v[104:105]\n"
      "v_pk_mul_f32 v[170:171], v[170:171], v[106:107]\n"
      "v_pk_mul_f32 v[172:173], v[172:173], v[108:109]\n"
      "v_pk_mul_f32 v[174:175], v[174:175], v[110:111]\n"
      "v_cvt_pk_bf16_f32 v160, 0, v160\n"
      "v_cvt_pk_bf16_f32 v161, 0, v161\n"
      "v_cvt_pk_bf16_f32 v162, 0, v162\n"
      "v_cvt_pk_bf16_f32 v163, 0, v163\n"
      "v_cvt_pk_bf16_f32 v164, 0, v164\n"
      "v_cvt_pk_bf16_f32 v165, 0, v165\n"
      "v_cvt_pk_bf16_f32 v166, 0, v166\n"
      "v_cvt_pk_bf16_f32 v167, 0, v167\n"
      "v_cvt_pk_bf16_f32 v168, 0, v168\n"
      "v_cvt_pk_bf16_f32 v169, 0, v169\n"
      "v_cvt_pk_bf16_f32 v170, 0, v170\n"
      "v_cvt_pk_bf16_f32 v171, 0, v171\n"
      "v_cvt_pk_bf16_f32 v172, 0, v172\n"
      "v_cvt_pk_bf16_f32 v173, 0, v173\n"
      "v_cvt_pk_bf16_f32 v174, 0, v174\n"
      "v_cvt_pk_bf16_f32 v175, 0, v175\n"
      "v_pk_add_f32 v[160:161], v[160:161], v[80:81]\n"
      "v_pk_add_f32 v[162:163], v[162:163], v[82:83]\n"
      "v_pk_add_f32 v[164:165], v[164:165], v[84:85]\n"
      "v_pk_add_f32 v[166:167], v[166:167], v[86:87]\n"
      "v_pk_add_f32 v[168:169], v[168:169], v[88:89]\n"
      "v_pk_add_f32 v[170:171], v[170:171], v[90:91]\n"
      "v_pk_add_f32 v[172:173], v[172:173], v[92:93]\n"
      "v_pk_add_f32 v[174:175], v[174:175], v[94:95]\n"
      "v_cvt_pk_bf16_f32 v176, v160, v161\n"
      "v_cvt_pk_bf16_f32 v178, v162, v163\n"
      "v_cvt_pk_bf16_f32 v180, v164, v165\n"
      "v_cvt_pk_bf16_f32 v182, v166, v167\n"
      "v_cvt_pk_bf16_f32 v184, v168, v169\n"
      "v_cvt_pk_bf16_f32 v186, v170, v171\n"
      "v_cvt_pk_bf16_f32 v188, v172, v173\n"
      "v_cvt_pk_bf16_f32 v190, v174, v175\n"
      "buffer_store_dwordx4 v[176:179], v223, s[72:75], s84 offen offset:0 sc1\n"
      "buffer_store_dwordx4 v[180:183], v223, s[72:75], s84 offen offset:128 sc1\n"
      "buffer_store_dwordx4 v[184:187], v227, s[72:75], s84 offen offset:0 sc1\n"
      "buffer_store_dwordx4 v[188:191], v227, s[72:75], s84 offen offset:128 sc1\n"
      "v_cvt_pk_bf16_f32 v200, v128, v129\n"
      "v_cvt_pk_bf16_f32 v202, v130, v131\n"
      "v_cvt_pk_bf16_f32 v204, v132, v133\n"
      "v_cvt_pk_bf16_f32 v206, v134, v135\n"
      "v_cvt_pk_bf16_f32 v208, v136, v137\n"
      "v_cvt_pk_bf16_f32 v210, v138, v139\n"
      "v_cvt_pk_bf16_f32 v212, v140, v141\n"
      "v_cvt_pk_bf16_f32 v214, v142, v143\n"
      "buffer_store_dwordx4 v[200:203], v223, s[72:75], s84 offen offset:1792 sc1\n"
      "buffer_store_dwordx4 v[204:207], v223, s[72:75], s84 offen offset:1920 sc1\n"
      "buffer_store_dwordx4 v[208:211], v227, s[72:75], s84 offen offset:1792 sc1\n"
      "buffer_store_dwordx4 v[212:215], v227, s[72:75], s84 offen offset:1920 sc1\n"
      "s_lshl_b32 s87, s93, 11\n"
      "s_add_u32 s87, s87, 147456\n"
      "s_cmp_lg_u32 s80, 3\n"
      "s_cbranch_scc1 TNP_%=\n"
      "buffer_load_dwordx4 v[32:35], v242, s[72:75], s87 offen offset:0 sc1\n"
      "buffer_load_dwordx4 v[36:39], v242, s[72:75], s87 offen offset:1024 sc1\n"
      "TNP_%=:\n"
      "v_cvt_pk_bf16_f32 v160, 0, v160\n"
      "v_cvt_pk_bf16_f32 v161, 0, v161\n"
      "v_cvt_pk_bf16_f32 v162, 0, v162\n"
      "v_cvt_pk_bf16_f32 v163, 0, v163\n"
      "v_cvt_pk_bf16_f32 v164, 0, v164\n"
      "v_cvt_pk_bf16_f32 v165, 0, v165\n"
      "v_cvt_pk_bf16_f32 v166, 0, v166\n"
      "v_cvt_pk_bf16_f32 v167, 0, v167\n"
      "v_cvt_pk_bf16_f32 v168, 0, v168\n"
      "v_cvt_pk_bf16_f32 v169, 0, v169\n"
      "v_cvt_pk_bf16_f32 v170, 0, v170\n"
      "v_cvt_pk_bf16_f32 v171, 0, v171\n"
      "v_cvt_pk_bf16_f32 v172, 0, v172\n"
      "v_cvt_pk_bf16_f32 v173, 0, v173\n"
      "v_cvt_pk_bf16_f32 v174, 0, v174\n"
      "v_cvt_pk_bf16_f32 v175, 0, v175\n"
      "v_pk_mul_f32 v[144:145], v[160:161], v[112:113]\n"
      "v_pk_mul_f32 v[146:147], v[162:163], v[114:115]\n"
      "v_pk_mul_f32 v[148:149], v[164:165], v[116:117]\n"
      "v_pk_mul_f32 v[150:151], v[166:167], v[118:119]\n"
      "v_pk_mul_f32 v[152:153], v[168:169], v[120:121]\n"
      "v_pk_mul_f32 v[154:155], v[170:171], v[122:123]\n"
      "v_pk_mul_f32 v[156:157], v[172:173], v[124:125]\n"
      "v_pk_mul_f32 v[158:159], v[174:175], v[126:127]\n"
      "v_cvt_pk_bf16_f32 v144, 0, v144\n"
      "v_cvt_pk_bf16_f32 v145, 0, v145\n"
      "v_cvt_pk_bf16_f32 v146, 0, v146\n"
      "v_cvt_pk_bf16_f32 v147, 0, v147\n"
      "v_cvt_pk_bf16_f32 v148, 0, v148\n"
      "v_cvt_pk_bf16_f32 v149, 0, v149\n"
      "v_cvt_pk_bf16_f32 v150, 0, v150\n"
      "v_cvt_pk_bf16_f32 v151, 0, v151\n"
      "v_cvt_pk_bf16_f32 v152, 0, v152\n"
      "v_cvt_pk_bf16_f32 v153, 0, v153\n"
      "v_cvt_pk_bf16_f32 v154, 0, v154\n"
      "v_cvt_pk_bf16_f32 v155, 0, v155\n"
      "v_cvt_pk_bf16_f32 v156, 0, v156\n"
      "v_cvt_pk_bf16_f32 v157, 0, v157\n"
      "v_cvt_pk_bf16_f32 v158, 0, v158\n"
      "v_cvt_pk_bf16_f32 v159, 0, v159\n"
      "v_pk_add_f32 v[144:145], v[144:145], v[128:129]\n"
      "v_pk_add_f32 v[146:147], v[146:147], v[130:131]\n"
      "v_pk_add_f32 v[148:149], v[148:149], v[132:133]\n"
      "v_pk_add_f32 v[150:151], v[150:151], v[134:135]\n"
      "v_pk_add_f32 v[152:153], v[152:153], v[136:137]\n"
      "v_pk_add_f32 v[154:155], v[154:155], v[138:139]\n"
      "v_pk_add_f32 v[156:157], v[156:157], v[140:141]\n"
      "v_pk_add_f32 v[158:159], v[158:159], v[142:143]\n"
      "v_cvt_pk_bf16_f32 v144, v144, v145\n"
      "v_cvt_pk_bf16_f32 v145, v146, v147\n"
      "v_cvt_pk_bf16_f32 v146, v148, v149\n"
      "v_cvt_pk_bf16_f32 v147, v150, v151\n"
      "v_cvt_pk_bf16_f32 v148, v152, v153\n"
      "v_cvt_pk_bf16_f32 v149, v154, v155\n"
      "v_cvt_pk_bf16_f32 v150, v156, v157\n"
      "v_cvt_pk_bf16_f32 v151, v158, v159\n"
      "s_mov_b32 s36, s64\n"
      "s_and_b32 s37, s65, 0xffff\n"
      "s_cmp_ge_u32 s92, 28\n"
      "s_cbranch_scc1 TDF_%=\n"
      "ds_read_b64_tr_b16 a[0:1], v235 offset:0\n"
      "ds_read_b64_tr_b16 a[2:3], v235 offset:64\n"
      "ds_read_b64_tr_b16 a[4:5], v235 offset:512\n"
      "ds_read_b64_tr_b16 a[6:7], v235 offset:576\n"
      "ds_read_b64_tr_b16 a[8:9], v235 offset:1024\n"
      "ds_read_b64_tr_b16 a[10:11], v235 offset:1088\n"
      "ds_read_b64_tr_b16 a[12:13], v235 offset:1536\n"
      "ds_read_b64_tr_b16 a[14:15], v235 offset:1600\n"
      "TDG_%=:\n"
      "s_cmp_lg_u32 s80, 3\n"
      "s_cbranch_scc1 TWAIT_%=\n"
      "s_mov_b32 s85, 0x20000\n"
      "s_sleep 4\n"
      "TBA_%=:\n"
      "buffer_load_dwordx4 v[40:43], v242, s[72:75], s87 offen offset:0 sc1\n"
      "buffer_load_dwordx4 v[44:47], v242, s[72:75], s87 offen offset:1024 sc1\n"
      "s_waitcnt vmcnt(2)\n"
      "v_xor_b32 v243, s53, v33\n"
      "v_bitop3_b32 v243, v35, v177, v243 bitop3:0xbe\n"
      "v_bitop3_b32 v243, v37, v177, v243 bitop3:0xbe\n"
      "v_bitop3_b32 v243, v39, v177, v243 bitop3:0xbe\n"
      "v_cmp_ne_u32 vcc, 0, v243\n"
      "s_cbranch_vccz TBB_%=\n"
      "buffer_load_dwordx4 v[32:35], v242, s[72:75], s87 offen offset:0 sc1\n"
      "buffer_load_dwordx4 v[36:39], v242, s[72:75], s87 offen offset:1024 sc1\n"
      "s_waitcnt vmcnt(2)\n"
      "v_xor_b32 v243, s53, v41\n"
      "v_bitop3_b32 v243, v43, v177, v243 bitop3:0xbe\n"
      "v_bitop3_b32 v243, v45, v177, v243 bitop3:0xbe\n"
      "v_bitop3_b32 v243, v47, v177, v243 bitop3:0xbe\n"
      "v_cmp_ne_u32 vcc, 0, v243\n"
      "s_cbranch_vccz TBC_%=\n"
      "s_sub_u32 s85, s85, 1\n"
      "s_cbranch_scc0 TBA_%=\n"
      "TBC_%=:\n"
      "v_mov_b32 v48, v40\n"
      "v_mov_b32 v49, v41\n"
      "v_mov_b32 v50, v42\n"
      "v_mov_b32 v51, v43\n"
      "v_mov_b32 v52, v44\n"
      "v_mov_b32 v53, v45\n"
      "v_mov_b32 v54, v46\n"
      "v_mov_b32 v55, v47\n"
      "s_branch TBD_%=\n"
      "TBB_%=:\n"
      "v_mov_b32 v48, v32\n"
      "v_mov_b32 v49, v33\n"
      "v_mov_b32 v50, v34\n"
      "v_mov_b32 v51, v35\n"
      "v_mov_b32 v52, v36\n"
      "v_mov_b32 v53, v37\n"
      "v_mov_b32 v54, v38\n"
      "v_mov_b32 v55, v39\n"
      "TBD_%=:\n"
      "v_max3_u32 v243, v48, v50, v52\n"
      "v_max_u32 v243, v243, v54\n"
      "s_nop 1\n"
      "v_max_u32_dpp v243, v243, v243 row_ror:1 row_mask:0xf bank_mask:0xf\n"
      "s_nop 1\n"
      "v_max_u32_dpp v243, v243, v243 row_ror:2 row_mask:0xf bank_mask:0xf\n"
      "s_nop 1\n"
      "v_max_u32_dpp v243, v243, v243 row_ror:4 row_mask:0xf bank_mask:0xf\n"
      "s_nop 1\n"
      "v_max_u32_dpp v243, v243, v243 row_ror:8 row_mask:0xf bank_mask:0xf\n"
      "v_mov_b32 v252, v243\n"
      "s_nop 1\n"
      "v_permlane16_swap_b32 v243, v252\n"
      "v_max_u32 v243, v243, v252\n"
      "v_mov_b32 v252, v243\n"
      "s_nop 1\n"
      "v_permlane32_swap_b32 v243, v252\n"
      "v_max_u32 v243, v243, v252\n"
      "ds_write_b32 v241, v243 offset:384\n"
      "TWAIT_%=:\n"
      "s_waitcnt lgkmcnt(0)\n"
      "s_barrier\n"
      "ds_read_b32 v243, v241 offset:384\n"
      "v_lshrrev_b32 v246, 1, v242\n"
      "v_lshl_add_u32 v246, s80, 9, v246\n"
      "v_lshrrev_b32 v247, 2, v242\n"
      "v_lshl_add_u32 v247, s80, 10, v247\n"
      "v_add_u32 v247, 1024, v247\n"
      "s_waitcnt lgkmcnt(0)\n"
      "v_readfirstlane_b32 s97, v243\n"
      "v_cmp_eq_u32 vcc, v243, v218\n"
      "v_mov_b32 v252, 1073741824\n"
      "s_andn2_b32 s98, 0x7ff, s97\n"
      "s_lshr_b32 s84, s98, 5\n"
      "s_lshl_b32 s84, s84, 2\n"
      "s_bfe_u32 s86, s98, 0x20002\n"
      "s_add_u32 s84, s84, s86\n"
      "s_mul_i32 s84, s84, 3584\n"
      "s_add_u32 s84, s84, 196608\n"
      "buffer_load_dwordx2 v[244:245], v246, s[72:75], s84 offen sc1\n"
      "v_cndmask_b32 v221, v221, v252, vcc\n"
      "v_cndmask_b32 v222, v222, v252, vcc\n"
      "buffer_store_dwordx2 v[144:145], v221, s[36:39], 0 offen offset:0\n"
      "buffer_store_dwordx2 v[146:147], v221, s[36:39], 0 offen offset:64\n"
      "buffer_store_dwordx2 v[148:149], v222, s[36:39], 0 offen offset:512\n"
      "buffer_store_dwordx2 v[150:151], v222, s[36:39], 0 offen offset:576\n"
      "s_cmp_ge_u32 s92, 28\n"
      "s_cbranch_scc1 TDOWN_%=\n"
      "s_mov_b32 s85, 0x40000\n"
      "s_waitcnt vmcnt(4)\n"
      "THA_%=:\n"
      "v_cmp_ne_u32 vcc, s53, v245\n"
      "s_cbranch_vccz THB_%=\n"
      "s_sub_u32 s85, s85, 1\n"
      "s_cbranch_scc1 THB_%=\n"
      "s_sleep 1\n"
      "buffer_load_dwordx2 v[244:245], v246, s[72:75], s84 offen sc1\n"
      "s_waitcnt vmcnt(0)\n"
      "s_branch THA_%=\n"
      "THB_%=:\n"
      "ds_write_b32 v247, v244\n"
      "s_waitcnt lgkmcnt(0)\n"
      "ds_read_b128 v[16:19], v236 offset:0\n"
      "ds_read_b128 v[20:23], v236 offset:64\n"
      "ds_read_b128 v[24:27], v236 offset:128\n"
      "ds_read_b128 v[28:31], v236 offset:192\n"
      "s_waitcnt lgkmcnt(3)\n"
      "v_mfma_f32_16x16x32_bf16 v[0:3], a[0:3], v[16:19], 0\n"
      "s_waitcnt lgkmcnt(2)\n"
      "v_mfma_f32_16x16x32_bf16 v[0:3], a[4:7], v[20:23], v[0:3]\n"
      "s_waitcnt lgkmcnt(1)\n"
      "v_mfma_f32_16x16x32_bf16 v[0:3], a[8:11], v[24:27], v[0:3]\n"
      "s_waitcnt lgkmcnt(0)\n"
      "v_mfma_f32_16x16x32_bf16 v[0:3], a[12:15], v[28:31], v[0:3]\n"
      "v_lshl_add_u32 v243, s80, 10, v242\n"
      "v_add_u32 v243, 8192, v243\n"
      "v_add_u32 v252, 8192, v242\n"
      "s_nop 4\n"
      "ds_write_b128 v243, v[0:3]\n"
      "s_waitcnt lgkmcnt(0)\n"
      "s_barrier\n"
      "s_cmp_lg_u32 s80, 0\n"
      "s_cbranch_scc1 TEND_%=\n"
      "ds_read_b128 v[0:3], v252 offset:0\n"
      "ds_read_b128 v[4:7], v252 offset:1024\n"
      "ds_read_b128 v[8:11], v252 offset:2048\n"
      "ds_read_b128 v[12:15], v252 offset:3072\n"
      "s_waitcnt lgkmcnt(0)\n"
      "v_pk_add_f32 v[0:1], v[0:1], v[4:5]\n"
      "v_pk_add_f32 v[8:9], v[8:9], v[12:13]\n"
      "v_pk_add_f32 v[2:3], v[2:3], v[6:7]\n"
      "v_pk_add_f32 v[10:11], v[10:11], v[14:15]\n"
      "v_pk_add_f32 v[0:1], v[0:1], v[8:9]\n"
      "v_pk_add_f32 v[2:3], v[2:3], v[10:11]\n"
      "v_cvt_pk_bf16_f32 v0, 0, v0\n"
      "v_cvt_pk_bf16_f32 v1, 0, v1\n"
      "v_cvt_pk_bf16_f32 v2, 0, v2\n"
      "v_cvt_pk_bf16_f32 v3, 0, v3\n"
      "v_mov_b32 v4, v0\n"
      "v_mov_b32 v5, v1\n"
      "v_mov_b32 v6, v2\n"
      "v_mov_b32 v7, v3\n"
      "v_mul_f32 v8, 0xbfb8aa3b, v0\n"
      "v_mul_f32 v9, 0xbfb8aa3b, v1\n"
      "v_mul_f32 v10, 0xbfb8aa3b, v2\n"
      "v_mul_f32 v11, 0xbfb8aa3b, v3\n"
      "v_exp_f32 v8, v8\n"
      "v_exp_f32 v9, v9\n"
      "v_exp_f32 v10, v10\n"
      "v_exp_f32 v11, v11\n"
      "v_permlane32_swap_b32 v0, v4\n"
      "v_permlane32_swap_b32 v1, v5\n"
      "v_permlane32_swap_b32 v2, v6\n"
      "v_permlane32_swap_b32 v3, v7\n"
      "v_add_f32 v8, 1.0, v8\n"
      "v_add_f32 v9, 1.0, v9\n"
      "v_add_f32 v10, 1.0, v10\n"
      "v_add_f32 v11, 1.0, v11\n"
      "v_rcp_f32 v8, v8\n"
      "v_rcp_f32 v9, v9\n"
      "v_rcp_f32 v10, v10\n"
      "v_rcp_f32 v11, v11\n"
      "v_and_b32 v243, 47, v254\n"
      "v_mul_f32 v0, v0, v8\n"
      "v_mul_f32 v1, v1, v9\n"
      "v_mul_f32 v2, v2, v10\n"
      "v_mul_f32 v3, v3, v11\n"
      "v_min_u32 v243, 1, v243\n"
      "v_cvt_pk_bf16_f32 v0, 0, v0\n"
      "v_cvt_pk_bf16_f32 v1, 0, v1\n"
      "v_cvt_pk_bf16_f32 v2, 0, v2\n"
      "v_cvt_pk_bf16_f32 v3, 0, v3\n"
      "v_lshl_add_u32 v243, v243, 30, v254\n"
      "v_mul_f32 v0, v0, v4\n"
      "v_mul_f32 v1, v1, v5\n"
      "v_mul_f32 v2, v2, v6\n"
      "v_mul_f32 v3, v3, v7\n"
      "s_lshl_b32 s84, s94, 5\n"
      "s_add_u32 s84, s84, 139264\n"
      "v_cvt_pk_bf16_f32 v176, v0, v1\n"
      "v_cvt_pk_bf16_f32 v178, v2, v3\n"
      "buffer_store_dwordx4 v[176:179], v243, s[72:75], s84 offen sc1\n"
      "s_branch TEND_%=\n"
      "TDOWN_%=:\n"
      "s_cmp_eq_u32 s93, 7\n"
      "s_cbranch_scc1 TEND_%=\n"
      "s_cmp_eq_u32 s94, 0\n"
      "s_cselect_b32 s86, 131072, 1073741824\n"
      "buffer_store_dword v177, v216, s[72:75], s86 offen sc1\n"
      "v_lshrrev_b32 v252, 4, v254\n"
      "v_lshlrev_b32 v252, 4, v252\n"
      "v_lshl_add_u32 v252, s94, 6, v252\n"
      "s_mul_i32 s87, s80, 1792\n"
      "s_add_u32 s87, s87, 139264\n"
      "v_min_u32 v248, 47, v254\n"
      "v_lshlrev_b32 v248, 4, v248\n"
      "buffer_load_dwordx4 v[64:67], v252, s[72:75], s84 offen sc1\n"
      "buffer_load_dwordx4 v[68:71], v252, s[72:75], s84 offen offset:1792 sc1\n"
      "buffer_load_dwordx4 v[72:75], v252, s[72:75], s90 offen offset:1792 sc1\n"
      "v_lshrrev_b32 v249, 1, v242\n"
      "v_lshl_add_u32 v249, s80, 10, v249\n"
      "v_add_u32 v249, 1024, v249\n"
      "s_and_b32 s86, s97, 0xffff0000\n"
      "s_ashr_i32 s97, s86, 31\n"
      "s_andn2_b32 s97, 0x7fff0000, s97\n"
      "s_xor_b32 s86, s86, s97\n"
      "s_xor_b32 s86, s86, 0x80000000\n"
      "v_mov_b32 v76, 0xbfb8aa3b\n"
      "v_mul_f32 v76, s86, v76\n"
      "v_exp_f32 v76, v76\n"
      "s_nop 0\n"
      "v_add_f32 v76, 1.0, v76\n"
      "v_rcp_f32 v76, v76\n"
      "s_nop 0\n"
      "v_cvt_pk_bf16_f32 v76, 0, v76\n"
      "s_mov_b32 s85, 0x40000\n"
      "s_waitcnt vmcnt(0)\n"
      "TXA_%=:\n"
      "v_xor_b32 v243, s53, v65\n"
      "v_bitop3_b32 v243, v67, v177, v243 bitop3:0xbe\n"
      "v_bitop3_b32 v243, v69, v177, v243 bitop3:0xbe\n"
      "v_bitop3_b32 v243, v71, v177, v243 bitop3:0xbe\n"
      "v_bitop3_b32 v243, v73, v177, v243 bitop3:0xbe\n"
      "v_bitop3_b32 v243, v75, v177, v243 bitop3:0xbe\n"
      "v_cmp_ne_u32 vcc, 0, v243\n"
      "s_cbranch_vccz TXB_%=\n"
      "s_sub_u32 s85, s85, 1\n"
      "s_cbranch_scc1 TXB_%=\n"
      "s_sleep 1\n"
      "buffer_load_dwordx4 v[64:67], v252, s[72:75], s84 offen sc1\n"
      "buffer_load_dwordx4 v[68:71], v252, s[72:75], s84 offen offset:1792 sc1\n"
      "buffer_load_dwordx4 v[72:75], v252, s[72:75], s90 offen offset:1792 sc1\n"
      "s_waitcnt vmcnt(0)\n"
      "s_branch TXA_%=\n"
      "TXB_%=:\n"
      "v_and_b32 v65, v253, v64\n"
      "v_lshlrev_b32 v64, 16, v64\n"
      "v_and_b32 v67, v253, v66\n"
      "v_lshlrev_b32 v66, 16, v66\n"
      "v_and_b32 v69, v253, v68\n"
      "v_lshlrev_b32 v68, 16, v68\n"
      "v_and_b32 v71, v253, v70\n"
      "v_lshlrev_b32 v70, 16, v70\n"
      "v_and_b32 v73, v253, v72\n"
      "v_lshlrev_b32 v72, 16, v72\n"
      "v_and_b32 v75, v253, v74\n"
      "v_lshlrev_b32 v74, 16, v74\n"
      "buffer_load_dwordx4 v[80:83], v242, s[72:75], s87 offen sc1\n"
      "buffer_load_dwordx4 v[84:87], v248, s[72:75], s87 offen offset:1024 sc1\n"
      "s_mov_b32 s85, 0x20000\n"
      "s_sleep 4\n"
      "TGA_%=:\n"
      "buffer_load_dwordx4 v[88:91], v242, s[72:75], s87 offen sc1\n"
      "buffer_load_dwordx4 v[92:95], v248, s[72:75], s87 offen offset:1024 sc1\n"
      "s_waitcnt vmcnt(2)\n"
      "v_xor_b32 v243, s53, v81\n"
      "v_bitop3_b32 v243, v83, v177, v243 bitop3:0xbe\n"
      "v_bitop3_b32 v243, v85, v177, v243 bitop3:0xbe\n"
      "v_bitop3_b32 v243, v87, v177, v243 bitop3:0xbe\n"
      "v_cmp_ne_u32 vcc, 0, v243\n"
      "s_cbranch_vccz TGB_%=\n"
      "buffer_load_dwordx4 v[80:83], v242, s[72:75], s87 offen sc1\n"
      "buffer_load_dwordx4 v[84:87], v248, s[72:75], s87 offen offset:1024 sc1\n"
      "s_waitcnt vmcnt(2)\n"
      "v_xor_b32 v243, s53, v89\n"
      "v_bitop3_b32 v243, v91, v177, v243 bitop3:0xbe\n"
      "v_bitop3_b32 v243, v93, v177, v243 bitop3:0xbe\n"
      "v_bitop3_b32 v243, v95, v177, v243 bitop3:0xbe\n"
      "v_cmp_ne_u32 vcc, 0, v243\n"
      "s_cbranch_vccz TGC_%=\n"
      "s_sub_u32 s85, s85, 1\n"
      "s_cbranch_scc0 TGA_%=\n"
      "TGC_%=:\n"
      "v_mov_b32 v96, v88\n"
      "v_mov_b32 v97, v89\n"
      "v_mov_b32 v98, v90\n"
      "v_mov_b32 v99, v91\n"
      "v_mov_b32 v100, v92\n"
      "v_mov_b32 v101, v93\n"
      "v_mov_b32 v102, v94\n"
      "v_mov_b32 v103, v95\n"
      "s_branch TGD_%=\n"
      "TGB_%=:\n"
      "v_mov_b32 v96, v80\n"
      "v_mov_b32 v97, v81\n"
      "v_mov_b32 v98, v82\n"
      "v_mov_b32 v99, v83\n"
      "v_mov_b32 v100, v84\n"
      "v_mov_b32 v101, v85\n"
      "v_mov_b32 v102, v86\n"
      "v_mov_b32 v103, v87\n"
      "TGD_%=:\n"
      "v_mov_b32 v97, v98\n"
      "v_mov_b32 v101, v102\n"
      "ds_write_b64 v249, v[96:97]\n"
      "ds_write_b64 v249, v[100:101] offset:512\n"
      "s_waitcnt lgkmcnt(0)\n"
      "ds_read_b128 v[128:131], v236 offset:0\n"
      "ds_read_b128 v[132:135], v236 offset:64\n"
      "ds_read_b128 v[136:139], v236 offset:128\n"
      "ds_read_b128 v[140:143], v236 offset:192\n"
      "ds_read_b128 v[144:147], v236 offset:256\n"
      "ds_read_b128 v[148:151], v236 offset:320\n"
      "ds_read_b128 v[152:155], v236 offset:384\n"
      "ds_read_b128 v[156:159], v236 offset:448\n"
      "ds_read_b128 v[160:163], v236 offset:512\n"
      "ds_read_b128 v[164:167], v236 offset:576\n"
      "ds_read_b128 v[168:171], v236 offset:640\n"
      "ds_read_b128 v[172:175], v236 offset:704\n"
      "ds_read_b128 v[176:179], v236 offset:768\n"
      "ds_read_b128 v[180:183], v236 offset:832\n"
      "s_waitcnt lgkmcnt(13)\n"
      "v_mfma_f32_16x16x32_bf16 v[0:3], a[0:3], v[128:131], 0\n"
      "s_waitcnt lgkmcnt(12)\n"
      "v_mfma_f32_16x16x32_bf16 v[0:3], a[4:7], v[132:135], v[0:3]\n"
      "s_waitcnt lgkmcnt(11)\n"
      "v_mfma_f32_16x16x32_bf16 v[0:3], a[8:11], v[136:139], v[0:3]\n"
      "s_waitcnt lgkmcnt(10)\n"
      "v_mfma_f32_16x16x32_bf16 v[0:3], a[12:15], v[140:143], v[0:3]\n"
      "s_waitcnt lgkmcnt(9)\n"
      "v_mfma_f32_16x16x32_bf16 v[0:3], a[16:19], v[144:147], v[0:3]\n"
      "s_waitcnt lgkmcnt(8)\n"
      "v_mfma_f32_16x16x32_bf16 v[0:3], a[20:23], v[148:151], v[0:3]\n"
      "s_waitcnt lgkmcnt(7)\n"
      "v_mfma_f32_16x16x32_bf16 v[0:3], a[24:27], v[152:155], v[0:3]\n"
      "s_waitcnt lgkmcnt(6)\n"
      "v_mfma_f32_16x16x32_bf16 v[0:3], a[28:31], v[156:159], v[0:3]\n"
      "s_waitcnt lgkmcnt(5)\n"
      "v_mfma_f32_16x16x32_bf16 v[0:3], a[32:35], v[160:163], v[0:3]\n"
      "s_waitcnt lgkmcnt(4)\n"
      "v_mfma_f32_16x16x32_bf16 v[0:3], a[36:39], v[164:167], v[0:3]\n"
      "s_waitcnt lgkmcnt(3)\n"
      "v_mfma_f32_16x16x32_bf16 v[0:3], a[40:43], v[168:171], v[0:3]\n"
      "s_waitcnt lgkmcnt(2)\n"
      "v_mfma_f32_16x16x32_bf16 v[0:3], a[44:47], v[172:175], v[0:3]\n"
      "s_waitcnt lgkmcnt(1)\n"
      "v_mfma_f32_16x16x32_bf16 v[0:3], a[48:51], v[176:179], v[0:3]\n"
      "s_waitcnt lgkmcnt(0)\n"
      "v_mfma_f32_16x16x32_bf16 v[0:3], a[52:55], v[180:183], v[0:3]\n"
      "v_lshl_add_u32 v243, s80, 10, v242\n"
      "v_add_u32 v243, 8192, v243\n"
      "v_add_u32 v252, 8192, v242\n"
      "s_nop 4\n"
      "ds_write_b128 v243, v[0:3]\n"
      "s_waitcnt lgkmcnt(0)\n"
      "s_barrier\n"
      "s_cmp_lg_u32 s80, 0\n"
      "s_cbranch_scc1 TEND_%=\n"
      "ds_read_b128 v[0:3], v252 offset:0\n"
      "ds_read_b128 v[4:7], v252 offset:1024\n"
      "ds_read_b128 v[8:11], v252 offset:2048\n"
      "ds_read_b128 v[12:15], v252 offset:3072\n"
      "s_waitcnt lgkmcnt(0)\n"
      "v_pk_add_f32 v[0:1], v[0:1], v[4:5]\n"
      "v_pk_add_f32 v[8:9], v[8:9], v[12:13]\n"
      "v_pk_add_f32 v[2:3], v[2:3], v[6:7]\n"
      "v_pk_add_f32 v[10:11], v[10:11], v[14:15]\n"
      "v_pk_add_f32 v[0:1], v[0:1], v[8:9]\n"
      "v_pk_add_f32 v[2:3], v[2:3], v[10:11]\n"
      "v_cvt_pk_bf16_f32 v0, 0, v0\n"
      "v_cvt_pk_bf16_f32 v1, 0, v1\n"
      "v_cvt_pk_bf16_f32 v2, 0, v2\n"
      "v_cvt_pk_bf16_f32 v3, 0, v3\n"
      "v_mul_f32 v0, v0, v76\n"
      "v_mul_f32 v1, v1, v76\n"
      "v_mul_f32 v2, v2, v76\n"
      "v_mul_f32 v3, v3, v76\n"
      "v_cvt_pk_bf16_f32 v0, 0, v0\n"
      "v_cvt_pk_bf16_f32 v1, 0, v1\n"
      "v_cvt_pk_bf16_f32 v2, 0, v2\n"
      "v_cvt_pk_bf16_f32 v3, 0, v3\n"
      "v_add_f32 v0, v0, v64\n"
      "v_add_f32 v1, v1, v65\n"
      "v_add_f32 v2, v2, v66\n"
      "v_add_f32 v3, v3, v67\n"
      "v_cvt_pk_bf16_f32 v0, 0, v0\n"
      "v_cvt_pk_bf16_f32 v1, 0, v1\n"
      "v_cvt_pk_bf16_f32 v2, 0, v2\n"
      "v_cvt_pk_bf16_f32 v3, 0, v3\n"
      "v_mul_f32 v0, v0, v72\n"
      "v_mul_f32 v1, v1, v73\n"
      "v_mul_f32 v2, v2, v74\n"
      "v_mul_f32 v3, v3, v75\n"
      "v_and_b32 v243, 15, v254\n"
      "v_cvt_pk_bf16_f32 v0, 0, v0\n"
      "v_cvt_pk_bf16_f32 v1, 0, v1\n"
      "v_cvt_pk_bf16_f32 v2, 0, v2\n"
      "v_cvt_pk_bf16_f32 v3, 0, v3\n"
      "v_min_u32 v243, 1, v243\n"
      "v_lshrrev_b32 v252, 1, v254\n"
      "v_add_f32 v0, v0, v68\n"
      "v_add_f32 v1, v1, v69\n"
      "v_add_f32 v2, v2, v70\n"
      "v_add_f32 v3, v3, v71\n"
      "v_lshl_add_u32 v243, v243, 30, v252\n"
      "s_mul_i32 s84, s98, 896\n"
      "s_lshl_b32 s86, s94, 5\n"
      "s_add_u32 s84, s84, s86\n"
      "v_cvt_pk_bf16_f32 v0, v0, v1\n"
      "v_cvt_pk_bf16_f32 v1, v2, v3\n"
      "buffer_store_dwordx2 v[0:1], v243, s[36:39], s84 offen\n"
      "TEND_%=:\n"
      "s_branch KEND_%=\n"
      "R2_1_%=:\n"
      "s_nop 7\n"
      "v_add_u32 v243, s97, v242\n"
      "s_xor_b32 s84, s80, 1\n"
      "s_lshl_b32 s84, s84, 3\n"
      "s_add_u32 s84, s84, 147520\n"
      "v_mov_b32 v253, s84\n"
      "ds_read_b128 v[244:247], v243 offset:0\n"
      "ds_read_b32 v252, v253 offset:0\n"
      "s_waitcnt lgkmcnt(0)\n"
      "v_cmp_eq_u32 vcc, s88, v252\n"
      "s_nop 1\n"
      "v_cndmask_b32 v244, 0, v244, vcc\n"
      "v_cndmask_b32 v245, 0, v245, vcc\n"
      "v_cndmask_b32 v246, 0, v246, vcc\n"
      "v_cndmask_b32 v247, 0, v247, vcc\n"
      "v_max_f32 v248, v212, v244\n"
      "v_max_f32 v249, v213, v245\n"
      "v_max_f32 v250, v214, v246\n"
      "v_max_f32 v251, v215, v247\n"
      "v_exp_f32_e64 v243, -v248\n"
      "v_sub_f32 v212, v212, v248\n"
      "v_sub_f32 v244, v244, v248\n"
      "v_exp_f32 v212, v212\n"
      "v_exp_f32 v244, v244\n"
      "v_mul_f32 v0, v0, v243\n"
      "v_mul_f32 v1, v1, v243\n"
      "v_mul_f32 v2, v2, v243\n"
      "v_mul_f32 v3, v3, v243\n"
      "v_mul_f32 v16, v16, v243\n"
      "v_mul_f32 v17, v17, v243\n"
      "v_mul_f32 v18, v18, v243\n"
      "v_mul_f32 v19, v19, v243\n"
      "v_mul_f32 v32, v32, v243\n"
      "v_mul_f32 v33, v33, v243\n"
      "v_mul_f32 v34, v34, v243\n"
      "v_mul_f32 v35, v35, v243\n"
      "v_mul_f32 v48, v48, v243\n"
      "v_mul_f32 v49, v49, v243\n"
      "v_mul_f32 v50, v50, v243\n"
      "v_mul_f32 v51, v51, v243\n"
      "v_mul_f32 v64, v64, v243\n"
      "v_mul_f32 v65, v65, v243\n"
      "v_mul_f32 v66, v66, v243\n"
      "v_mul_f32 v67, v67, v243\n"
      "v_mul_f32 v80, v80, v243\n"
      "v_mul_f32 v81, v81, v243\n"
      "v_mul_f32 v82, v82, v243\n"
      "v_mul_f32 v83, v83, v243\n"
      "v_mul_f32 v96, v96, v243\n"
      "v_mul_f32 v97, v97, v243\n"
      "v_mul_f32 v98, v98, v243\n"
      "v_mul_f32 v99, v99, v243\n"
      "v_mul_f32 v112, v112, v243\n"
      "v_mul_f32 v113, v113, v243\n"
      "v_mul_f32 v114, v114, v243\n"
      "v_mul_f32 v115, v115, v243\n"
      "v_sub_f32 v160, v160, v248\n"
      "v_sub_f32 v161, v161, v248\n"
      "v_sub_f32 v162, v162, v248\n"
      "v_sub_f32 v163, v163, v248\n"
      "v_sub_f32 v176, v176, v248\n"
      "v_sub_f32 v177, v177, v248\n"
      "v_sub_f32 v178, v178, v248\n"
      "v_sub_f32 v179, v179, v248\n"
      "v_sub_f32 v192, v192, v248\n"
      "v_sub_f32 v193, v193, v248\n"
      "v_sub_f32 v194, v194, v248\n"
      "v_sub_f32 v195, v195, v248\n"
      "v_mul_f32 v208, v208, v212\n"
      "v_lshlrev_b32 v252, 16, v128\n"
      "v_and_b32 v253, 0xffff0000, v128\n"
      "v_mul_f32 v252, v252, v212\n"
      "v_mul_f32 v253, v253, v212\n"
      "v_cvt_pk_bf16_f32 v128, v252, v253\n"
      "v_lshlrev_b32 v252, 16, v129\n"
      "v_and_b32 v253, 0xffff0000, v129\n"
      "v_mul_f32 v252, v252, v212\n"
      "v_mul_f32 v253, v253, v212\n"
      "v_cvt_pk_bf16_f32 v129, v252, v253\n"
      "v_lshlrev_b32 v252, 16, v130\n"
      "v_and_b32 v253, 0xffff0000, v130\n"
      "v_mul_f32 v252, v252, v212\n"
      "v_mul_f32 v253, v253, v212\n"
      "v_cvt_pk_bf16_f32 v130, v252, v253\n"
      "v_lshlrev_b32 v252, 16, v131\n"
      "v_and_b32 v253, 0xffff0000, v131\n"
      "v_mul_f32 v252, v252, v212\n"
      "v_mul_f32 v253, v253, v212\n"
      "v_cvt_pk_bf16_f32 v131, v252, v253\n"
      "v_lshlrev_b32 v252, 16, v144\n"
      "v_and_b32 v253, 0xffff0000, v144\n"
      "v_mul_f32 v252, v252, v244\n"
      "v_mul_f32 v253, v253, v244\n"
      "v_cvt_pk_bf16_f32 v144, v252, v253\n"
      "v_lshlrev_b32 v252, 16, v145\n"
      "v_and_b32 v253, 0xffff0000, v145\n"
      "v_mul_f32 v252, v252, v244\n"
      "v_mul_f32 v253, v253, v244\n"
      "v_cvt_pk_bf16_f32 v145, v252, v253\n"
      "v_lshlrev_b32 v252, 16, v146\n"
      "v_and_b32 v253, 0xffff0000, v146\n"
      "v_mul_f32 v252, v252, v244\n"
      "v_mul_f32 v253, v253, v244\n"
      "v_cvt_pk_bf16_f32 v146, v252, v253\n"
      "v_lshlrev_b32 v252, 16, v147\n"
      "v_and_b32 v253, 0xffff0000, v147\n"
      "v_mul_f32 v252, v252, v244\n"
      "v_mul_f32 v253, v253, v244\n"
      "v_cvt_pk_bf16_f32 v147, v252, v253\n"
      "v_mov_b32 v212, 0\n"
      "v_exp_f32_e64 v243, -v249\n"
      "v_sub_f32 v213, v213, v249\n"
      "v_sub_f32 v245, v245, v249\n"
      "v_exp_f32 v213, v213\n"
      "v_exp_f32 v245, v245\n"
      "v_mul_f32 v4, v4, v243\n"
      "v_mul_f32 v5, v5, v243\n"
      "v_mul_f32 v6, v6, v243\n"
      "v_mul_f32 v7, v7, v243\n"
      "v_mul_f32 v20, v20, v243\n"
      "v_mul_f32 v21, v21, v243\n"
      "v_mul_f32 v22, v22, v243\n"
      "v_mul_f32 v23, v23, v243\n"
      "v_mul_f32 v36, v36, v243\n"
      "v_mul_f32 v37, v37, v243\n"
      "v_mul_f32 v38, v38, v243\n"
      "v_mul_f32 v39, v39, v243\n"
      "v_mul_f32 v52, v52, v243\n"
      "v_mul_f32 v53, v53, v243\n"
      "v_mul_f32 v54, v54, v243\n"
      "v_mul_f32 v55, v55, v243\n"
      "v_mul_f32 v68, v68, v243\n"
      "v_mul_f32 v69, v69, v243\n"
      "v_mul_f32 v70, v70, v243\n"
      "v_mul_f32 v71, v71, v243\n"
      "v_mul_f32 v84, v84, v243\n"
      "v_mul_f32 v85, v85, v243\n"
      "v_mul_f32 v86, v86, v243\n"
      "v_mul_f32 v87, v87, v243\n"
      "v_mul_f32 v100, v100, v243\n"
      "v_mul_f32 v101, v101, v243\n"
      "v_mul_f32 v102, v102, v243\n"
      "v_mul_f32 v103, v103, v243\n"
      "v_mul_f32 v116, v116, v243\n"
      "v_mul_f32 v117, v117, v243\n"
      "v_mul_f32 v118, v118, v243\n"
      "v_mul_f32 v119, v119, v243\n"
      "v_sub_f32 v164, v164, v249\n"
      "v_sub_f32 v165, v165, v249\n"
      "v_sub_f32 v166, v166, v249\n"
      "v_sub_f32 v167, v167, v249\n"
      "v_sub_f32 v180, v180, v249\n"
      "v_sub_f32 v181, v181, v249\n"
      "v_sub_f32 v182, v182, v249\n"
      "v_sub_f32 v183, v183, v249\n"
      "v_sub_f32 v196, v196, v249\n"
      "v_sub_f32 v197, v197, v249\n"
      "v_sub_f32 v198, v198, v249\n"
      "v_sub_f32 v199, v199, v249\n"
      "v_mul_f32 v209, v209, v213\n"
      "v_lshlrev_b32 v252, 16, v132\n"
      "v_and_b32 v253, 0xffff0000, v132\n"
      "v_mul_f32 v252, v252, v213\n"
      "v_mul_f32 v253, v253, v213\n"
      "v_cvt_pk_bf16_f32 v132, v252, v253\n"
      "v_lshlrev_b32 v252, 16, v133\n"
      "v_and_b32 v253, 0xffff0000, v133\n"
      "v_mul_f32 v252, v252, v213\n"
      "v_mul_f32 v253, v253, v213\n"
      "v_cvt_pk_bf16_f32 v133, v252, v253\n"
      "v_lshlrev_b32 v252, 16, v134\n"
      "v_and_b32 v253, 0xffff0000, v134\n"
      "v_mul_f32 v252, v252, v213\n"
      "v_mul_f32 v253, v253, v213\n"
      "v_cvt_pk_bf16_f32 v134, v252, v253\n"
      "v_lshlrev_b32 v252, 16, v135\n"
      "v_and_b32 v253, 0xffff0000, v135\n"
      "v_mul_f32 v252, v252, v213\n"
      "v_mul_f32 v253, v253, v213\n"
      "v_cvt_pk_bf16_f32 v135, v252, v253\n"
      "v_lshlrev_b32 v252, 16, v148\n"
      "v_and_b32 v253, 0xffff0000, v148\n"
      "v_mul_f32 v252, v252, v245\n"
      "v_mul_f32 v253, v253, v245\n"
      "v_cvt_pk_bf16_f32 v148, v252, v253\n"
      "v_lshlrev_b32 v252, 16, v149\n"
      "v_and_b32 v253, 0xffff0000, v149\n"
      "v_mul_f32 v252, v252, v245\n"
      "v_mul_f32 v253, v253, v245\n"
      "v_cvt_pk_bf16_f32 v149, v252, v253\n"
      "v_lshlrev_b32 v252, 16, v150\n"
      "v_and_b32 v253, 0xffff0000, v150\n"
      "v_mul_f32 v252, v252, v245\n"
      "v_mul_f32 v253, v253, v245\n"
      "v_cvt_pk_bf16_f32 v150, v252, v253\n"
      "v_lshlrev_b32 v252, 16, v151\n"
      "v_and_b32 v253, 0xffff0000, v151\n"
      "v_mul_f32 v252, v252, v245\n"
      "v_mul_f32 v253, v253, v245\n"
      "v_cvt_pk_bf16_f32 v151, v252, v253\n"
      "v_mov_b32 v213, 0\n"
      "v_exp_f32_e64 v243, -v250\n"
      "v_sub_f32 v214, v214, v250\n"
      "v_sub_f32 v246, v246, v250\n"
      "v_exp_f32 v214, v214\n"
      "v_exp_f32 v246, v246\n"
      "v_mul_f32 v8, v8, v243\n"
      "v_mul_f32 v9, v9, v243\n"
      "v_mul_f32 v10, v10, v243\n"
      "v_mul_f32 v11, v11, v243\n"
      "v_mul_f32 v24, v24, v243\n"
      "v_mul_f32 v25, v25, v243\n"
      "v_mul_f32 v26, v26, v243\n"
      "v_mul_f32 v27, v27, v243\n"
      "v_mul_f32 v40, v40, v243\n"
      "v_mul_f32 v41, v41, v243\n"
      "v_mul_f32 v42, v42, v243\n"
      "v_mul_f32 v43, v43, v243\n"
      "v_mul_f32 v56, v56, v243\n"
      "v_mul_f32 v57, v57, v243\n"
      "v_mul_f32 v58, v58, v243\n"
      "v_mul_f32 v59, v59, v243\n"
      "v_mul_f32 v72, v72, v243\n"
      "v_mul_f32 v73, v73, v243\n"
      "v_mul_f32 v74, v74, v243\n"
      "v_mul_f32 v75, v75, v243\n"
      "v_mul_f32 v88, v88, v243\n"
      "v_mul_f32 v89, v89, v243\n"
      "v_mul_f32 v90, v90, v243\n"
      "v_mul_f32 v91, v91, v243\n"
      "v_mul_f32 v104, v104, v243\n"
      "v_mul_f32 v105, v105, v243\n"
      "v_mul_f32 v106, v106, v243\n"
      "v_mul_f32 v107, v107, v243\n"
      "v_mul_f32 v120, v120, v243\n"
      "v_mul_f32 v121, v121, v243\n"
      "v_mul_f32 v122, v122, v243\n"
      "v_mul_f32 v123, v123, v243\n"
      "v_sub_f32 v168, v168, v250\n"
      "v_sub_f32 v169, v169, v250\n"
      "v_sub_f32 v170, v170, v250\n"
      "v_sub_f32 v171, v171, v250\n"
      "v_sub_f32 v184, v184, v250\n"
      "v_sub_f32 v185, v185, v250\n"
      "v_sub_f32 v186, v186, v250\n"
      "v_sub_f32 v187, v187, v250\n"
      "v_sub_f32 v200, v200, v250\n"
      "v_sub_f32 v201, v201, v250\n"
      "v_sub_f32 v202, v202, v250\n"
      "v_sub_f32 v203, v203, v250\n"
      "v_mul_f32 v210, v210, v214\n"
      "v_lshlrev_b32 v252, 16, v136\n"
      "v_and_b32 v253, 0xffff0000, v136\n"
      "v_mul_f32 v252, v252, v214\n"
      "v_mul_f32 v253, v253, v214\n"
      "v_cvt_pk_bf16_f32 v136, v252, v253\n"
      "v_lshlrev_b32 v252, 16, v137\n"
      "v_and_b32 v253, 0xffff0000, v137\n"
      "v_mul_f32 v252, v252, v214\n"
      "v_mul_f32 v253, v253, v214\n"
      "v_cvt_pk_bf16_f32 v137, v252, v253\n"
      "v_lshlrev_b32 v252, 16, v138\n"
      "v_and_b32 v253, 0xffff0000, v138\n"
      "v_mul_f32 v252, v252, v214\n"
      "v_mul_f32 v253, v253, v214\n"
      "v_cvt_pk_bf16_f32 v138, v252, v253\n"
      "v_lshlrev_b32 v252, 16, v139\n"
      "v_and_b32 v253, 0xffff0000, v139\n"
      "v_mul_f32 v252, v252, v214\n"
      "v_mul_f32 v253, v253, v214\n"
      "v_cvt_pk_bf16_f32 v139, v252, v253\n"
      "v_lshlrev_b32 v252, 16, v152\n"
      "v_and_b32 v253, 0xffff0000, v152\n"
      "v_mul_f32 v252, v252, v246\n"
      "v_mul_f32 v253, v253, v246\n"
      "v_cvt_pk_bf16_f32 v152, v252, v253\n"
      "v_lshlrev_b32 v252, 16, v153\n"
      "v_and_b32 v253, 0xffff0000, v153\n"
      "v_mul_f32 v252, v252, v246\n"
      "v_mul_f32 v253, v253, v246\n"
      "v_cvt_pk_bf16_f32 v153, v252, v253\n"
      "v_lshlrev_b32 v252, 16, v154\n"
      "v_and_b32 v253, 0xffff0000, v154\n"
      "v_mul_f32 v252, v252, v246\n"
      "v_mul_f32 v253, v253, v246\n"
      "v_cvt_pk_bf16_f32 v154, v252, v253\n"
      "v_lshlrev_b32 v252, 16, v155\n"
      "v_and_b32 v253, 0xffff0000, v155\n"
      "v_mul_f32 v252, v252, v246\n"
      "v_mul_f32 v253, v253, v246\n"
      "v_cvt_pk_bf16_f32 v155, v252, v253\n"
      "v_mov_b32 v214, 0\n"
      "v_exp_f32_e64 v243, -v251\n"
      "v_sub_f32 v215, v215, v251\n"
      "v_sub_f32 v247, v247, v251\n"
      "v_exp_f32 v215, v215\n"
      "v_exp_f32 v247, v247\n"
      "v_mul_f32 v12, v12, v243\n"
      "v_mul_f32 v13, v13, v243\n"
      "v_mul_f32 v14, v14, v243\n"
      "v_mul_f32 v15, v15, v243\n"
      "v_mul_f32 v28, v28, v243\n"
      "v_mul_f32 v29, v29, v243\n"
      "v_mul_f32 v30, v30, v243\n"
      "v_mul_f32 v31, v31, v243\n"
      "v_mul_f32 v44, v44, v243\n"
      "v_mul_f32 v45, v45, v243\n"
      "v_mul_f32 v46, v46, v243\n"
      "v_mul_f32 v47, v47, v243\n"
      "v_mul_f32 v60, v60, v243\n"
      "v_mul_f32 v61, v61, v243\n"
      "v_mul_f32 v62, v62, v243\n"
      "v_mul_f32 v63, v63, v243\n"
      "v_mul_f32 v76, v76, v243\n"
      "v_mul_f32 v77, v77, v243\n"
      "v_mul_f32 v78, v78, v243\n"
      "v_mul_f32 v79, v79, v243\n"
      "v_mul_f32 v92, v92, v243\n"
      "v_mul_f32 v93, v93, v243\n"
      "v_mul_f32 v94, v94, v243\n"
      "v_mul_f32 v95, v95, v243\n"
      "v_mul_f32 v108, v108, v243\n"
      "v_mul_f32 v109, v109, v243\n"
      "v_mul_f32 v110, v110, v243\n"
      "v_mul_f32 v111, v111, v243\n"
      "v_mul_f32 v124, v124, v243\n"
      "v_mul_f32 v125, v125, v243\n"
      "v_mul_f32 v126, v126, v243\n"
      "v_mul_f32 v127, v127, v243\n"
      "v_sub_f32 v172, v172, v251\n"
      "v_sub_f32 v173, v173, v251\n"
      "v_sub_f32 v174, v174, v251\n"
      "v_sub_f32 v175, v175, v251\n"
      "v_sub_f32 v188, v188, v251\n"
      "v_sub_f32 v189, v189, v251\n"
      "v_sub_f32 v190, v190, v251\n"
      "v_sub_f32 v191, v191, v251\n"
      "v_sub_f32 v204, v204, v251\n"
      "v_sub_f32 v205, v205, v251\n"
      "v_sub_f32 v206, v206, v251\n"
      "v_sub_f32 v207, v207, v251\n"
      "v_mul_f32 v211, v211, v215\n"
      "v_lshlrev_b32 v252, 16, v140\n"
      "v_and_b32 v253, 0xffff0000, v140\n"
      "v_mul_f32 v252, v252, v215\n"
      "v_mul_f32 v253, v253, v215\n"
      "v_cvt_pk_bf16_f32 v140, v252, v253\n"
      "v_lshlrev_b32 v252, 16, v141\n"
      "v_and_b32 v253, 0xffff0000, v141\n"
      "v_mul_f32 v252, v252, v215\n"
      "v_mul_f32 v253, v253, v215\n"
      "v_cvt_pk_bf16_f32 v141, v252, v253\n"
      "v_lshlrev_b32 v252, 16, v142\n"
      "v_and_b32 v253, 0xffff0000, v142\n"
      "v_mul_f32 v252, v252, v215\n"
      "v_mul_f32 v253, v253, v215\n"
      "v_cvt_pk_bf16_f32 v142, v252, v253\n"
      "v_lshlrev_b32 v252, 16, v143\n"
      "v_and_b32 v253, 0xffff0000, v143\n"
      "v_mul_f32 v252, v252, v215\n"
      "v_mul_f32 v253, v253, v215\n"
      "v_cvt_pk_bf16_f32 v143, v252, v253\n"
      "v_lshlrev_b32 v252, 16, v156\n"
      "v_and_b32 v253, 0xffff0000, v156\n"
      "v_mul_f32 v252, v252, v247\n"
      "v_mul_f32 v253, v253, v247\n"
      "v_cvt_pk_bf16_f32 v156, v252, v253\n"
      "v_lshlrev_b32 v252, 16, v157\n"
      "v_and_b32 v253, 0xffff0000, v157\n"
      "v_mul_f32 v252, v252, v247\n"
      "v_mul_f32 v253, v253, v247\n"
      "v_cvt_pk_bf16_f32 v157, v252, v253\n"
      "v_lshlrev_b32 v252, 16, v158\n"
      "v_and_b32 v253, 0xffff0000, v158\n"
      "v_mul_f32 v252, v252, v247\n"
      "v_mul_f32 v253, v253, v247\n"
      "v_cvt_pk_bf16_f32 v158, v252, v253\n"
      "v_lshlrev_b32 v252, 16, v159\n"
      "v_and_b32 v253, 0xffff0000, v159\n"
      "v_mul_f32 v252, v252, v247\n"
      "v_mul_f32 v253, v253, v247\n"
      "v_cvt_pk_bf16_f32 v159, v252, v253\n"
      "v_mov_b32 v215, 0\n"
      "v_max3_f32 v216, v160, v161, v161\n"
      "v_max3_f32 v216, v162, v163, v216\n"
      "v_max3_f32 v216, v164, v165, v216\n"
      "v_max3_f32 v216, v166, v167, v216\n"
      "v_max3_f32 v216, v168, v169, v216\n"
      "v_max3_f32 v216, v170, v171, v216\n"
      "v_max3_f32 v216, v172, v173, v216\n"
      "v_max3_f32 v216, v174, v175, v216\n"
      "v_max3_f32 v216, v176, v177, v216\n"
      "v_max3_f32 v216, v178, v179, v216\n"
      "v_max3_f32 v216, v180, v181, v216\n"
      "v_max3_f32 v216, v182, v183, v216\n"
      "s_nop 1\n"
      "s_branch B2_2_%=\n"
      "R1_3_%=:\n"
      "v_max3_f32 v244, v160, v161, v162\n"
      "v_max3_f32 v244, v244, v163, v176\n"
      "v_max3_f32 v244, v244, v177, v178\n"
      "v_max_f32 v244, v244, v179\n"
      "v_max3_f32 v245, v164, v165, v166\n"
      "v_max3_f32 v245, v245, v167, v180\n"
      "v_max3_f32 v245, v245, v181, v182\n"
      "v_max_f32 v245, v245, v183\n"
      "v_max3_f32 v246, v168, v169, v170\n"
      "v_max3_f32 v246, v246, v171, v184\n"
      "v_max3_f32 v246, v246, v185, v186\n"
      "v_max_f32 v246, v246, v187\n"
      "v_max3_f32 v247, v172, v173, v174\n"
      "v_max3_f32 v247, v247, v175, v188\n"
      "v_max3_f32 v247, v247, v189, v190\n"
      "v_max_f32 v247, v247, v191\n"
      "v_mov_b32 v248, v244\n"
      "v_mov_b32 v249, v245\n"
      "v_mov_b32 v250, v246\n"
      "v_mov_b32 v251, v247\n"
      "s_nop 1\n"
      "v_permlane32_swap_b32 v244, v248\n"
      "v_permlane32_swap_b32 v245, v249\n"
      "v_permlane32_swap_b32 v246, v250\n"
      "v_permlane32_swap_b32 v247, v251\n"
      "s_nop 1\n"
      "v_max_f32 v244, v244, v248\n"
      "v_max_f32 v245, v245, v249\n"
      "v_max_f32 v246, v246, v250\n"
      "v_max_f32 v247, v247, v251\n"
      "v_mov_b32 v248, v244\n"
      "v_mov_b32 v249, v245\n"
      "v_mov_b32 v250, v246\n"
      "v_mov_b32 v251, v247\n"
      "s_nop 1\n"
      "v_permlane16_swap_b32 v244, v248\n"
      "v_permlane16_swap_b32 v245, v249\n"
      "v_permlane16_swap_b32 v246, v250\n"
      "v_permlane16_swap_b32 v247, v251\n"
      "s_nop 1\n"
      "v_max_f32 v244, v244, v248\n"
      "v_max_f32 v245, v245, v249\n"
      "v_max_f32 v246, v246, v250\n"
      "v_max_f32 v247, v247, v251\n"
      "v_add_f32 v244, 0x42cc0000, v244\n"
      "v_ceil_f32 v244, v244\n"
      "v_max_f32 v212, 0, v244\n"
      "v_exp_f32_e64 v248, -v212\n"
      "v_sub_f32 v160, v160, v212\n"
      "v_sub_f32 v161, v161, v212\n"
      "v_sub_f32 v162, v162, v212\n"
      "v_sub_f32 v163, v163, v212\n"
      "v_sub_f32 v176, v176, v212\n"
      "v_sub_f32 v177, v177, v212\n"
      "v_sub_f32 v178, v178, v212\n"
      "v_sub_f32 v179, v179, v212\n"
      "v_mul_f32 v208, v208, v248\n"
      "v_add_f32 v245, 0x42cc0000, v245\n"
      "v_ceil_f32 v245, v245\n"
      "v_max_f32 v213, 0, v245\n"
      "v_exp_f32_e64 v249, -v213\n"
      "v_sub_f32 v164, v164, v213\n"
      "v_sub_f32 v165, v165, v213\n"
      "v_sub_f32 v166, v166, v213\n"
      "v_sub_f32 v167, v167, v213\n"
      "v_sub_f32 v180, v180, v213\n"
      "v_sub_f32 v181, v181, v213\n"
      "v_sub_f32 v182, v182, v213\n"
      "v_sub_f32 v183, v183, v213\n"
      "v_mul_f32 v209, v209, v249\n"
      "v_add_f32 v246, 0x42cc0000, v246\n"
      "v_ceil_f32 v246, v246\n"
      "v_max_f32 v214, 0, v246\n"
      "v_exp_f32_e64 v250, -v214\n"
      "v_sub_f32 v168, v168, v214\n"
      "v_sub_f32 v169, v169, v214\n"
      "v_sub_f32 v170, v170, v214\n"
      "v_sub_f32 v171, v171, v214\n"
      "v_sub_f32 v184, v184, v214\n"
      "v_sub_f32 v185, v185, v214\n"
      "v_sub_f32 v186, v186, v214\n"
      "v_sub_f32 v187, v187, v214\n"
      "v_mul_f32 v210, v210, v250\n"
      "v_add_f32 v247, 0x42cc0000, v247\n"
      "v_ceil_f32 v247, v247\n"
      "v_max_f32 v215, 0, v247\n"
      "v_exp_f32_e64 v251, -v215\n"
      "v_sub_f32 v172, v172, v215\n"
      "v_sub_f32 v173, v173, v215\n"
      "v_sub_f32 v174, v174, v215\n"
      "v_sub_f32 v175, v175, v215\n"
      "v_sub_f32 v188, v188, v215\n"
      "v_sub_f32 v189, v189, v215\n"
      "v_sub_f32 v190, v190, v215\n"
      "v_sub_f32 v191, v191, v215\n"
      "v_mul_f32 v211, v211, v251\n"
      "v_add_u32 v243, s99, v242\n"
      "v_mov_b32 v252, s88\n"
      "s_lshl_b32 s84, s80, 3\n"
      "s_add_u32 s84, s84, 147520\n"
      "v_mov_b32 v253, s84\n"
      "ds_write_b128 v243, v[212:215] offset:1024\n"
      "ds_write_b32 v241, v252 offset:4\n"
      "ds_write_b32 v253, v252 offset:4\n"
      "s_waitcnt lgkmcnt(0)\n"
      "s_branch B1_4_%=\n"
      "R2_5_%=:\n"
      "s_nop 7\n"
      "v_add_u32 v243, s97, v242\n"
      "s_xor_b32 s84, s80, 1\n"
      "s_lshl_b32 s84, s84, 3\n"
      "s_add_u32 s84, s84, 147520\n"
      "v_mov_b32 v253, s84\n"
      "ds_read_b128 v[244:247], v243 offset:1024\n"
      "ds_read_b32 v252, v253 offset:4\n"
      "s_waitcnt lgkmcnt(0)\n"
      "v_cmp_eq_u32 vcc, s88, v252\n"
      "s_nop 1\n"
      "v_cndmask_b32 v244, 0, v244, vcc\n"
      "v_cndmask_b32 v245, 0, v245, vcc\n"
      "v_cndmask_b32 v246, 0, v246, vcc\n"
      "v_cndmask_b32 v247, 0, v247, vcc\n"
      "v_max_f32 v248, v212, v244\n"
      "v_max_f32 v249, v213, v245\n"
      "v_max_f32 v250, v214, v246\n"
      "v_max_f32 v251, v215, v247\n"
      "v_exp_f32_e64 v243, -v248\n"
      "v_sub_f32 v212, v212, v248\n"
      "v_sub_f32 v244, v244, v248\n"
      "v_exp_f32 v212, v212\n"
      "v_exp_f32 v244, v244\n"
      "v_mul_f32 v0, v0, v243\n"
      "v_mul_f32 v1, v1, v243\n"
      "v_mul_f32 v2, v2, v243\n"
      "v_mul_f32 v3, v3, v243\n"
      "v_mul_f32 v16, v16, v243\n"
      "v_mul_f32 v17, v17, v243\n"
      "v_mul_f32 v18, v18, v243\n"
      "v_mul_f32 v19, v19, v243\n"
      "v_mul_f32 v32, v32, v243\n"
      "v_mul_f32 v33, v33, v243\n"
      "v_mul_f32 v34, v34, v243\n"
      "v_mul_f32 v35, v35, v243\n"
      "v_mul_f32 v48, v48, v243\n"
      "v_mul_f32 v49, v49, v243\n"
      "v_mul_f32 v50, v50, v243\n"
      "v_mul_f32 v51, v51, v243\n"
      "v_mul_f32 v64, v64, v243\n"
      "v_mul_f32 v65, v65, v243\n"
      "v_mul_f32 v66, v66, v243\n"
      "v_mul_f32 v67, v67, v243\n"
      "v_mul_f32 v80, v80, v243\n"
      "v_mul_f32 v81, v81, v243\n"
      "v_mul_f32 v82, v82, v243\n"
      "v_mul_f32 v83, v83, v243\n"
      "v_mul_f32 v96, v96, v243\n"
      "v_mul_f32 v97, v97, v243\n"
      "v_mul_f32 v98, v98, v243\n"
      "v_mul_f32 v99, v99, v243\n"
      "v_mul_f32 v112, v112, v243\n"
      "v_mul_f32 v113, v113, v243\n"
      "v_mul_f32 v114, v114, v243\n"
      "v_mul_f32 v115, v115, v243\n"
      "v_sub_f32 v128, v128, v248\n"
      "v_sub_f32 v129, v129, v248\n"
      "v_sub_f32 v130, v130, v248\n"
      "v_sub_f32 v131, v131, v248\n"
      "v_sub_f32 v144, v144, v248\n"
      "v_sub_f32 v145, v145, v248\n"
      "v_sub_f32 v146, v146, v248\n"
      "v_sub_f32 v147, v147, v248\n"
      "v_sub_f32 v192, v192, v248\n"
      "v_sub_f32 v193, v193, v248\n"
      "v_sub_f32 v194, v194, v248\n"
      "v_sub_f32 v195, v195, v248\n"
      "v_mul_f32 v208, v208, v212\n"
      "v_lshlrev_b32 v252, 16, v160\n"
      "v_and_b32 v253, 0xffff0000, v160\n"
      "v_mul_f32 v252, v252, v212\n"
      "v_mul_f32 v253, v253, v212\n"
      "v_cvt_pk_bf16_f32 v160, v252, v253\n"
      "v_lshlrev_b32 v252, 16, v161\n"
      "v_and_b32 v253, 0xffff0000, v161\n"
      "v_mul_f32 v252, v252, v212\n"
      "v_mul_f32 v253, v253, v212\n"
      "v_cvt_pk_bf16_f32 v161, v252, v253\n"
      "v_lshlrev_b32 v252, 16, v162\n"
      "v_and_b32 v253, 0xffff0000, v162\n"
      "v_mul_f32 v252, v252, v212\n"
      "v_mul_f32 v253, v253, v212\n"
      "v_cvt_pk_bf16_f32 v162, v252, v253\n"
      "v_lshlrev_b32 v252, 16, v163\n"
      "v_and_b32 v253, 0xffff0000, v163\n"
      "v_mul_f32 v252, v252, v212\n"
      "v_mul_f32 v253, v253, v212\n"
      "v_cvt_pk_bf16_f32 v163, v252, v253\n"
      "v_lshlrev_b32 v252, 16, v176\n"
      "v_and_b32 v253, 0xffff0000, v176\n"
      "v_mul_f32 v252, v252, v244\n"
      "v_mul_f32 v253, v253, v244\n"
      "v_cvt_pk_bf16_f32 v176, v252, v253\n"
      "v_lshlrev_b32 v252, 16, v177\n"
      "v_and_b32 v253, 0xffff0000, v177\n"
      "v_mul_f32 v252, v252, v244\n"
      "v_mul_f32 v253, v253, v244\n"
      "v_cvt_pk_bf16_f32 v177, v252, v253\n"
      "v_lshlrev_b32 v252, 16, v178\n"
      "v_and_b32 v253, 0xffff0000, v178\n"
      "v_mul_f32 v252, v252, v244\n"
      "v_mul_f32 v253, v253, v244\n"
      "v_cvt_pk_bf16_f32 v178, v252, v253\n"
      "v_lshlrev_b32 v252, 16, v179\n"
      "v_and_b32 v253, 0xffff0000, v179\n"
      "v_mul_f32 v252, v252, v244\n"
      "v_mul_f32 v253, v253, v244\n"
      "v_cvt_pk_bf16_f32 v179, v252, v253\n"
      "v_mov_b32 v212, 0\n"
      "v_exp_f32_e64 v243, -v249\n"
      "v_sub_f32 v213, v213, v249\n"
      "v_sub_f32 v245, v245, v249\n"
      "v_exp_f32 v213, v213\n"
      "v_exp_f32 v245, v245\n"
      "v_mul_f32 v4, v4, v243\n"
      "v_mul_f32 v5, v5, v243\n"
      "v_mul_f32 v6, v6, v243\n"
      "v_mul_f32 v7, v7, v243\n"
      "v_mul_f32 v20, v20, v243\n"
      "v_mul_f32 v21, v21, v243\n"
      "v_mul_f32 v22, v22, v243\n"
      "v_mul_f32 v23, v23, v243\n"
      "v_mul_f32 v36, v36, v243\n"
      "v_mul_f32 v37, v37, v243\n"
      "v_mul_f32 v38, v38, v243\n"
      "v_mul_f32 v39, v39, v243\n"
      "v_mul_f32 v52, v52, v243\n"
      "v_mul_f32 v53, v53, v243\n"
      "v_mul_f32 v54, v54, v243\n"
      "v_mul_f32 v55, v55, v243\n"
      "v_mul_f32 v68, v68, v243\n"
      "v_mul_f32 v69, v69, v243\n"
      "v_mul_f32 v70, v70, v243\n"
      "v_mul_f32 v71, v71, v243\n"
      "v_mul_f32 v84, v84, v243\n"
      "v_mul_f32 v85, v85, v243\n"
      "v_mul_f32 v86, v86, v243\n"
      "v_mul_f32 v87, v87, v243\n"
      "v_mul_f32 v100, v100, v243\n"
      "v_mul_f32 v101, v101, v243\n"
      "v_mul_f32 v102, v102, v243\n"
      "v_mul_f32 v103, v103, v243\n"
      "v_mul_f32 v116, v116, v243\n"
      "v_mul_f32 v117, v117, v243\n"
      "v_mul_f32 v118, v118, v243\n"
      "v_mul_f32 v119, v119, v243\n"
      "v_sub_f32 v132, v132, v249\n"
      "v_sub_f32 v133, v133, v249\n"
      "v_sub_f32 v134, v134, v249\n"
      "v_sub_f32 v135, v135, v249\n"
      "v_sub_f32 v148, v148, v249\n"
      "v_sub_f32 v149, v149, v249\n"
      "v_sub_f32 v150, v150, v249\n"
      "v_sub_f32 v151, v151, v249\n"
      "v_sub_f32 v196, v196, v249\n"
      "v_sub_f32 v197, v197, v249\n"
      "v_sub_f32 v198, v198, v249\n"
      "v_sub_f32 v199, v199, v249\n"
      "v_mul_f32 v209, v209, v213\n"
      "v_lshlrev_b32 v252, 16, v164\n"
      "v_and_b32 v253, 0xffff0000, v164\n"
      "v_mul_f32 v252, v252, v213\n"
      "v_mul_f32 v253, v253, v213\n"
      "v_cvt_pk_bf16_f32 v164, v252, v253\n"
      "v_lshlrev_b32 v252, 16, v165\n"
      "v_and_b32 v253, 0xffff0000, v165\n"
      "v_mul_f32 v252, v252, v213\n"
      "v_mul_f32 v253, v253, v213\n"
      "v_cvt_pk_bf16_f32 v165, v252, v253\n"
      "v_lshlrev_b32 v252, 16, v166\n"
      "v_and_b32 v253, 0xffff0000, v166\n"
      "v_mul_f32 v252, v252, v213\n"
      "v_mul_f32 v253, v253, v213\n"
      "v_cvt_pk_bf16_f32 v166, v252, v253\n"
      "v_lshlrev_b32 v252, 16, v167\n"
      "v_and_b32 v253, 0xffff0000, v167\n"
      "v_mul_f32 v252, v252, v213\n"
      "v_mul_f32 v253, v253, v213\n"
      "v_cvt_pk_bf16_f32 v167, v252, v253\n"
      "v_lshlrev_b32 v252, 16, v180\n"
      "v_and_b32 v253, 0xffff0000, v180\n"
      "v_mul_f32 v252, v252, v245\n"
      "v_mul_f32 v253, v253, v245\n"
      "v_cvt_pk_bf16_f32 v180, v252, v253\n"
      "v_lshlrev_b32 v252, 16, v181\n"
      "v_and_b32 v253, 0xffff0000, v181\n"
      "v_mul_f32 v252, v252, v245\n"
      "v_mul_f32 v253, v253, v245\n"
      "v_cvt_pk_bf16_f32 v181, v252, v253\n"
      "v_lshlrev_b32 v252, 16, v182\n"
      "v_and_b32 v253, 0xffff0000, v182\n"
      "v_mul_f32 v252, v252, v245\n"
      "v_mul_f32 v253, v253, v245\n"
      "v_cvt_pk_bf16_f32 v182, v252, v253\n"
      "v_lshlrev_b32 v252, 16, v183\n"
      "v_and_b32 v253, 0xffff0000, v183\n"
      "v_mul_f32 v252, v252, v245\n"
      "v_mul_f32 v253, v253, v245\n"
      "v_cvt_pk_bf16_f32 v183, v252, v253\n"
      "v_mov_b32 v213, 0\n"
      "v_exp_f32_e64 v243, -v250\n"
      "v_sub_f32 v214, v214, v250\n"
      "v_sub_f32 v246, v246, v250\n"
      "v_exp_f32 v214, v214\n"
      "v_exp_f32 v246, v246\n"
      "v_mul_f32 v8, v8, v243\n"
      "v_mul_f32 v9, v9, v243\n"
      "v_mul_f32 v10, v10, v243\n"
      "v_mul_f32 v11, v11, v243\n"
      "v_mul_f32 v24, v24, v243\n"
      "v_mul_f32 v25, v25, v243\n"
      "v_mul_f32 v26, v26, v243\n"
      "v_mul_f32 v27, v27, v243\n"
      "v_mul_f32 v40, v40, v243\n"
      "v_mul_f32 v41, v41, v243\n"
      "v_mul_f32 v42, v42, v243\n"
      "v_mul_f32 v43, v43, v243\n"
      "v_mul_f32 v56, v56, v243\n"
      "v_mul_f32 v57, v57, v243\n"
      "v_mul_f32 v58, v58, v243\n"
      "v_mul_f32 v59, v59, v243\n"
      "v_mul_f32 v72, v72, v243\n"
      "v_mul_f32 v73, v73, v243\n"
      "v_mul_f32 v74, v74, v243\n"
      "v_mul_f32 v75, v75, v243\n"
      "v_mul_f32 v88, v88, v243\n"
      "v_mul_f32 v89, v89, v243\n"
      "v_mul_f32 v90, v90, v243\n"
      "v_mul_f32 v91, v91, v243\n"
      "v_mul_f32 v104, v104, v243\n"
      "v_mul_f32 v105, v105, v243\n"
      "v_mul_f32 v106, v106, v243\n"
      "v_mul_f32 v107, v107, v243\n"
      "v_mul_f32 v120, v120, v243\n"
      "v_mul_f32 v121, v121, v243\n"
      "v_mul_f32 v122, v122, v243\n"
      "v_mul_f32 v123, v123, v243\n"
      "v_sub_f32 v136, v136, v250\n"
      "v_sub_f32 v137, v137, v250\n"
      "v_sub_f32 v138, v138, v250\n"
      "v_sub_f32 v139, v139, v250\n"
      "v_sub_f32 v152, v152, v250\n"
      "v_sub_f32 v153, v153, v250\n"
      "v_sub_f32 v154, v154, v250\n"
      "v_sub_f32 v155, v155, v250\n"
      "v_sub_f32 v200, v200, v250\n"
      "v_sub_f32 v201, v201, v250\n"
      "v_sub_f32 v202, v202, v250\n"
      "v_sub_f32 v203, v203, v250\n"
      "v_mul_f32 v210, v210, v214\n"
      "v_lshlrev_b32 v252, 16, v168\n"
      "v_and_b32 v253, 0xffff0000, v168\n"
      "v_mul_f32 v252, v252, v214\n"
      "v_mul_f32 v253, v253, v214\n"
      "v_cvt_pk_bf16_f32 v168, v252, v253\n"
      "v_lshlrev_b32 v252, 16, v169\n"
      "v_and_b32 v253, 0xffff0000, v169\n"
      "v_mul_f32 v252, v252, v214\n"
      "v_mul_f32 v253, v253, v214\n"
      "v_cvt_pk_bf16_f32 v169, v252, v253\n"
      "v_lshlrev_b32 v252, 16, v170\n"
      "v_and_b32 v253, 0xffff0000, v170\n"
      "v_mul_f32 v252, v252, v214\n"
      "v_mul_f32 v253, v253, v214\n"
      "v_cvt_pk_bf16_f32 v170, v252, v253\n"
      "v_lshlrev_b32 v252, 16, v171\n"
      "v_and_b32 v253, 0xffff0000, v171\n"
      "v_mul_f32 v252, v252, v214\n"
      "v_mul_f32 v253, v253, v214\n"
      "v_cvt_pk_bf16_f32 v171, v252, v253\n"
      "v_lshlrev_b32 v252, 16, v184\n"
      "v_and_b32 v253, 0xffff0000, v184\n"
      "v_mul_f32 v252, v252, v246\n"
      "v_mul_f32 v253, v253, v246\n"
      "v_cvt_pk_bf16_f32 v184, v252, v253\n"
      "v_lshlrev_b32 v252, 16, v185\n"
      "v_and_b32 v253, 0xffff0000, v185\n"
      "v_mul_f32 v252, v252, v246\n"
      "v_mul_f32 v253, v253, v246\n"
      "v_cvt_pk_bf16_f32 v185, v252, v253\n"
      "v_lshlrev_b32 v252, 16, v186\n"
      "v_and_b32 v253, 0xffff0000, v186\n"
      "v_mul_f32 v252, v252, v246\n"
      "v_mul_f32 v253, v253, v246\n"
      "v_cvt_pk_bf16_f32 v186, v252, v253\n"
      "v_lshlrev_b32 v252, 16, v187\n"
      "v_and_b32 v253, 0xffff0000, v187\n"
      "v_mul_f32 v252, v252, v246\n"
      "v_mul_f32 v253, v253, v246\n"
      "v_cvt_pk_bf16_f32 v187, v252, v253\n"
      "v_mov_b32 v214, 0\n"
      "v_exp_f32_e64 v243, -v251\n"
      "v_sub_f32 v215, v215, v251\n"
      "v_sub_f32 v247, v247, v251\n"
      "v_exp_f32 v215, v215\n"
      "v_exp_f32 v247, v247\n"
      "v_mul_f32 v12, v12, v243\n"
      "v_mul_f32 v13, v13, v243\n"
      "v_mul_f32 v14, v14, v243\n"
      "v_mul_f32 v15, v15, v243\n"
      "v_mul_f32 v28, v28, v243\n"
      "v_mul_f32 v29, v29, v243\n"
      "v_mul_f32 v30, v30, v243\n"
      "v_mul_f32 v31, v31, v243\n"
      "v_mul_f32 v44, v44, v243\n"
      "v_mul_f32 v45, v45, v243\n"
      "v_mul_f32 v46, v46, v243\n"
      "v_mul_f32 v47, v47, v243\n"
      "v_mul_f32 v60, v60, v243\n"
      "v_mul_f32 v61, v61, v243\n"
      "v_mul_f32 v62, v62, v243\n"
      "v_mul_f32 v63, v63, v243\n"
      "v_mul_f32 v76, v76, v243\n"
      "v_mul_f32 v77, v77, v243\n"
      "v_mul_f32 v78, v78, v243\n"
      "v_mul_f32 v79, v79, v243\n"
      "v_mul_f32 v92, v92, v243\n"
      "v_mul_f32 v93, v93, v243\n"
      "v_mul_f32 v94, v94, v243\n"
      "v_mul_f32 v95, v95, v243\n"
      "v_mul_f32 v108, v108, v243\n"
      "v_mul_f32 v109, v109, v243\n"
      "v_mul_f32 v110, v110, v243\n"
      "v_mul_f32 v111, v111, v243\n"
      "v_mul_f32 v124, v124, v243\n"
      "v_mul_f32 v125, v125, v243\n"
      "v_mul_f32 v126, v126, v243\n"
      "v_mul_f32 v127, v127, v243\n"
      "v_sub_f32 v140, v140, v251\n"
      "v_sub_f32 v141, v141, v251\n"
      "v_sub_f32 v142, v142, v251\n"
      "v_sub_f32 v143, v143, v251\n"
      "v_sub_f32 v156, v156, v251\n"
      "v_sub_f32 v157, v157, v251\n"
      "v_sub_f32 v158, v158, v251\n"
      "v_sub_f32 v159, v159, v251\n"
      "v_sub_f32 v204, v204, v251\n"
      "v_sub_f32 v205, v205, v251\n"
      "v_sub_f32 v206, v206, v251\n"
      "v_sub_f32 v207, v207, v251\n"
      "v_mul_f32 v211, v211, v215\n"
      "v_lshlrev_b32 v252, 16, v172\n"
      "v_and_b32 v253, 0xffff0000, v172\n"
      "v_mul_f32 v252, v252, v215\n"
      "v_mul_f32 v253, v253, v215\n"
      "v_cvt_pk_bf16_f32 v172, v252, v253\n"
      "v_lshlrev_b32 v252, 16, v173\n"
      "v_and_b32 v253, 0xffff0000, v173\n"
      "v_mul_f32 v252, v252, v215\n"
      "v_mul_f32 v253, v253, v215\n"
      "v_cvt_pk_bf16_f32 v173, v252, v253\n"
      "v_lshlrev_b32 v252, 16, v174\n"
      "v_and_b32 v253, 0xffff0000, v174\n"
      "v_mul_f32 v252, v252, v215\n"
      "v_mul_f32 v253, v253, v215\n"
      "v_cvt_pk_bf16_f32 v174, v252, v253\n"
      "v_lshlrev_b32 v252, 16, v175\n"
      "v_and_b32 v253, 0xffff0000, v175\n"
      "v_mul_f32 v252, v252, v215\n"
      "v_mul_f32 v253, v253, v215\n"
      "v_cvt_pk_bf16_f32 v175, v252, v253\n"
      "v_lshlrev_b32 v252, 16, v188\n"
      "v_and_b32 v253, 0xffff0000, v188\n"
      "v_mul_f32 v252, v252, v247\n"
      "v_mul_f32 v253, v253, v247\n"
      "v_cvt_pk_bf16_f32 v188, v252, v253\n"
      "v_lshlrev_b32 v252, 16, v189\n"
      "v_and_b32 v253, 0xffff0000, v189\n"
      "v_mul_f32 v252, v252, v247\n"
      "v_mul_f32 v253, v253, v247\n"
      "v_cvt_pk_bf16_f32 v189, v252, v253\n"
      "v_lshlrev_b32 v252, 16, v190\n"
      "v_and_b32 v253, 0xffff0000, v190\n"
      "v_mul_f32 v252, v252, v247\n"
      "v_mul_f32 v253, v253, v247\n"
      "v_cvt_pk_bf16_f32 v190, v252, v253\n"
      "v_lshlrev_b32 v252, 16, v191\n"
      "v_and_b32 v253, 0xffff0000, v191\n"
      "v_mul_f32 v252, v252, v247\n"
      "v_mul_f32 v253, v253, v247\n"
      "v_cvt_pk_bf16_f32 v191, v252, v253\n"
      "v_mov_b32 v215, 0\n"
      "v_max3_f32 v216, v128, v129, v129\n"
      "v_max3_f32 v216, v130, v131, v216\n"
      "v_max3_f32 v216, v132, v133, v216\n"
      "v_max3_f32 v216, v134, v135, v216\n"
      "v_max3_f32 v216, v136, v137, v216\n"
      "v_max3_f32 v216, v138, v139, v216\n"
      "v_max3_f32 v216, v140, v141, v216\n"
      "v_max3_f32 v216, v142, v143, v216\n"
      "v_max3_f32 v216, v144, v145, v216\n"
      "v_max3_f32 v216, v146, v147, v216\n"
      "v_max3_f32 v216, v148, v149, v216\n"
      "v_max3_f32 v216, v150, v151, v216\n"
      "s_nop 1\n"
      "s_branch B2_6_%=\n"
      "R1_7_%=:\n"
      "v_max3_f32 v244, v128, v129, v130\n"
      "v_max3_f32 v244, v244, v131, v144\n"
      "v_max3_f32 v244, v244, v145, v146\n"
      "v_max_f32 v244, v244, v147\n"
      "v_max3_f32 v245, v132, v133, v134\n"
      "v_max3_f32 v245, v245, v135, v148\n"
      "v_max3_f32 v245, v245, v149, v150\n"
      "v_max_f32 v245, v245, v151\n"
      "v_max3_f32 v246, v136, v137, v138\n"
      "v_max3_f32 v246, v246, v139, v152\n"
      "v_max3_f32 v246, v246, v153, v154\n"
      "v_max_f32 v246, v246, v155\n"
      "v_max3_f32 v247, v140, v141, v142\n"
      "v_max3_f32 v247, v247, v143, v156\n"
      "v_max3_f32 v247, v247, v157, v158\n"
      "v_max_f32 v247, v247, v159\n"
      "v_mov_b32 v248, v244\n"
      "v_mov_b32 v249, v245\n"
      "v_mov_b32 v250, v246\n"
      "v_mov_b32 v251, v247\n"
      "s_nop 1\n"
      "v_permlane32_swap_b32 v244, v248\n"
      "v_permlane32_swap_b32 v245, v249\n"
      "v_permlane32_swap_b32 v246, v250\n"
      "v_permlane32_swap_b32 v247, v251\n"
      "s_nop 1\n"
      "v_max_f32 v244, v244, v248\n"
      "v_max_f32 v245, v245, v249\n"
      "v_max_f32 v246, v246, v250\n"
      "v_max_f32 v247, v247, v251\n"
      "v_mov_b32 v248, v244\n"
      "v_mov_b32 v249, v245\n"
      "v_mov_b32 v250, v246\n"
      "v_mov_b32 v251, v247\n"
      "s_nop 1\n"
      "v_permlane16_swap_b32 v244, v248\n"
      "v_permlane16_swap_b32 v245, v249\n"
      "v_permlane16_swap_b32 v246, v250\n"
      "v_permlane16_swap_b32 v247, v251\n"
      "s_nop 1\n"
      "v_max_f32 v244, v244, v248\n"
      "v_max_f32 v245, v245, v249\n"
      "v_max_f32 v246, v246, v250\n"
      "v_max_f32 v247, v247, v251\n"
      "v_add_f32 v244, 0x42cc0000, v244\n"
      "v_ceil_f32 v244, v244\n"
      "v_max_f32 v212, 0, v244\n"
      "v_exp_f32_e64 v248, -v212\n"
      "v_sub_f32 v128, v128, v212\n"
      "v_sub_f32 v129, v129, v212\n"
      "v_sub_f32 v130, v130, v212\n"
      "v_sub_f32 v131, v131, v212\n"
      "v_sub_f32 v144, v144, v212\n"
      "v_sub_f32 v145, v145, v212\n"
      "v_sub_f32 v146, v146, v212\n"
      "v_sub_f32 v147, v147, v212\n"
      "v_mul_f32 v208, v208, v248\n"
      "v_add_f32 v245, 0x42cc0000, v245\n"
      "v_ceil_f32 v245, v245\n"
      "v_max_f32 v213, 0, v245\n"
      "v_exp_f32_e64 v249, -v213\n"
      "v_sub_f32 v132, v132, v213\n"
      "v_sub_f32 v133, v133, v213\n"
      "v_sub_f32 v134, v134, v213\n"
      "v_sub_f32 v135, v135, v213\n"
      "v_sub_f32 v148, v148, v213\n"
      "v_sub_f32 v149, v149, v213\n"
      "v_sub_f32 v150, v150, v213\n"
      "v_sub_f32 v151, v151, v213\n"
      "v_mul_f32 v209, v209, v249\n"
      "v_add_f32 v246, 0x42cc0000, v246\n"
      "v_ceil_f32 v246, v246\n"
      "v_max_f32 v214, 0, v246\n"
      "v_exp_f32_e64 v250, -v214\n"
      "v_sub_f32 v136, v136, v214\n"
      "v_sub_f32 v137, v137, v214\n"
      "v_sub_f32 v138, v138, v214\n"
      "v_sub_f32 v139, v139, v214\n"
      "v_sub_f32 v152, v152, v214\n"
      "v_sub_f32 v153, v153, v214\n"
      "v_sub_f32 v154, v154, v214\n"
      "v_sub_f32 v155, v155, v214\n"
      "v_mul_f32 v210, v210, v250\n"
      "v_add_f32 v247, 0x42cc0000, v247\n"
      "v_ceil_f32 v247, v247\n"
      "v_max_f32 v215, 0, v247\n"
      "v_exp_f32_e64 v251, -v215\n"
      "v_sub_f32 v140, v140, v215\n"
      "v_sub_f32 v141, v141, v215\n"
      "v_sub_f32 v142, v142, v215\n"
      "v_sub_f32 v143, v143, v215\n"
      "v_sub_f32 v156, v156, v215\n"
      "v_sub_f32 v157, v157, v215\n"
      "v_sub_f32 v158, v158, v215\n"
      "v_sub_f32 v159, v159, v215\n"
      "v_mul_f32 v211, v211, v251\n"
      "v_add_u32 v243, s99, v242\n"
      "v_mov_b32 v252, s88\n"
      "s_lshl_b32 s84, s80, 3\n"
      "s_add_u32 s84, s84, 147520\n"
      "v_mov_b32 v253, s84\n"
      "ds_write_b128 v243, v[212:215] offset:0\n"
      "ds_write_b32 v241, v252 offset:0\n"
      "ds_write_b32 v253, v252 offset:0\n"
      "s_waitcnt lgkmcnt(0)\n"
      "s_branch B1_8_%=\n"
      "R2_9_%=:\n"
      "s_nop 7\n"
      "v_add_u32 v243, s97, v242\n"
      "s_xor_b32 s84, s80, 1\n"
      "s_lshl_b32 s84, s84, 3\n"
      "s_add_u32 s84, s84, 147520\n"
      "v_mov_b32 v253, s84\n"
      "ds_read_b128 v[244:247], v243 offset:0\n"
      "ds_read_b32 v252, v253 offset:0\n"
      "s_waitcnt lgkmcnt(0)\n"
      "v_cmp_eq_u32 vcc, s88, v252\n"
      "s_nop 1\n"
      "v_cndmask_b32 v244, 0, v244, vcc\n"
      "v_cndmask_b32 v245, 0, v245, vcc\n"
      "v_cndmask_b32 v246, 0, v246, vcc\n"
      "v_cndmask_b32 v247, 0, v247, vcc\n"
      "v_max_f32 v248, v212, v244\n"
      "v_max_f32 v249, v213, v245\n"
      "v_max_f32 v250, v214, v246\n"
      "v_max_f32 v251, v215, v247\n"
      "v_exp_f32_e64 v243, -v248\n"
      "v_sub_f32 v212, v212, v248\n"
      "v_sub_f32 v244, v244, v248\n"
      "v_exp_f32 v212, v212\n"
      "v_exp_f32 v244, v244\n"
      "v_mul_f32 v0, v0, v243\n"
      "v_mul_f32 v1, v1, v243\n"
      "v_mul_f32 v2, v2, v243\n"
      "v_mul_f32 v3, v3, v243\n"
      "v_mul_f32 v16, v16, v243\n"
      "v_mul_f32 v17, v17, v243\n"
      "v_mul_f32 v18, v18, v243\n"
      "v_mul_f32 v19, v19, v243\n"
      "v_mul_f32 v32, v32, v243\n"
      "v_mul_f32 v33, v33, v243\n"
      "v_mul_f32 v34, v34, v243\n"
      "v_mul_f32 v35, v35, v243\n"
      "v_mul_f32 v48, v48, v243\n"
      "v_mul_f32 v49, v49, v243\n"
      "v_mul_f32 v50, v50, v243\n"
      "v_mul_f32 v51, v51, v243\n"
      "v_mul_f32 v64, v64, v243\n"
      "v_mul_f32 v65, v65, v243\n"
      "v_mul_f32 v66, v66, v243\n"
      "v_mul_f32 v67, v67, v243\n"
      "v_mul_f32 v80, v80, v243\n"
      "v_mul_f32 v81, v81, v243\n"
      "v_mul_f32 v82, v82, v243\n"
      "v_mul_f32 v83, v83, v243\n"
      "v_mul_f32 v96, v96, v243\n"
      "v_mul_f32 v97, v97, v243\n"
      "v_mul_f32 v98, v98, v243\n"
      "v_mul_f32 v99, v99, v243\n"
      "v_mul_f32 v112, v112, v243\n"
      "v_mul_f32 v113, v113, v243\n"
      "v_mul_f32 v114, v114, v243\n"
      "v_mul_f32 v115, v115, v243\n"
      "v_sub_f32 v160, v160, v248\n"
      "v_sub_f32 v161, v161, v248\n"
      "v_sub_f32 v162, v162, v248\n"
      "v_sub_f32 v163, v163, v248\n"
      "v_sub_f32 v176, v176, v248\n"
      "v_sub_f32 v177, v177, v248\n"
      "v_sub_f32 v178, v178, v248\n"
      "v_sub_f32 v179, v179, v248\n"
      "v_sub_f32 v192, v192, v248\n"
      "v_sub_f32 v193, v193, v248\n"
      "v_sub_f32 v194, v194, v248\n"
      "v_sub_f32 v195, v195, v248\n"
      "v_mul_f32 v208, v208, v212\n"
      "v_lshlrev_b32 v252, 16, v128\n"
      "v_and_b32 v253, 0xffff0000, v128\n"
      "v_mul_f32 v252, v252, v212\n"
      "v_mul_f32 v253, v253, v212\n"
      "v_cvt_pk_bf16_f32 v128, v252, v253\n"
      "v_lshlrev_b32 v252, 16, v129\n"
      "v_and_b32 v253, 0xffff0000, v129\n"
      "v_mul_f32 v252, v252, v212\n"
      "v_mul_f32 v253, v253, v212\n"
      "v_cvt_pk_bf16_f32 v129, v252, v253\n"
      "v_lshlrev_b32 v252, 16, v130\n"
      "v_and_b32 v253, 0xffff0000, v130\n"
      "v_mul_f32 v252, v252, v212\n"
      "v_mul_f32 v253, v253, v212\n"
      "v_cvt_pk_bf16_f32 v130, v252, v253\n"
      "v_lshlrev_b32 v252, 16, v131\n"
      "v_and_b32 v253, 0xffff0000, v131\n"
      "v_mul_f32 v252, v252, v212\n"
      "v_mul_f32 v253, v253, v212\n"
      "v_cvt_pk_bf16_f32 v131, v252, v253\n"
      "v_lshlrev_b32 v252, 16, v144\n"
      "v_and_b32 v253, 0xffff0000, v144\n"
      "v_mul_f32 v252, v252, v244\n"
      "v_mul_f32 v253, v253, v244\n"
      "v_cvt_pk_bf16_f32 v144, v252, v253\n"
      "v_lshlrev_b32 v252, 16, v145\n"
      "v_and_b32 v253, 0xffff0000, v145\n"
      "v_mul_f32 v252, v252, v244\n"
      "v_mul_f32 v253, v253, v244\n"
      "v_cvt_pk_bf16_f32 v145, v252, v253\n"
      "v_lshlrev_b32 v252, 16, v146\n"
      "v_and_b32 v253, 0xffff0000, v146\n"
      "v_mul_f32 v252, v252, v244\n"
      "v_mul_f32 v253, v253, v244\n"
      "v_cvt_pk_bf16_f32 v146, v252, v253\n"
      "v_lshlrev_b32 v252, 16, v147\n"
      "v_and_b32 v253, 0xffff0000, v147\n"
      "v_mul_f32 v252, v252, v244\n"
      "v_mul_f32 v253, v253, v244\n"
      "v_cvt_pk_bf16_f32 v147, v252, v253\n"
      "v_mov_b32 v212, 0\n"
      "v_exp_f32_e64 v243, -v249\n"
      "v_sub_f32 v213, v213, v249\n"
      "v_sub_f32 v245, v245, v249\n"
      "v_exp_f32 v213, v213\n"
      "v_exp_f32 v245, v245\n"
      "v_mul_f32 v4, v4, v243\n"
      "v_mul_f32 v5, v5, v243\n"
      "v_mul_f32 v6, v6, v243\n"
      "v_mul_f32 v7, v7, v243\n"
      "v_mul_f32 v20, v20, v243\n"
      "v_mul_f32 v21, v21, v243\n"
      "v_mul_f32 v22, v22, v243\n"
      "v_mul_f32 v23, v23, v243\n"
      "v_mul_f32 v36, v36, v243\n"
      "v_mul_f32 v37, v37, v243\n"
      "v_mul_f32 v38, v38, v243\n"
      "v_mul_f32 v39, v39, v243\n"
      "v_mul_f32 v52, v52, v243\n"
      "v_mul_f32 v53, v53, v243\n"
      "v_mul_f32 v54, v54, v243\n"
      "v_mul_f32 v55, v55, v243\n"
      "v_mul_f32 v68, v68, v243\n"
      "v_mul_f32 v69, v69, v243\n"
      "v_mul_f32 v70, v70, v243\n"
      "v_mul_f32 v71, v71, v243\n"
      "v_mul_f32 v84, v84, v243\n"
      "v_mul_f32 v85, v85, v243\n"
      "v_mul_f32 v86, v86, v243\n"
      "v_mul_f32 v87, v87, v243\n"
      "v_mul_f32 v100, v100, v243\n"
      "v_mul_f32 v101, v101, v243\n"
      "v_mul_f32 v102, v102, v243\n"
      "v_mul_f32 v103, v103, v243\n"
      "v_mul_f32 v116, v116, v243\n"
      "v_mul_f32 v117, v117, v243\n"
      "v_mul_f32 v118, v118, v243\n"
      "v_mul_f32 v119, v119, v243\n"
      "v_sub_f32 v164, v164, v249\n"
      "v_sub_f32 v165, v165, v249\n"
      "v_sub_f32 v166, v166, v249\n"
      "v_sub_f32 v167, v167, v249\n"
      "v_sub_f32 v180, v180, v249\n"
      "v_sub_f32 v181, v181, v249\n"
      "v_sub_f32 v182, v182, v249\n"
      "v_sub_f32 v183, v183, v249\n"
      "v_sub_f32 v196, v196, v249\n"
      "v_sub_f32 v197, v197, v249\n"
      "v_sub_f32 v198, v198, v249\n"
      "v_sub_f32 v199, v199, v249\n"
      "v_mul_f32 v209, v209, v213\n"
      "v_lshlrev_b32 v252, 16, v132\n"
      "v_and_b32 v253, 0xffff0000, v132\n"
      "v_mul_f32 v252, v252, v213\n"
      "v_mul_f32 v253, v253, v213\n"
      "v_cvt_pk_bf16_f32 v132, v252, v253\n"
      "v_lshlrev_b32 v252, 16, v133\n"
      "v_and_b32 v253, 0xffff0000, v133\n"
      "v_mul_f32 v252, v252, v213\n"
      "v_mul_f32 v253, v253, v213\n"
      "v_cvt_pk_bf16_f32 v133, v252, v253\n"
      "v_lshlrev_b32 v252, 16, v134\n"
      "v_and_b32 v253, 0xffff0000, v134\n"
      "v_mul_f32 v252, v252, v213\n"
      "v_mul_f32 v253, v253, v213\n"
      "v_cvt_pk_bf16_f32 v134, v252, v253\n"
      "v_lshlrev_b32 v252, 16, v135\n"
      "v_and_b32 v253, 0xffff0000, v135\n"
      "v_mul_f32 v252, v252, v213\n"
      "v_mul_f32 v253, v253, v213\n"
      "v_cvt_pk_bf16_f32 v135, v252, v253\n"
      "v_lshlrev_b32 v252, 16, v148\n"
      "v_and_b32 v253, 0xffff0000, v148\n"
      "v_mul_f32 v252, v252, v245\n"
      "v_mul_f32 v253, v253, v245\n"
      "v_cvt_pk_bf16_f32 v148, v252, v253\n"
      "v_lshlrev_b32 v252, 16, v149\n"
      "v_and_b32 v253, 0xffff0000, v149\n"
      "v_mul_f32 v252, v252, v245\n"
      "v_mul_f32 v253, v253, v245\n"
      "v_cvt_pk_bf16_f32 v149, v252, v253\n"
      "v_lshlrev_b32 v252, 16, v150\n"
      "v_and_b32 v253, 0xffff0000, v150\n"
      "v_mul_f32 v252, v252, v245\n"
      "v_mul_f32 v253, v253, v245\n"
      "v_cvt_pk_bf16_f32 v150, v252, v253\n"
      "v_lshlrev_b32 v252, 16, v151\n"
      "v_and_b32 v253, 0xffff0000, v151\n"
      "v_mul_f32 v252, v252, v245\n"
      "v_mul_f32 v253, v253, v245\n"
      "v_cvt_pk_bf16_f32 v151, v252, v253\n"
      "v_mov_b32 v213, 0\n"
      "v_exp_f32_e64 v243, -v250\n"
      "v_sub_f32 v214, v214, v250\n"
      "v_sub_f32 v246, v246, v250\n"
      "v_exp_f32 v214, v214\n"
      "v_exp_f32 v246, v246\n"
      "v_mul_f32 v8, v8, v243\n"
      "v_mul_f32 v9, v9, v243\n"
      "v_mul_f32 v10, v10, v243\n"
      "v_mul_f32 v11, v11, v243\n"
      "v_mul_f32 v24, v24, v243\n"
      "v_mul_f32 v25, v25, v243\n"
      "v_mul_f32 v26, v26, v243\n"
      "v_mul_f32 v27, v27, v243\n"
      "v_mul_f32 v40, v40, v243\n"
      "v_mul_f32 v41, v41, v243\n"
      "v_mul_f32 v42, v42, v243\n"
      "v_mul_f32 v43, v43, v243\n"
      "v_mul_f32 v56, v56, v243\n"
      "v_mul_f32 v57, v57, v243\n"
      "v_mul_f32 v58, v58, v243\n"
      "v_mul_f32 v59, v59, v243\n"
      "v_mul_f32 v72, v72, v243\n"
      "v_mul_f32 v73, v73, v243\n"
      "v_mul_f32 v74, v74, v243\n"
      "v_mul_f32 v75, v75, v243\n"
      "v_mul_f32 v88, v88, v243\n"
      "v_mul_f32 v89, v89, v243\n"
      "v_mul_f32 v90, v90, v243\n"
      "v_mul_f32 v91, v91, v243\n"
      "v_mul_f32 v104, v104, v243\n"
      "v_mul_f32 v105, v105, v243\n"
      "v_mul_f32 v106, v106, v243\n"
      "v_mul_f32 v107, v107, v243\n"
      "v_mul_f32 v120, v120, v243\n"
      "v_mul_f32 v121, v121, v243\n"
      "v_mul_f32 v122, v122, v243\n"
      "v_mul_f32 v123, v123, v243\n"
      "v_sub_f32 v168, v168, v250\n"
      "v_sub_f32 v169, v169, v250\n"
      "v_sub_f32 v170, v170, v250\n"
      "v_sub_f32 v171, v171, v250\n"
      "v_sub_f32 v184, v184, v250\n"
      "v_sub_f32 v185, v185, v250\n"
      "v_sub_f32 v186, v186, v250\n"
      "v_sub_f32 v187, v187, v250\n"
      "v_sub_f32 v200, v200, v250\n"
      "v_sub_f32 v201, v201, v250\n"
      "v_sub_f32 v202, v202, v250\n"
      "v_sub_f32 v203, v203, v250\n"
      "v_mul_f32 v210, v210, v214\n"
      "v_lshlrev_b32 v252, 16, v136\n"
      "v_and_b32 v253, 0xffff0000, v136\n"
      "v_mul_f32 v252, v252, v214\n"
      "v_mul_f32 v253, v253, v214\n"
      "v_cvt_pk_bf16_f32 v136, v252, v253\n"
      "v_lshlrev_b32 v252, 16, v137\n"
      "v_and_b32 v253, 0xffff0000, v137\n"
      "v_mul_f32 v252, v252, v214\n"
      "v_mul_f32 v253, v253, v214\n"
      "v_cvt_pk_bf16_f32 v137, v252, v253\n"
      "v_lshlrev_b32 v252, 16, v138\n"
      "v_and_b32 v253, 0xffff0000, v138\n"
      "v_mul_f32 v252, v252, v214\n"
      "v_mul_f32 v253, v253, v214\n"
      "v_cvt_pk_bf16_f32 v138, v252, v253\n"
      "v_lshlrev_b32 v252, 16, v139\n"
      "v_and_b32 v253, 0xffff0000, v139\n"
      "v_mul_f32 v252, v252, v214\n"
      "v_mul_f32 v253, v253, v214\n"
      "v_cvt_pk_bf16_f32 v139, v252, v253\n"
      "v_lshlrev_b32 v252, 16, v152\n"
      "v_and_b32 v253, 0xffff0000, v152\n"
      "v_mul_f32 v252, v252, v246\n"
      "v_mul_f32 v253, v253, v246\n"
      "v_cvt_pk_bf16_f32 v152, v252, v253\n"
      "v_lshlrev_b32 v252, 16, v153\n"
      "v_and_b32 v253, 0xffff0000, v153\n"
      "v_mul_f32 v252, v252, v246\n"
      "v_mul_f32 v253, v253, v246\n"
      "v_cvt_pk_bf16_f32 v153, v252, v253\n"
      "v_lshlrev_b32 v252, 16, v154\n"
      "v_and_b32 v253, 0xffff0000, v154\n"
      "v_mul_f32 v252, v252, v246\n"
      "v_mul_f32 v253, v253, v246\n"
      "v_cvt_pk_bf16_f32 v154, v252, v253\n"
      "v_lshlrev_b32 v252, 16, v155\n"
      "v_and_b32 v253, 0xffff0000, v155\n"
      "v_mul_f32 v252, v252, v246\n"
      "v_mul_f32 v253, v253, v246\n"
      "v_cvt_pk_bf16_f32 v155, v252, v253\n"
      "v_mov_b32 v214, 0\n"
      "v_exp_f32_e64 v243, -v251\n"
      "v_sub_f32 v215, v215, v251\n"
      "v_sub_f32 v247, v247, v251\n"
      "v_exp_f32 v215, v215\n"
      "v_exp_f32 v247, v247\n"
      "v_mul_f32 v12, v12, v243\n"
      "v_mul_f32 v13, v13, v243\n"
      "v_mul_f32 v14, v14, v243\n"
      "v_mul_f32 v15, v15, v243\n"
      "v_mul_f32 v28, v28, v243\n"
      "v_mul_f32 v29, v29, v243\n"
      "v_mul_f32 v30, v30, v243\n"
      "v_mul_f32 v31, v31, v243\n"
      "v_mul_f32 v44, v44, v243\n"
      "v_mul_f32 v45, v45, v243\n"
      "v_mul_f32 v46, v46, v243\n"
      "v_mul_f32 v47, v47, v243\n"
      "v_mul_f32 v60, v60, v243\n"
      "v_mul_f32 v61, v61, v243\n"
      "v_mul_f32 v62, v62, v243\n"
      "v_mul_f32 v63, v63, v243\n"
      "v_mul_f32 v76, v76, v243\n"
      "v_mul_f32 v77, v77, v243\n"
      "v_mul_f32 v78, v78, v243\n"
      "v_mul_f32 v79, v79, v243\n"
      "v_mul_f32 v92, v92, v243\n"
      "v_mul_f32 v93, v93, v243\n"
      "v_mul_f32 v94, v94, v243\n"
      "v_mul_f32 v95, v95, v243\n"
      "v_mul_f32 v108, v108, v243\n"
      "v_mul_f32 v109, v109, v243\n"
      "v_mul_f32 v110, v110, v243\n"
      "v_mul_f32 v111, v111, v243\n"
      "v_mul_f32 v124, v124, v243\n"
      "v_mul_f32 v125, v125, v243\n"
      "v_mul_f32 v126, v126, v243\n"
      "v_mul_f32 v127, v127, v243\n"
      "v_sub_f32 v172, v172, v251\n"
      "v_sub_f32 v173, v173, v251\n"
      "v_sub_f32 v174, v174, v251\n"
      "v_sub_f32 v175, v175, v251\n"
      "v_sub_f32 v188, v188, v251\n"
      "v_sub_f32 v189, v189, v251\n"
      "v_sub_f32 v190, v190, v251\n"
      "v_sub_f32 v191, v191, v251\n"
      "v_sub_f32 v204, v204, v251\n"
      "v_sub_f32 v205, v205, v251\n"
      "v_sub_f32 v206, v206, v251\n"
      "v_sub_f32 v207, v207, v251\n"
      "v_mul_f32 v211, v211, v215\n"
      "v_lshlrev_b32 v252, 16, v140\n"
      "v_and_b32 v253, 0xffff0000, v140\n"
      "v_mul_f32 v252, v252, v215\n"
      "v_mul_f32 v253, v253, v215\n"
      "v_cvt_pk_bf16_f32 v140, v252, v253\n"
      "v_lshlrev_b32 v252, 16, v141\n"
      "v_and_b32 v253, 0xffff0000, v141\n"
      "v_mul_f32 v252, v252, v215\n"
      "v_mul_f32 v253, v253, v215\n"
      "v_cvt_pk_bf16_f32 v141, v252, v253\n"
      "v_lshlrev_b32 v252, 16, v142\n"
      "v_and_b32 v253, 0xffff0000, v142\n"
      "v_mul_f32 v252, v252, v215\n"
      "v_mul_f32 v253, v253, v215\n"
      "v_cvt_pk_bf16_f32 v142, v252, v253\n"
      "v_lshlrev_b32 v252, 16, v143\n"
      "v_and_b32 v253, 0xffff0000, v143\n"
      "v_mul_f32 v252, v252, v215\n"
      "v_mul_f32 v253, v253, v215\n"
      "v_cvt_pk_bf16_f32 v143, v252, v253\n"
      "v_lshlrev_b32 v252, 16, v156\n"
      "v_and_b32 v253, 0xffff0000, v156\n"
      "v_mul_f32 v252, v252, v247\n"
      "v_mul_f32 v253, v253, v247\n"
      "v_cvt_pk_bf16_f32 v156, v252, v253\n"
      "v_lshlrev_b32 v252, 16, v157\n"
      "v_and_b32 v253, 0xffff0000, v157\n"
      "v_mul_f32 v252, v252, v247\n"
      "v_mul_f32 v253, v253, v247\n"
      "v_cvt_pk_bf16_f32 v157, v252, v253\n"
      "v_lshlrev_b32 v252, 16, v158\n"
      "v_and_b32 v253, 0xffff0000, v158\n"
      "v_mul_f32 v252, v252, v247\n"
      "v_mul_f32 v253, v253, v247\n"
      "v_cvt_pk_bf16_f32 v158, v252, v253\n"
      "v_lshlrev_b32 v252, 16, v159\n"
      "v_and_b32 v253, 0xffff0000, v159\n"
      "v_mul_f32 v252, v252, v247\n"
      "v_mul_f32 v253, v253, v247\n"
      "v_cvt_pk_bf16_f32 v159, v252, v253\n"
      "v_mov_b32 v215, 0\n"
      "v_max3_f32 v216, v160, v161, v161\n"
      "v_max3_f32 v216, v162, v163, v216\n"
      "v_max3_f32 v216, v164, v165, v216\n"
      "v_max3_f32 v216, v166, v167, v216\n"
      "v_max3_f32 v216, v168, v169, v216\n"
      "v_max3_f32 v216, v170, v171, v216\n"
      "v_max3_f32 v216, v172, v173, v216\n"
      "v_max3_f32 v216, v174, v175, v216\n"
      "v_max3_f32 v216, v176, v177, v216\n"
      "v_max3_f32 v216, v178, v179, v216\n"
      "v_max3_f32 v216, v180, v181, v216\n"
      "v_max3_f32 v216, v182, v183, v216\n"
      "s_nop 1\n"
      "s_branch B2_10_%=\n"
      "R1_11_%=:\n"
      "v_max3_f32 v244, v128, v129, v130\n"
      "v_max3_f32 v244, v244, v131, v144\n"
      "v_max3_f32 v244, v244, v145, v146\n"
      "v_max_f32 v244, v244, v147\n"
      "v_max3_f32 v245, v132, v133, v134\n"
      "v_max3_f32 v245, v245, v135, v148\n"
      "v_max3_f32 v245, v245, v149, v150\n"
      "v_max_f32 v245, v245, v151\n"
      "v_max3_f32 v246, v136, v137, v138\n"
      "v_max3_f32 v246, v246, v139, v152\n"
      "v_max3_f32 v246, v246, v153, v154\n"
      "v_max_f32 v246, v246, v155\n"
      "v_max3_f32 v247, v140, v141, v142\n"
      "v_max3_f32 v247, v247, v143, v156\n"
      "v_max3_f32 v247, v247, v157, v158\n"
      "v_max_f32 v247, v247, v159\n"
      "v_mov_b32 v248, v244\n"
      "v_mov_b32 v249, v245\n"
      "v_mov_b32 v250, v246\n"
      "v_mov_b32 v251, v247\n"
      "s_nop 1\n"
      "v_permlane32_swap_b32 v244, v248\n"
      "v_permlane32_swap_b32 v245, v249\n"
      "v_permlane32_swap_b32 v246, v250\n"
      "v_permlane32_swap_b32 v247, v251\n"
      "s_nop 1\n"
      "v_max_f32 v244, v244, v248\n"
      "v_max_f32 v245, v245, v249\n"
      "v_max_f32 v246, v246, v250\n"
      "v_max_f32 v247, v247, v251\n"
      "v_mov_b32 v248, v244\n"
      "v_mov_b32 v249, v245\n"
      "v_mov_b32 v250, v246\n"
      "v_mov_b32 v251, v247\n"
      "s_nop 1\n"
      "v_permlane16_swap_b32 v244, v248\n"
      "v_permlane16_swap_b32 v245, v249\n"
      "v_permlane16_swap_b32 v246, v250\n"
      "v_permlane16_swap_b32 v247, v251\n"
      "s_nop 1\n"
      "v_max_f32 v244, v244, v248\n"
      "v_max_f32 v245, v245, v249\n"
      "v_max_f32 v246, v246, v250\n"
      "v_max_f32 v247, v247, v251\n"
      "v_add_f32 v244, 0x42cc0000, v244\n"
      "v_ceil_f32 v244, v244\n"
      "v_max_f32 v212, 0, v244\n"
      "v_exp_f32_e64 v248, -v212\n"
      "v_sub_f32 v128, v128, v212\n"
      "v_sub_f32 v129, v129, v212\n"
      "v_sub_f32 v130, v130, v212\n"
      "v_sub_f32 v131, v131, v212\n"
      "v_sub_f32 v144, v144, v212\n"
      "v_sub_f32 v145, v145, v212\n"
      "v_sub_f32 v146, v146, v212\n"
      "v_sub_f32 v147, v147, v212\n"
      "v_mul_f32 v208, v208, v248\n"
      "v_add_f32 v245, 0x42cc0000, v245\n"
      "v_ceil_f32 v245, v245\n"
      "v_max_f32 v213, 0, v245\n"
      "v_exp_f32_e64 v249, -v213\n"
      "v_sub_f32 v132, v132, v213\n"
      "v_sub_f32 v133, v133, v213\n"
      "v_sub_f32 v134, v134, v213\n"
      "v_sub_f32 v135, v135, v213\n"
      "v_sub_f32 v148, v148, v213\n"
      "v_sub_f32 v149, v149, v213\n"
      "v_sub_f32 v150, v150, v213\n"
      "v_sub_f32 v151, v151, v213\n"
      "v_mul_f32 v209, v209, v249\n"
      "v_add_f32 v246, 0x42cc0000, v246\n"
      "v_ceil_f32 v246, v246\n"
      "v_max_f32 v214, 0, v246\n"
      "v_exp_f32_e64 v250, -v214\n"
      "v_sub_f32 v136, v136, v214\n"
      "v_sub_f32 v137, v137, v214\n"
      "v_sub_f32 v138, v138, v214\n"
      "v_sub_f32 v139, v139, v214\n"
      "v_sub_f32 v152, v152, v214\n"
      "v_sub_f32 v153, v153, v214\n"
      "v_sub_f32 v154, v154, v214\n"
      "v_sub_f32 v155, v155, v214\n"
      "v_mul_f32 v210, v210, v250\n"
      "v_add_f32 v247, 0x42cc0000, v247\n"
      "v_ceil_f32 v247, v247\n"
      "v_max_f32 v215, 0, v247\n"
      "v_exp_f32_e64 v251, -v215\n"
      "v_sub_f32 v140, v140, v215\n"
      "v_sub_f32 v141, v141, v215\n"
      "v_sub_f32 v142, v142, v215\n"
      "v_sub_f32 v143, v143, v215\n"
      "v_sub_f32 v156, v156, v215\n"
      "v_sub_f32 v157, v157, v215\n"
      "v_sub_f32 v158, v158, v215\n"
      "v_sub_f32 v159, v159, v215\n"
      "v_mul_f32 v211, v211, v251\n"
      "v_add_u32 v243, s99, v242\n"
      "v_mov_b32 v252, s88\n"
      "s_lshl_b32 s84, s80, 3\n"
      "s_add_u32 s84, s84, 147520\n"
      "v_mov_b32 v253, s84\n"
      "ds_write_b128 v243, v[212:215] offset:0\n"
      "ds_write_b32 v241, v252 offset:0\n"
      "ds_write_b32 v253, v252 offset:0\n"
      "s_waitcnt lgkmcnt(0)\n"
      "s_branch B1_12_%=\n"
      "R2_13_%=:\n"
      "s_nop 7\n"
      "v_add_u32 v243, s97, v242\n"
      "s_xor_b32 s84, s80, 1\n"
      "s_lshl_b32 s84, s84, 3\n"
      "s_add_u32 s84, s84, 147520\n"
      "v_mov_b32 v253, s84\n"
      "ds_read_b128 v[244:247], v243 offset:0\n"
      "ds_read_b32 v252, v253 offset:0\n"
      "s_waitcnt lgkmcnt(0)\n"
      "v_cmp_eq_u32 vcc, s88, v252\n"
      "s_nop 1\n"
      "v_cndmask_b32 v244, 0, v244, vcc\n"
      "v_cndmask_b32 v245, 0, v245, vcc\n"
      "v_cndmask_b32 v246, 0, v246, vcc\n"
      "v_cndmask_b32 v247, 0, v247, vcc\n"
      "v_max_f32 v248, v212, v244\n"
      "v_max_f32 v249, v213, v245\n"
      "v_max_f32 v250, v214, v246\n"
      "v_max_f32 v251, v215, v247\n"
      "v_exp_f32_e64 v243, -v248\n"
      "v_sub_f32 v212, v212, v248\n"
      "v_sub_f32 v244, v244, v248\n"
      "v_exp_f32 v212, v212\n"
      "v_exp_f32 v244, v244\n"
      "v_mul_f32 v0, v0, v243\n"
      "v_mul_f32 v1, v1, v243\n"
      "v_mul_f32 v2, v2, v243\n"
      "v_mul_f32 v3, v3, v243\n"
      "v_mul_f32 v16, v16, v243\n"
      "v_mul_f32 v17, v17, v243\n"
      "v_mul_f32 v18, v18, v243\n"
      "v_mul_f32 v19, v19, v243\n"
      "v_mul_f32 v32, v32, v243\n"
      "v_mul_f32 v33, v33, v243\n"
      "v_mul_f32 v34, v34, v243\n"
      "v_mul_f32 v35, v35, v243\n"
      "v_mul_f32 v48, v48, v243\n"
      "v_mul_f32 v49, v49, v243\n"
      "v_mul_f32 v50, v50, v243\n"
      "v_mul_f32 v51, v51, v243\n"
      "v_mul_f32 v64, v64, v243\n"
      "v_mul_f32 v65, v65, v243\n"
      "v_mul_f32 v66, v66, v243\n"
      "v_mul_f32 v67, v67, v243\n"
      "v_mul_f32 v80, v80, v243\n"
      "v_mul_f32 v81, v81, v243\n"
      "v_mul_f32 v82, v82, v243\n"
      "v_mul_f32 v83, v83, v243\n"
      "v_mul_f32 v96, v96, v243\n"
      "v_mul_f32 v97, v97, v243\n"
      "v_mul_f32 v98, v98, v243\n"
      "v_mul_f32 v99, v99, v243\n"
      "v_mul_f32 v112, v112, v243\n"
      "v_mul_f32 v113, v113, v243\n"
      "v_mul_f32 v114, v114, v243\n"
      "v_mul_f32 v115, v115, v243\n"
      "v_sub_f32 v160, v160, v248\n"
      "v_sub_f32 v161, v161, v248\n"
      "v_sub_f32 v162, v162, v248\n"
      "v_sub_f32 v163, v163, v248\n"
      "v_sub_f32 v176, v176, v248\n"
      "v_sub_f32 v177, v177, v248\n"
      "v_sub_f32 v178, v178, v248\n"
      "v_sub_f32 v179, v179, v248\n"
      "v_sub_f32 v192, v192, v248\n"
      "v_sub_f32 v193, v193, v248\n"
      "v_sub_f32 v194, v194, v248\n"
      "v_sub_f32 v195, v195, v248\n"
      "v_mul_f32 v208, v208, v212\n"
      "v_lshlrev_b32 v252, 16, v128\n"
      "v_and_b32 v253, 0xffff0000, v128\n"
      "v_mul_f32 v252, v252, v212\n"
      "v_mul_f32 v253, v253, v212\n"
      "v_cvt_pk_bf16_f32 v128, v252, v253\n"
      "v_lshlrev_b32 v252, 16, v129\n"
      "v_and_b32 v253, 0xffff0000, v129\n"
      "v_mul_f32 v252, v252, v212\n"
      "v_mul_f32 v253, v253, v212\n"
      "v_cvt_pk_bf16_f32 v129, v252, v253\n"
      "v_lshlrev_b32 v252, 16, v130\n"
      "v_and_b32 v253, 0xffff0000, v130\n"
      "v_mul_f32 v252, v252, v212\n"
      "v_mul_f32 v253, v253, v212\n"
      "v_cvt_pk_bf16_f32 v130, v252, v253\n"
      "v_lshlrev_b32 v252, 16, v131\n"
      "v_and_b32 v253, 0xffff0000, v131\n"
      "v_mul_f32 v252, v252, v212\n"
      "v_mul_f32 v253, v253, v212\n"
      "v_cvt_pk_bf16_f32 v131, v252, v253\n"
      "v_lshlrev_b32 v252, 16, v144\n"
      "v_and_b32 v253, 0xffff0000, v144\n"
      "v_mul_f32 v252, v252, v244\n"
      "v_mul_f32 v253, v253, v244\n"
      "v_cvt_pk_bf16_f32 v144, v252, v253\n"
      "v_lshlrev_b32 v252, 16, v145\n"
      "v_and_b32 v253, 0xffff0000, v145\n"
      "v_mul_f32 v252, v252, v244\n"
      "v_mul_f32 v253, v253, v244\n"
      "v_cvt_pk_bf16_f32 v145, v252, v253\n"
      "v_lshlrev_b32 v252, 16, v146\n"
      "v_and_b32 v253, 0xffff0000, v146\n"
      "v_mul_f32 v252, v252, v244\n"
      "v_mul_f32 v253, v253, v244\n"
      "v_cvt_pk_bf16_f32 v146, v252, v253\n"
      "v_lshlrev_b32 v252, 16, v147\n"
      "v_and_b32 v253, 0xffff0000, v147\n"
      "v_mul_f32 v252, v252, v244\n"
      "v_mul_f32 v253, v253, v244\n"
      "v_cvt_pk_bf16_f32 v147, v252, v253\n"
      "v_mov_b32 v212, 0\n"
      "v_exp_f32_e64 v243, -v249\n"
      "v_sub_f32 v213, v213, v249\n"
      "v_sub_f32 v245, v245, v249\n"
      "v_exp_f32 v213, v213\n"
      "v_exp_f32 v245, v245\n"
      "v_mul_f32 v4, v4, v243\n"
      "v_mul_f32 v5, v5, v243\n"
      "v_mul_f32 v6, v6, v243\n"
      "v_mul_f32 v7, v7, v243\n"
      "v_mul_f32 v20, v20, v243\n"
      "v_mul_f32 v21, v21, v243\n"
      "v_mul_f32 v22, v22, v243\n"
      "v_mul_f32 v23, v23, v243\n"
      "v_mul_f32 v36, v36, v243\n"
      "v_mul_f32 v37, v37, v243\n"
      "v_mul_f32 v38, v38, v243\n"
      "v_mul_f32 v39, v39, v243\n"
      "v_mul_f32 v52, v52, v243\n"
      "v_mul_f32 v53, v53, v243\n"
      "v_mul_f32 v54, v54, v243\n"
      "v_mul_f32 v55, v55, v243\n"
      "v_mul_f32 v68, v68, v243\n"
      "v_mul_f32 v69, v69, v243\n"
      "v_mul_f32 v70, v70, v243\n"
      "v_mul_f32 v71, v71, v243\n"
      "v_mul_f32 v84, v84, v243\n"
      "v_mul_f32 v85, v85, v243\n"
      "v_mul_f32 v86, v86, v243\n"
      "v_mul_f32 v87, v87, v243\n"
      "v_mul_f32 v100, v100, v243\n"
      "v_mul_f32 v101, v101, v243\n"
      "v_mul_f32 v102, v102, v243\n"
      "v_mul_f32 v103, v103, v243\n"
      "v_mul_f32 v116, v116, v243\n"
      "v_mul_f32 v117, v117, v243\n"
      "v_mul_f32 v118, v118, v243\n"
      "v_mul_f32 v119, v119, v243\n"
      "v_sub_f32 v164, v164, v249\n"
      "v_sub_f32 v165, v165, v249\n"
      "v_sub_f32 v166, v166, v249\n"
      "v_sub_f32 v167, v167, v249\n"
      "v_sub_f32 v180, v180, v249\n"
      "v_sub_f32 v181, v181, v249\n"
      "v_sub_f32 v182, v182, v249\n"
      "v_sub_f32 v183, v183, v249\n"
      "v_sub_f32 v196, v196, v249\n"
      "v_sub_f32 v197, v197, v249\n"
      "v_sub_f32 v198, v198, v249\n"
      "v_sub_f32 v199, v199, v249\n"
      "v_mul_f32 v209, v209, v213\n"
      "v_lshlrev_b32 v252, 16, v132\n"
      "v_and_b32 v253, 0xffff0000, v132\n"
      "v_mul_f32 v252, v252, v213\n"
      "v_mul_f32 v253, v253, v213\n"
      "v_cvt_pk_bf16_f32 v132, v252, v253\n"
      "v_lshlrev_b32 v252, 16, v133\n"
      "v_and_b32 v253, 0xffff0000, v133\n"
      "v_mul_f32 v252, v252, v213\n"
      "v_mul_f32 v253, v253, v213\n"
      "v_cvt_pk_bf16_f32 v133, v252, v253\n"
      "v_lshlrev_b32 v252, 16, v134\n"
      "v_and_b32 v253, 0xffff0000, v134\n"
      "v_mul_f32 v252, v252, v213\n"
      "v_mul_f32 v253, v253, v213\n"
      "v_cvt_pk_bf16_f32 v134, v252, v253\n"
      "v_lshlrev_b32 v252, 16, v135\n"
      "v_and_b32 v253, 0xffff0000, v135\n"
      "v_mul_f32 v252, v252, v213\n"
      "v_mul_f32 v253, v253, v213\n"
      "v_cvt_pk_bf16_f32 v135, v252, v253\n"
      "v_lshlrev_b32 v252, 16, v148\n"
      "v_and_b32 v253, 0xffff0000, v148\n"
      "v_mul_f32 v252, v252, v245\n"
      "v_mul_f32 v253, v253, v245\n"
      "v_cvt_pk_bf16_f32 v148, v252, v253\n"
      "v_lshlrev_b32 v252, 16, v149\n"
      "v_and_b32 v253, 0xffff0000, v149\n"
      "v_mul_f32 v252, v252, v245\n"
      "v_mul_f32 v253, v253, v245\n"
      "v_cvt_pk_bf16_f32 v149, v252, v253\n"
      "v_lshlrev_b32 v252, 16, v150\n"
      "v_and_b32 v253, 0xffff0000, v150\n"
      "v_mul_f32 v252, v252, v245\n"
      "v_mul_f32 v253, v253, v245\n"
      "v_cvt_pk_bf16_f32 v150, v252, v253\n"
      "v_lshlrev_b32 v252, 16, v151\n"
      "v_and_b32 v253, 0xffff0000, v151\n"
      "v_mul_f32 v252, v252, v245\n"
      "v_mul_f32 v253, v253, v245\n"
      "v_cvt_pk_bf16_f32 v151, v252, v253\n"
      "v_mov_b32 v213, 0\n"
      "v_exp_f32_e64 v243, -v250\n"
      "v_sub_f32 v214, v214, v250\n"
      "v_sub_f32 v246, v246, v250\n"
      "v_exp_f32 v214, v214\n"
      "v_exp_f32 v246, v246\n"
      "v_mul_f32 v8, v8, v243\n"
      "v_mul_f32 v9, v9, v243\n"
      "v_mul_f32 v10, v10, v243\n"
      "v_mul_f32 v11, v11, v243\n"
      "v_mul_f32 v24, v24, v243\n"
      "v_mul_f32 v25, v25, v243\n"
      "v_mul_f32 v26, v26, v243\n"
      "v_mul_f32 v27, v27, v243\n"
      "v_mul_f32 v40, v40, v243\n"
      "v_mul_f32 v41, v41, v243\n"
      "v_mul_f32 v42, v42, v243\n"
      "v_mul_f32 v43, v43, v243\n"
      "v_mul_f32 v56, v56, v243\n"
      "v_mul_f32 v57, v57, v243\n"
      "v_mul_f32 v58, v58, v243\n"
      "v_mul_f32 v59, v59, v243\n"
      "v_mul_f32 v72, v72, v243\n"
      "v_mul_f32 v73, v73, v243\n"
      "v_mul_f32 v74, v74, v243\n"
      "v_mul_f32 v75, v75, v243\n"
      "v_mul_f32 v88, v88, v243\n"
      "v_mul_f32 v89, v89, v243\n"
      "v_mul_f32 v90, v90, v243\n"
      "v_mul_f32 v91, v91, v243\n"
      "v_mul_f32 v104, v104, v243\n"
      "v_mul_f32 v105, v105, v243\n"
      "v_mul_f32 v106, v106, v243\n"
      "v_mul_f32 v107, v107, v243\n"
      "v_mul_f32 v120, v120, v243\n"
      "v_mul_f32 v121, v121, v243\n"
      "v_mul_f32 v122, v122, v243\n"
      "v_mul_f32 v123, v123, v243\n"
      "v_sub_f32 v168, v168, v250\n"
      "v_sub_f32 v169, v169, v250\n"
      "v_sub_f32 v170, v170, v250\n"
      "v_sub_f32 v171, v171, v250\n"
      "v_sub_f32 v184, v184, v250\n"
      "v_sub_f32 v185, v185, v250\n"
      "v_sub_f32 v186, v186, v250\n"
      "v_sub_f32 v187, v187, v250\n"
      "v_sub_f32 v200, v200, v250\n"
      "v_sub_f32 v201, v201, v250\n"
      "v_sub_f32 v202, v202, v250\n"
      "v_sub_f32 v203, v203, v250\n"
      "v_mul_f32 v210, v210, v214\n"
      "v_lshlrev_b32 v252, 16, v136\n"
      "v_and_b32 v253, 0xffff0000, v136\n"
      "v_mul_f32 v252, v252, v214\n"
      "v_mul_f32 v253, v253, v214\n"
      "v_cvt_pk_bf16_f32 v136, v252, v253\n"
      "v_lshlrev_b32 v252, 16, v137\n"
      "v_and_b32 v253, 0xffff0000, v137\n"
      "v_mul_f32 v252, v252, v214\n"
      "v_mul_f32 v253, v253, v214\n"
      "v_cvt_pk_bf16_f32 v137, v252, v253\n"
      "v_lshlrev_b32 v252, 16, v138\n"
      "v_and_b32 v253, 0xffff0000, v138\n"
      "v_mul_f32 v252, v252, v214\n"
      "v_mul_f32 v253, v253, v214\n"
      "v_cvt_pk_bf16_f32 v138, v252, v253\n"
      "v_lshlrev_b32 v252, 16, v139\n"
      "v_and_b32 v253, 0xffff0000, v139\n"
      "v_mul_f32 v252, v252, v214\n"
      "v_mul_f32 v253, v253, v214\n"
      "v_cvt_pk_bf16_f32 v139, v252, v253\n"
      "v_lshlrev_b32 v252, 16, v152\n"
      "v_and_b32 v253, 0xffff0000, v152\n"
      "v_mul_f32 v252, v252, v246\n"
      "v_mul_f32 v253, v253, v246\n"
      "v_cvt_pk_bf16_f32 v152, v252, v253\n"
      "v_lshlrev_b32 v252, 16, v153\n"
      "v_and_b32 v253, 0xffff0000, v153\n"
      "v_mul_f32 v252, v252, v246\n"
      "v_mul_f32 v253, v253, v246\n"
      "v_cvt_pk_bf16_f32 v153, v252, v253\n"
      "v_lshlrev_b32 v252, 16, v154\n"
      "v_and_b32 v253, 0xffff0000, v154\n"
      "v_mul_f32 v252, v252, v246\n"
      "v_mul_f32 v253, v253, v246\n"
      "v_cvt_pk_bf16_f32 v154, v252, v253\n"
      "v_lshlrev_b32 v252, 16, v155\n"
      "v_and_b32 v253, 0xffff0000, v155\n"
      "v_mul_f32 v252, v252, v246\n"
      "v_mul_f32 v253, v253, v246\n"
      "v_cvt_pk_bf16_f32 v155, v252, v253\n"
      "v_mov_b32 v214, 0\n"
      "v_exp_f32_e64 v243, -v251\n"
      "v_sub_f32 v215, v215, v251\n"
      "v_sub_f32 v247, v247, v251\n"
      "v_exp_f32 v215, v215\n"
      "v_exp_f32 v247, v247\n"
      "v_mul_f32 v12, v12, v243\n"
      "v_mul_f32 v13, v13, v243\n"
      "v_mul_f32 v14, v14, v243\n"
      "v_mul_f32 v15, v15, v243\n"
      "v_mul_f32 v28, v28, v243\n"
      "v_mul_f32 v29, v29, v243\n"
      "v_mul_f32 v30, v30, v243\n"
      "v_mul_f32 v31, v31, v243\n"
      "v_mul_f32 v44, v44, v243\n"
      "v_mul_f32 v45, v45, v243\n"
      "v_mul_f32 v46, v46, v243\n"
      "v_mul_f32 v47, v47, v243\n"
      "v_mul_f32 v60, v60, v243\n"
      "v_mul_f32 v61, v61, v243\n"
      "v_mul_f32 v62, v62, v243\n"
      "v_mul_f32 v63, v63, v243\n"
      "v_mul_f32 v76, v76, v243\n"
      "v_mul_f32 v77, v77, v243\n"
      "v_mul_f32 v78, v78, v243\n"
      "v_mul_f32 v79, v79, v243\n"
      "v_mul_f32 v92, v92, v243\n"
      "v_mul_f32 v93, v93, v243\n"
      "v_mul_f32 v94, v94, v243\n"
      "v_mul_f32 v95, v95, v243\n"
      "v_mul_f32 v108, v108, v243\n"
      "v_mul_f32 v109, v109, v243\n"
      "v_mul_f32 v110, v110, v243\n"
      "v_mul_f32 v111, v111, v243\n"
      "v_mul_f32 v124, v124, v243\n"
      "v_mul_f32 v125, v125, v243\n"
      "v_mul_f32 v126, v126, v243\n"
      "v_mul_f32 v127, v127, v243\n"
      "v_sub_f32 v172, v172, v251\n"
      "v_sub_f32 v173, v173, v251\n"
      "v_sub_f32 v174, v174, v251\n"
      "v_sub_f32 v175, v175, v251\n"
      "v_sub_f32 v188, v188, v251\n"
      "v_sub_f32 v189, v189, v251\n"
      "v_sub_f32 v190, v190, v251\n"
      "v_sub_f32 v191, v191, v251\n"
      "v_sub_f32 v204, v204, v251\n"
      "v_sub_f32 v205, v205, v251\n"
      "v_sub_f32 v206, v206, v251\n"
      "v_sub_f32 v207, v207, v251\n"
      "v_mul_f32 v211, v211, v215\n"
      "v_lshlrev_b32 v252, 16, v140\n"
      "v_and_b32 v253, 0xffff0000, v140\n"
      "v_mul_f32 v252, v252, v215\n"
      "v_mul_f32 v253, v253, v215\n"
      "v_cvt_pk_bf16_f32 v140, v252, v253\n"
      "v_lshlrev_b32 v252, 16, v141\n"
      "v_and_b32 v253, 0xffff0000, v141\n"
      "v_mul_f32 v252, v252, v215\n"
      "v_mul_f32 v253, v253, v215\n"
      "v_cvt_pk_bf16_f32 v141, v252, v253\n"
      "v_lshlrev_b32 v252, 16, v142\n"
      "v_and_b32 v253, 0xffff0000, v142\n"
      "v_mul_f32 v252, v252, v215\n"
      "v_mul_f32 v253, v253, v215\n"
      "v_cvt_pk_bf16_f32 v142, v252, v253\n"
      "v_lshlrev_b32 v252, 16, v143\n"
      "v_and_b32 v253, 0xffff0000, v143\n"
      "v_mul_f32 v252, v252, v215\n"
      "v_mul_f32 v253, v253, v215\n"
      "v_cvt_pk_bf16_f32 v143, v252, v253\n"
      "v_lshlrev_b32 v252, 16, v156\n"
      "v_and_b32 v253, 0xffff0000, v156\n"
      "v_mul_f32 v252, v252, v247\n"
      "v_mul_f32 v253, v253, v247\n"
      "v_cvt_pk_bf16_f32 v156, v252, v253\n"
      "v_lshlrev_b32 v252, 16, v157\n"
      "v_and_b32 v253, 0xffff0000, v157\n"
      "v_mul_f32 v252, v252, v247\n"
      "v_mul_f32 v253, v253, v247\n"
      "v_cvt_pk_bf16_f32 v157, v252, v253\n"
      "v_lshlrev_b32 v252, 16, v158\n"
      "v_and_b32 v253, 0xffff0000, v158\n"
      "v_mul_f32 v252, v252, v247\n"
      "v_mul_f32 v253, v253, v247\n"
      "v_cvt_pk_bf16_f32 v158, v252, v253\n"
      "v_lshlrev_b32 v252, 16, v159\n"
      "v_and_b32 v253, 0xffff0000, v159\n"
      "v_mul_f32 v252, v252, v247\n"
      "v_mul_f32 v253, v253, v247\n"
      "v_cvt_pk_bf16_f32 v159, v252, v253\n"
      "v_mov_b32 v215, 0\n"
      "v_max3_f32 v216, v160, v161, v161\n"
      "v_max3_f32 v216, v162, v163, v216\n"
      "v_max3_f32 v216, v164, v165, v216\n"
      "v_max3_f32 v216, v166, v167, v216\n"
      "v_max3_f32 v216, v168, v169, v216\n"
      "v_max3_f32 v216, v170, v171, v216\n"
      "v_max3_f32 v216, v172, v173, v216\n"
      "v_max3_f32 v216, v174, v175, v216\n"
      "v_max3_f32 v216, v176, v177, v216\n"
      "v_max3_f32 v216, v178, v179, v216\n"
      "v_max3_f32 v216, v180, v181, v216\n"
      "v_max3_f32 v216, v182, v183, v216\n"
      "s_nop 1\n"
      "s_branch B2_14_%=\n"
      "R1_15_%=:\n"
      "v_max3_f32 v244, v160, v161, v162\n"
      "v_max3_f32 v244, v244, v163, v176\n"
      "v_max3_f32 v244, v244, v177, v178\n"
      "v_max_f32 v244, v244, v179\n"
      "v_max3_f32 v245, v164, v165, v166\n"
      "v_max3_f32 v245, v245, v167, v180\n"
      "v_max3_f32 v245, v245, v181, v182\n"
      "v_max_f32 v245, v245, v183\n"
      "v_max3_f32 v246, v168, v169, v170\n"
      "v_max3_f32 v246, v246, v171, v184\n"
      "v_max3_f32 v246, v246, v185, v186\n"
      "v_max_f32 v246, v246, v187\n"
      "v_max3_f32 v247, v172, v173, v174\n"
      "v_max3_f32 v247, v247, v175, v188\n"
      "v_max3_f32 v247, v247, v189, v190\n"
      "v_max_f32 v247, v247, v191\n"
      "v_mov_b32 v248, v244\n"
      "v_mov_b32 v249, v245\n"
      "v_mov_b32 v250, v246\n"
      "v_mov_b32 v251, v247\n"
      "s_nop 1\n"
      "v_permlane32_swap_b32 v244, v248\n"
      "v_permlane32_swap_b32 v245, v249\n"
      "v_permlane32_swap_b32 v246, v250\n"
      "v_permlane32_swap_b32 v247, v251\n"
      "s_nop 1\n"
      "v_max_f32 v244, v244, v248\n"
      "v_max_f32 v245, v245, v249\n"
      "v_max_f32 v246, v246, v250\n"
      "v_max_f32 v247, v247, v251\n"
      "v_mov_b32 v248, v244\n"
      "v_mov_b32 v249, v245\n"
      "v_mov_b32 v250, v246\n"
      "v_mov_b32 v251, v247\n"
      "s_nop 1\n"
      "v_permlane16_swap_b32 v244, v248\n"
      "v_permlane16_swap_b32 v245, v249\n"
      "v_permlane16_swap_b32 v246, v250\n"
      "v_permlane16_swap_b32 v247, v251\n"
      "s_nop 1\n"
      "v_max_f32 v244, v244, v248\n"
      "v_max_f32 v245, v245, v249\n"
      "v_max_f32 v246, v246, v250\n"
      "v_max_f32 v247, v247, v251\n"
      "v_add_f32 v244, 0x42cc0000, v244\n"
      "v_ceil_f32 v244, v244\n"
      "v_max_f32 v212, 0, v244\n"
      "v_exp_f32_e64 v248, -v212\n"
      "v_sub_f32 v160, v160, v212\n"
      "v_sub_f32 v161, v161, v212\n"
      "v_sub_f32 v162, v162, v212\n"
      "v_sub_f32 v163, v163, v212\n"
      "v_sub_f32 v176, v176, v212\n"
      "v_sub_f32 v177, v177, v212\n"
      "v_sub_f32 v178, v178, v212\n"
      "v_sub_f32 v179, v179, v212\n"
      "v_mul_f32 v208, v208, v248\n"
      "v_add_f32 v245, 0x42cc0000, v245\n"
      "v_ceil_f32 v245, v245\n"
      "v_max_f32 v213, 0, v245\n"
      "v_exp_f32_e64 v249, -v213\n"
      "v_sub_f32 v164, v164, v213\n"
      "v_sub_f32 v165, v165, v213\n"
      "v_sub_f32 v166, v166, v213\n"
      "v_sub_f32 v167, v167, v213\n"
      "v_sub_f32 v180, v180, v213\n"
      "v_sub_f32 v181, v181, v213\n"
      "v_sub_f32 v182, v182, v213\n"
      "v_sub_f32 v183, v183, v213\n"
      "v_mul_f32 v209, v209, v249\n"
      "v_add_f32 v246, 0x42cc0000, v246\n"
      "v_ceil_f32 v246, v246\n"
      "v_max_f32 v214, 0, v246\n"
      "v_exp_f32_e64 v250, -v214\n"
      "v_sub_f32 v168, v168, v214\n"
      "v_sub_f32 v169, v169, v214\n"
      "v_sub_f32 v170, v170, v214\n"
      "v_sub_f32 v171, v171, v214\n"
      "v_sub_f32 v184, v184, v214\n"
      "v_sub_f32 v185, v185, v214\n"
      "v_sub_f32 v186, v186, v214\n"
      "v_sub_f32 v187, v187, v214\n"
      "v_mul_f32 v210, v210, v250\n"
      "v_add_f32 v247, 0x42cc0000, v247\n"
      "v_ceil_f32 v247, v247\n"
      "v_max_f32 v215, 0, v247\n"
      "v_exp_f32_e64 v251, -v215\n"
      "v_sub_f32 v172, v172, v215\n"
      "v_sub_f32 v173, v173, v215\n"
      "v_sub_f32 v174, v174, v215\n"
      "v_sub_f32 v175, v175, v215\n"
      "v_sub_f32 v188, v188, v215\n"
      "v_sub_f32 v189, v189, v215\n"
      "v_sub_f32 v190, v190, v215\n"
      "v_sub_f32 v191, v191, v215\n"
      "v_mul_f32 v211, v211, v251\n"
      "v_add_u32 v243, s99, v242\n"
      "v_mov_b32 v252, s88\n"
      "s_lshl_b32 s84, s80, 3\n"
      "s_add_u32 s84, s84, 147520\n"
      "v_mov_b32 v253, s84\n"
      "ds_write_b128 v243, v[212:215] offset:1024\n"
      "ds_write_b32 v241, v252 offset:4\n"
      "ds_write_b32 v253, v252 offset:4\n"
      "s_waitcnt lgkmcnt(0)\n"
      "s_branch B1_16_%=\n"
      "R2_17_%=:\n"
      "s_nop 7\n"
      "v_add_u32 v243, s97, v242\n"
      "s_xor_b32 s84, s80, 1\n"
      "s_lshl_b32 s84, s84, 3\n"
      "s_add_u32 s84, s84, 147520\n"
      "v_mov_b32 v253, s84\n"
      "ds_read_b128 v[244:247], v243 offset:1024\n"
      "ds_read_b32 v252, v253 offset:4\n"
      "s_waitcnt lgkmcnt(0)\n"
      "v_cmp_eq_u32 vcc, s88, v252\n"
      "s_nop 1\n"
      "v_cndmask_b32 v244, 0, v244, vcc\n"
      "v_cndmask_b32 v245, 0, v245, vcc\n"
      "v_cndmask_b32 v246, 0, v246, vcc\n"
      "v_cndmask_b32 v247, 0, v247, vcc\n"
      "v_max_f32 v248, v212, v244\n"
      "v_max_f32 v249, v213, v245\n"
      "v_max_f32 v250, v214, v246\n"
      "v_max_f32 v251, v215, v247\n"
      "v_exp_f32_e64 v243, -v248\n"
      "v_sub_f32 v212, v212, v248\n"
      "v_sub_f32 v244, v244, v248\n"
      "v_exp_f32 v212, v212\n"
      "v_exp_f32 v244, v244\n"
      "v_mul_f32 v0, v0, v243\n"
      "v_mul_f32 v1, v1, v243\n"
      "v_mul_f32 v2, v2, v243\n"
      "v_mul_f32 v3, v3, v243\n"
      "v_mul_f32 v16, v16, v243\n"
      "v_mul_f32 v17, v17, v243\n"
      "v_mul_f32 v18, v18, v243\n"
      "v_mul_f32 v19, v19, v243\n"
      "v_mul_f32 v32, v32, v243\n"
      "v_mul_f32 v33, v33, v243\n"
      "v_mul_f32 v34, v34, v243\n"
      "v_mul_f32 v35, v35, v243\n"
      "v_mul_f32 v48, v48, v243\n"
      "v_mul_f32 v49, v49, v243\n"
      "v_mul_f32 v50, v50, v243\n"
      "v_mul_f32 v51, v51, v243\n"
      "v_mul_f32 v64, v64, v243\n"
      "v_mul_f32 v65, v65, v243\n"
      "v_mul_f32 v66, v66, v243\n"
      "v_mul_f32 v67, v67, v243\n"
      "v_mul_f32 v80, v80, v243\n"
      "v_mul_f32 v81, v81, v243\n"
      "v_mul_f32 v82, v82, v243\n"
      "v_mul_f32 v83, v83, v243\n"
      "v_mul_f32 v96, v96, v243\n"
      "v_mul_f32 v97, v97, v243\n"
      "v_mul_f32 v98, v98, v243\n"
      "v_mul_f32 v99, v99, v243\n"
      "v_mul_f32 v112, v112, v243\n"
      "v_mul_f32 v113, v113, v243\n"
      "v_mul_f32 v114, v114, v243\n"
      "v_mul_f32 v115, v115, v243\n"
      "v_mul_f32 v208, v208, v212\n"
      "v_lshlrev_b32 v252, 16, v160\n"
      "v_and_b32 v253, 0xffff0000, v160\n"
      "v_mul_f32 v252, v252, v212\n"
      "v_mul_f32 v253, v253, v212\n"
      "v_cvt_pk_bf16_f32 v160, v252, v253\n"
      "v_lshlrev_b32 v252, 16, v161\n"
      "v_and_b32 v253, 0xffff0000, v161\n"
      "v_mul_f32 v252, v252, v212\n"
      "v_mul_f32 v253, v253, v212\n"
      "v_cvt_pk_bf16_f32 v161, v252, v253\n"
      "v_lshlrev_b32 v252, 16, v162\n"
      "v_and_b32 v253, 0xffff0000, v162\n"
      "v_mul_f32 v252, v252, v212\n"
      "v_mul_f32 v253, v253, v212\n"
      "v_cvt_pk_bf16_f32 v162, v252, v253\n"
      "v_lshlrev_b32 v252, 16, v163\n"
      "v_and_b32 v253, 0xffff0000, v163\n"
      "v_mul_f32 v252, v252, v212\n"
      "v_mul_f32 v253, v253, v212\n"
      "v_cvt_pk_bf16_f32 v163, v252, v253\n"
      "v_lshlrev_b32 v252, 16, v176\n"
      "v_and_b32 v253, 0xffff0000, v176\n"
      "v_mul_f32 v252, v252, v244\n"
      "v_mul_f32 v253, v253, v244\n"
      "v_cvt_pk_bf16_f32 v176, v252, v253\n"
      "v_lshlrev_b32 v252, 16, v177\n"
      "v_and_b32 v253, 0xffff0000, v177\n"
      "v_mul_f32 v252, v252, v244\n"
      "v_mul_f32 v253, v253, v244\n"
      "v_cvt_pk_bf16_f32 v177, v252, v253\n"
      "v_lshlrev_b32 v252, 16, v178\n"
      "v_and_b32 v253, 0xffff0000, v178\n"
      "v_mul_f32 v252, v252, v244\n"
      "v_mul_f32 v253, v253, v244\n"
      "v_cvt_pk_bf16_f32 v178, v252, v253\n"
      "v_lshlrev_b32 v252, 16, v179\n"
      "v_and_b32 v253, 0xffff0000, v179\n"
      "v_mul_f32 v252, v252, v244\n"
      "v_mul_f32 v253, v253, v244\n"
      "v_cvt_pk_bf16_f32 v179, v252, v253\n"
      "v_mov_b32 v212, 0\n"
      "v_exp_f32_e64 v243, -v249\n"
      "v_sub_f32 v213, v213, v249\n"
      "v_sub_f32 v245, v245, v249\n"
      "v_exp_f32 v213, v213\n"
      "v_exp_f32 v245, v245\n"
      "v_mul_f32 v4, v4, v243\n"
      "v_mul_f32 v5, v5, v243\n"
      "v_mul_f32 v6, v6, v243\n"
      "v_mul_f32 v7, v7, v243\n"
      "v_mul_f32 v20, v20, v243\n"
      "v_mul_f32 v21, v21, v243\n"
      "v_mul_f32 v22, v22, v243\n"
      "v_mul_f32 v23, v23, v243\n"
      "v_mul_f32 v36, v36, v243\n"
      "v_mul_f32 v37, v37, v243\n"
      "v_mul_f32 v38, v38, v243\n"
      "v_mul_f32 v39, v39, v243\n"
      "v_mul_f32 v52, v52, v243\n"
      "v_mul_f32 v53, v53, v243\n"
      "v_mul_f32 v54, v54, v243\n"
      "v_mul_f32 v55, v55, v243\n"
      "v_mul_f32 v68, v68, v243\n"
      "v_mul_f32 v69, v69, v243\n"
      "v_mul_f32 v70, v70, v243\n"
      "v_mul_f32 v71, v71, v243\n"
      "v_mul_f32 v84, v84, v243\n"
      "v_mul_f32 v85, v85, v243\n"
      "v_mul_f32 v86, v86, v243\n"
      "v_mul_f32 v87, v87, v243\n"
      "v_mul_f32 v100, v100, v243\n"
      "v_mul_f32 v101, v101, v243\n"
      "v_mul_f32 v102, v102, v243\n"
      "v_mul_f32 v103, v103, v243\n"
      "v_mul_f32 v116, v116, v243\n"
      "v_mul_f32 v117, v117, v243\n"
      "v_mul_f32 v118, v118, v243\n"
      "v_mul_f32 v119, v119, v243\n"
      "v_mul_f32 v209, v209, v213\n"
      "v_lshlrev_b32 v252, 16, v164\n"
      "v_and_b32 v253, 0xffff0000, v164\n"
      "v_mul_f32 v252, v252, v213\n"
      "v_mul_f32 v253, v253, v213\n"
      "v_cvt_pk_bf16_f32 v164, v252, v253\n"
      "v_lshlrev_b32 v252, 16, v165\n"
      "v_and_b32 v253, 0xffff0000, v165\n"
      "v_mul_f32 v252, v252, v213\n"
      "v_mul_f32 v253, v253, v213\n"
      "v_cvt_pk_bf16_f32 v165, v252, v253\n"
      "v_lshlrev_b32 v252, 16, v166\n"
      "v_and_b32 v253, 0xffff0000, v166\n"
      "v_mul_f32 v252, v252, v213\n"
      "v_mul_f32 v253, v253, v213\n"
      "v_cvt_pk_bf16_f32 v166, v252, v253\n"
      "v_lshlrev_b32 v252, 16, v167\n"
      "v_and_b32 v253, 0xffff0000, v167\n"
      "v_mul_f32 v252, v252, v213\n"
      "v_mul_f32 v253, v253, v213\n"
      "v_cvt_pk_bf16_f32 v167, v252, v253\n"
      "v_lshlrev_b32 v252, 16, v180\n"
      "v_and_b32 v253, 0xffff0000, v180\n"
      "v_mul_f32 v252, v252, v245\n"
      "v_mul_f32 v253, v253, v245\n"
      "v_cvt_pk_bf16_f32 v180, v252, v253\n"
      "v_lshlrev_b32 v252, 16, v181\n"
      "v_and_b32 v253, 0xffff0000, v181\n"
      "v_mul_f32 v252, v252, v245\n"
      "v_mul_f32 v253, v253, v245\n"
      "v_cvt_pk_bf16_f32 v181, v252, v253\n"
      "v_lshlrev_b32 v252, 16, v182\n"
      "v_and_b32 v253, 0xffff0000, v182\n"
      "v_mul_f32 v252, v252, v245\n"
      "v_mul_f32 v253, v253, v245\n"
      "v_cvt_pk_bf16_f32 v182, v252, v253\n"
      "v_lshlrev_b32 v252, 16, v183\n"
      "v_and_b32 v253, 0xffff0000, v183\n"
      "v_mul_f32 v252, v252, v245\n"
      "v_mul_f32 v253, v253, v245\n"
      "v_cvt_pk_bf16_f32 v183, v252, v253\n"
      "v_mov_b32 v213, 0\n"
      "v_exp_f32_e64 v243, -v250\n"
      "v_sub_f32 v214, v214, v250\n"
      "v_sub_f32 v246, v246, v250\n"
      "v_exp_f32 v214, v214\n"
      "v_exp_f32 v246, v246\n"
      "v_mul_f32 v8, v8, v243\n"
      "v_mul_f32 v9, v9, v243\n"
      "v_mul_f32 v10, v10, v243\n"
      "v_mul_f32 v11, v11, v243\n"
      "v_mul_f32 v24, v24, v243\n"
      "v_mul_f32 v25, v25, v243\n"
      "v_mul_f32 v26, v26, v243\n"
      "v_mul_f32 v27, v27, v243\n"
      "v_mul_f32 v40, v40, v243\n"
      "v_mul_f32 v41, v41, v243\n"
      "v_mul_f32 v42, v42, v243\n"
      "v_mul_f32 v43, v43, v243\n"
      "v_mul_f32 v56, v56, v243\n"
      "v_mul_f32 v57, v57, v243\n"
      "v_mul_f32 v58, v58, v243\n"
      "v_mul_f32 v59, v59, v243\n"
      "v_mul_f32 v72, v72, v243\n"
      "v_mul_f32 v73, v73, v243\n"
      "v_mul_f32 v74, v74, v243\n"
      "v_mul_f32 v75, v75, v243\n"
      "v_mul_f32 v88, v88, v243\n"
      "v_mul_f32 v89, v89, v243\n"
      "v_mul_f32 v90, v90, v243\n"
      "v_mul_f32 v91, v91, v243\n"
      "v_mul_f32 v104, v104, v243\n"
      "v_mul_f32 v105, v105, v243\n"
      "v_mul_f32 v106, v106, v243\n"
      "v_mul_f32 v107, v107, v243\n"
      "v_mul_f32 v120, v120, v243\n"
      "v_mul_f32 v121, v121, v243\n"
      "v_mul_f32 v122, v122, v243\n"
      "v_mul_f32 v123, v123, v243\n"
      "v_mul_f32 v210, v210, v214\n"
      "v_lshlrev_b32 v252, 16, v168\n"
      "v_and_b32 v253, 0xffff0000, v168\n"
      "v_mul_f32 v252, v252, v214\n"
      "v_mul_f32 v253, v253, v214\n"
      "v_cvt_pk_bf16_f32 v168, v252, v253\n"
      "v_lshlrev_b32 v252, 16, v169\n"
      "v_and_b32 v253, 0xffff0000, v169\n"
      "v_mul_f32 v252, v252, v214\n"
      "v_mul_f32 v253, v253, v214\n"
      "v_cvt_pk_bf16_f32 v169, v252, v253\n"
      "v_lshlrev_b32 v252, 16, v170\n"
      "v_and_b32 v253, 0xffff0000, v170\n"
      "v_mul_f32 v252, v252, v214\n"
      "v_mul_f32 v253, v253, v214\n"
      "v_cvt_pk_bf16_f32 v170, v252, v253\n"
      "v_lshlrev_b32 v252, 16, v171\n"
      "v_and_b32 v253, 0xffff0000, v171\n"
      "v_mul_f32 v252, v252, v214\n"
      "v_mul_f32 v253, v253, v214\n"
      "v_cvt_pk_bf16_f32 v171, v252, v253\n"
      "v_lshlrev_b32 v252, 16, v184\n"
      "v_and_b32 v253, 0xffff0000, v184\n"
      "v_mul_f32 v252, v252, v246\n"
      "v_mul_f32 v253, v253, v246\n"
      "v_cvt_pk_bf16_f32 v184, v252, v253\n"
      "v_lshlrev_b32 v252, 16, v185\n"
      "v_and_b32 v253, 0xffff0000, v185\n"
      "v_mul_f32 v252, v252, v246\n"
      "v_mul_f32 v253, v253, v246\n"
      "v_cvt_pk_bf16_f32 v185, v252, v253\n"
      "v_lshlrev_b32 v252, 16, v186\n"
      "v_and_b32 v253, 0xffff0000, v186\n"
      "v_mul_f32 v252, v252, v246\n"
      "v_mul_f32 v253, v253, v246\n"
      "v_cvt_pk_bf16_f32 v186, v252, v253\n"
      "v_lshlrev_b32 v252, 16, v187\n"
      "v_and_b32 v253, 0xffff0000, v187\n"
      "v_mul_f32 v252, v252, v246\n"
      "v_mul_f32 v253, v253, v246\n"
      "v_cvt_pk_bf16_f32 v187, v252, v253\n"
      "v_mov_b32 v214, 0\n"
      "v_exp_f32_e64 v243, -v251\n"
      "v_sub_f32 v215, v215, v251\n"
      "v_sub_f32 v247, v247, v251\n"
      "v_exp_f32 v215, v215\n"
      "v_exp_f32 v247, v247\n"
      "v_mul_f32 v12, v12, v243\n"
      "v_mul_f32 v13, v13, v243\n"
      "v_mul_f32 v14, v14, v243\n"
      "v_mul_f32 v15, v15, v243\n"
      "v_mul_f32 v28, v28, v243\n"
      "v_mul_f32 v29, v29, v243\n"
      "v_mul_f32 v30, v30, v243\n"
      "v_mul_f32 v31, v31, v243\n"
      "v_mul_f32 v44, v44, v243\n"
      "v_mul_f32 v45, v45, v243\n"
      "v_mul_f32 v46, v46, v243\n"
      "v_mul_f32 v47, v47, v243\n"
      "v_mul_f32 v60, v60, v243\n"
      "v_mul_f32 v61, v61, v243\n"
      "v_mul_f32 v62, v62, v243\n"
      "v_mul_f32 v63, v63, v243\n"
      "v_mul_f32 v76, v76, v243\n"
      "v_mul_f32 v77, v77, v243\n"
      "v_mul_f32 v78, v78, v243\n"
      "v_mul_f32 v79, v79, v243\n"
      "v_mul_f32 v92, v92, v243\n"
      "v_mul_f32 v93, v93, v243\n"
      "v_mul_f32 v94, v94, v243\n"
      "v_mul_f32 v95, v95, v243\n"
      "v_mul_f32 v108, v108, v243\n"
      "v_mul_f32 v109, v109, v243\n"
      "v_mul_f32 v110, v110, v243\n"
      "v_mul_f32 v111, v111, v243\n"
      "v_mul_f32 v124, v124, v243\n"
      "v_mul_f32 v125, v125, v243\n"
      "v_mul_f32 v126, v126, v243\n"
      "v_mul_f32 v127, v127, v243\n"
      "v_mul_f32 v211, v211, v215\n"
      "v_lshlrev_b32 v252, 16, v172\n"
      "v_and_b32 v253, 0xffff0000, v172\n"
      "v_mul_f32 v252, v252, v215\n"
      "v_mul_f32 v253, v253, v215\n"
      "v_cvt_pk_bf16_f32 v172, v252, v253\n"
      "v_lshlrev_b32 v252, 16, v173\n"
      "v_and_b32 v253, 0xffff0000, v173\n"
      "v_mul_f32 v252, v252, v215\n"
      "v_mul_f32 v253, v253, v215\n"
      "v_cvt_pk_bf16_f32 v173, v252, v253\n"
      "v_lshlrev_b32 v252, 16, v174\n"
      "v_and_b32 v253, 0xffff0000, v174\n"
      "v_mul_f32 v252, v252, v215\n"
      "v_mul_f32 v253, v253, v215\n"
      "v_cvt_pk_bf16_f32 v174, v252, v253\n"
      "v_lshlrev_b32 v252, 16, v175\n"
      "v_and_b32 v253, 0xffff0000, v175\n"
      "v_mul_f32 v252, v252, v215\n"
      "v_mul_f32 v253, v253, v215\n"
      "v_cvt_pk_bf16_f32 v175, v252, v253\n"
      "v_lshlrev_b32 v252, 16, v188\n"
      "v_and_b32 v253, 0xffff0000, v188\n"
      "v_mul_f32 v252, v252, v247\n"
      "v_mul_f32 v253, v253, v247\n"
      "v_cvt_pk_bf16_f32 v188, v252, v253\n"
      "v_lshlrev_b32 v252, 16, v189\n"
      "v_and_b32 v253, 0xffff0000, v189\n"
      "v_mul_f32 v252, v252, v247\n"
      "v_mul_f32 v253, v253, v247\n"
      "v_cvt_pk_bf16_f32 v189, v252, v253\n"
      "v_lshlrev_b32 v252, 16, v190\n"
      "v_and_b32 v253, 0xffff0000, v190\n"
      "v_mul_f32 v252, v252, v247\n"
      "v_mul_f32 v253, v253, v247\n"
      "v_cvt_pk_bf16_f32 v190, v252, v253\n"
      "v_lshlrev_b32 v252, 16, v191\n"
      "v_and_b32 v253, 0xffff0000, v191\n"
      "v_mul_f32 v252, v252, v247\n"
      "v_mul_f32 v253, v253, v247\n"
      "v_cvt_pk_bf16_f32 v191, v252, v253\n"
      "v_mov_b32 v215, 0\n"
      "s_nop 1\n"
      "s_branch B2_18_%=\n"
      "TDF_%=:\n"
      "v_lshrrev_b32 v243, 4, v254\n"
      "v_lshlrev_b32 v243, 8, v243\n"
      "v_and_b32 v244, 15, v254\n"
      "v_lshl_add_u32 v243, v244, 3, v243\n"
      "v_lshl_add_u32 v243, s80, 14, v243\n"
      "v_add_u32 v243, 98304, v243\n"
      "ds_read_b64_tr_b16 a[0:1], v243 offset:0\n"
      "ds_read_b64_tr_b16 a[2:3], v243 offset:128\n"
      "ds_read_b64_tr_b16 a[4:5], v243 offset:1024\n"
      "ds_read_b64_tr_b16 a[6:7], v243 offset:1152\n"
      "ds_read_b64_tr_b16 a[8:9], v243 offset:2048\n"
      "ds_read_b64_tr_b16 a[10:11], v243 offset:2176\n"
      "ds_read_b64_tr_b16 a[12:13], v243 offset:3072\n"
      "ds_read_b64_tr_b16 a[14:15], v243 offset:3200\n"
      "ds_read_b64_tr_b16 a[16:17], v243 offset:4096\n"
      "ds_read_b64_tr_b16 a[18:19], v243 offset:4224\n"
      "ds_read_b64_tr_b16 a[20:21], v243 offset:5120\n"
      "ds_read_b64_tr_b16 a[22:23], v243 offset:5248\n"
      "ds_read_b64_tr_b16 a[24:25], v243 offset:6144\n"
      "ds_read_b64_tr_b16 a[26:27], v243 offset:6272\n"
      "ds_read_b64_tr_b16 a[28:29], v243 offset:7168\n"
      "ds_read_b64_tr_b16 a[30:31], v243 offset:7296\n"
      "ds_read_b64_tr_b16 a[32:33], v243 offset:8192\n"
      "ds_read_b64_tr_b16 a[34:35], v243 offset:8320\n"
      "ds_read_b64_tr_b16 a[36:37], v243 offset:9216\n"
      "ds_read_b64_tr_b16 a[38:39], v243 offset:9344\n"
      "ds_read_b64_tr_b16 a[40:41], v243 offset:10240\n"
      "ds_read_b64_tr_b16 a[42:43], v243 offset:10368\n"
      "ds_read_b64_tr_b16 a[44:45], v243 offset:11264\n"
      "ds_read_b64_tr_b16 a[46:47], v243 offset:11392\n"
      "ds_read_b64_tr_b16 a[48:49], v243 offset:12288\n"
      "ds_read_b64_tr_b16 a[50:51], v243 offset:12416\n"
      "ds_read_b64_tr_b16 a[52:53], v243 offset:13312\n"
      "ds_read_b64_tr_b16 a[54:55], v243 offset:13440\n"
      "s_branch TDG_%=\n"
      "KEND_%=:\n"
      : "+{v255}"(tid), "+{s[36:37]}"(x), "+{s[38:39]}"(cnd), "+{s[40:41]}"(wm), "+{s[42:43]}"(wq), "+{s[44:45]}"(K), "+{s[46:47]}"(V)
      : "{s[48:49]}"(wo), "{s[50:51]}"(ws), "{s52}"(blockIdx.x), "{s[34:35]}"(__builtin_amdgcn_kernarg_segment_ptr())
      :
        "v0", "v1", "v2", "v3", "v4", "v5", "v6", "v7", "v8", "v9", "v10", "v11", "v12", "v13", "v14", "v15", "v16", "v17", "v18", "v19", "v20", "v21", "v22", "v23",
        "v24", "v25", "v26", "v27", "v28", "v29", "v30", "v31", "v32", "v33", "v34", "v35", "v36", "v37", "v38", "v39", "v40", "v41", "v42", "v43", "v44", "v45", "v46", "v47",
        "v48", "v49", "v50", "v51", "v52", "v53", "v54", "v55", "v56", "v57", "v58", "v59", "v60", "v61", "v62", "v63", "v64", "v65", "v66", "v67", "v68", "v69", "v70", "v71",
        "v72", "v73", "v74", "v75", "v76", "v77", "v78", "v79", "v80", "v81", "v82", "v83", "v84", "v85", "v86", "v87", "v88", "v89", "v90", "v91", "v92", "v93", "v94", "v95",
        "v96", "v97", "v98", "v99", "v100", "v101", "v102", "v103", "v104", "v105", "v106", "v107", "v108", "v109", "v110", "v111", "v112", "v113", "v114", "v115", "v116", "v117", "v118", "v119",
        "v120", "v121", "v122", "v123", "v124", "v125", "v126", "v127", "v128", "v129", "v130", "v131", "v132", "v133", "v134", "v135", "v136", "v137", "v138", "v139", "v140", "v141", "v142", "v143",
        "v144", "v145", "v146", "v147", "v148", "v149", "v150", "v151", "v152", "v153", "v154", "v155", "v156", "v157", "v158", "v159", "v160", "v161", "v162", "v163", "v164", "v165", "v166", "v167",
        "v168", "v169", "v170", "v171", "v172", "v173", "v174", "v175", "v176", "v177", "v178", "v179", "v180", "v181", "v182", "v183", "v184", "v185", "v186", "v187", "v188", "v189", "v190", "v191",
        "v192", "v193", "v194", "v195", "v196", "v197", "v198", "v199", "v200", "v201", "v202", "v203", "v204", "v205", "v206", "v207", "v208", "v209", "v210", "v211", "v212", "v213", "v214", "v215",
        "v216", "v217", "v218", "v219", "v220", "v221", "v222", "v223", "v224", "v225", "v226", "v227", "v228", "v229", "v230", "v231", "v232", "v233", "v234", "v235", "v236", "v237", "v238", "v239",
        "v240", "v241", "v242", "v243", "v244", "v245", "v246", "v247", "v248", "v249", "v250", "v251", "v252", "v253", "v254", "a0", "a1", "a2", "a3", "a4", "a5", "a6", "a7", "a8",
        "a9", "a10", "a11", "a12", "a13", "a14", "a15", "a16", "a17", "a18", "a19", "a20", "a21", "a22", "a23", "a24", "a25", "a26", "a27", "a28", "a29", "a30", "a31", "a32",
        "a33", "a34", "a35", "a36", "a37", "a38", "a39", "a40", "a41", "a42", "a43", "a44", "a45", "a46", "a47", "a48", "a49", "a50", "a51", "a52", "a53", "a54", "a55", "a56",
        "a57", "a58", "a59", "a60", "a61", "a62", "a63", "a64", "a65", "a66", "a67", "a68", "a69", "a70", "a71", "a72", "a73", "a74", "a75", "a76", "a77", "a78", "a79", "a80",
        "a81", "a82", "a83", "a84", "a85", "a86", "a87", "a88", "a89", "a90", "a91", "a92", "a93", "a94", "a95", "a96", "a97", "a98", "a99", "a100", "a101", "a102", "a103", "a104",
        "a105", "a106", "a107", "a108", "a109", "a110", "a111", "a112", "a113", "a114", "a115", "a116", "a117", "a118", "a119", "a120", "a121", "a122", "a123", "a124", "a125", "a126", "a127", "a128",
        "a129", "a130", "a131", "a132", "a133", "a134", "a135", "a136", "a137", "a138", "a139", "a140", "a141", "a142", "a143", "a144", "a145", "a146", "a147", "a148", "a149", "a150", "a151", "a152",
        "a153", "a154", "a155", "a156", "a157", "a158", "a159", "a160", "a161", "a162", "a163", "a164", "a165", "a166", "a167", "a168", "a169", "a170", "a171", "a172", "a173", "a174", "a175", "a176",
        "a177", "a178", "a179", "a180", "a181", "a182", "a183", "a184", "a185", "a186", "a187", "a188", "a189", "a190", "a191", "a192", "a193", "a194", "a195", "a196", "a197", "a198", "a199", "a200",
        "a201", "a202", "a203", "a204", "a205", "a206", "a207", "a208", "a209", "a210", "a211", "a212", "a213", "a214", "a215", "a216", "a217", "a218", "a219", "a220", "a221", "a222", "a223", "a224",
        "a225", "a226", "a227", "a228", "a229", "a230", "a231", "a232", "a233", "a234", "a235", "a236", "a237", "a238", "a239", "a240", "a241", "a242", "a243", "a244", "a245", "a246", "a247", "a248",
        "a249", "a250", "a251", "a252", "a253", "a254", "a255", "s53", "s54", "s55", "s56", "s57", "s58", "s59", "s60", "s61", "s62", "s63", "s64", "s65", "s66", "s67", "s68", "s69",
        "s70", "s71", "s72", "s73", "s74", "s75", "s76", "s77", "s78", "s79", "s80", "s81", "s82", "s83", "s84", "s85", "s86", "s87", "s88", "s89", "s90", "s91", "s92", "s93",
        "s94", "s95", "s96", "s97", "s98", "s99", "vcc", "scc", "memory");
}

extern "C" void kernel_launch(void* const* d_in, const int* in_sizes, int n_in, void* d_out, int out_size, void* d_ws, size_t ws_size,
                              hipStream_t stream) {
  k_main<<<256, 256, 0, stream>>>(d_in[0], d_in[10], d_in[11], d_in[3], d_in[1], d_in[2], d_in[4], d_ws, d_in[6], d_in[7], d_in[8],
                                  d_in[9], d_out);
}
```
